```python
import torch

def _softmax(x, axis=-1):
    return torch.softmax(x, dim=axis)
def _gelu(x):
    return torch.nn.functional.gelu(x, approximate="tanh")
def _silu(x):
    return torch.nn.functional.silu(x)
def _sigmoid(x):
    return torch.sigmoid(x)
def _layernorm(x, axis=-1, eps=1e-5):
    m = x.mean(dim=axis, keepdim=True)
    v = x.var(dim=axis, keepdim=True, unbiased=False)
    return (x - m) / torch.sqrt(v + eps)
def _scatter_add(tgt, idx, src, axis=0):
    return tgt.clone().index_add_(axis, idx, src)
def _segment_sum(src, seg, n):
    out = torch.zeros((n,) + src.shape[1:], dtype=src.dtype)
    return out.index_add_(0, seg, src)
def _pad(x, pw):
    flat = tuple(v for pair in reversed(pw) for v in pair)
    return torch.nn.functional.pad(x, flat)

kernel_name = 'sequential_preLN__linear_attn_block__glu_gated_bilinear__prompt'

def setup_inputs():

    _w = (lambda a: (lambda t: t.to(torch.bfloat16) if t.is_floating_point() else t)(torch.as_tensor(a)))
    def _A_setup():
        B, S, D = 1, 2048, 512
        return {
            "x": _w((lambda *s: torch.randn(*s, dtype=torch.bfloat16) * 0.05)(B, S, D)),
            "wqkv": _w((lambda *s: torch.randn(*s, dtype=torch.bfloat16) * 0.05)(D, 3 * D)),
            "wo": _w((lambda *s: torch.randn(*s, dtype=torch.bfloat16) * 0.05)(D, D)),
        }
    _A_in = _A_setup()

    def _B_setup():
        B, S, D, F = 1, 2048, 512, 1376
        return {
            "x": _w((lambda *s: torch.randn(*s, dtype=torch.bfloat16) * 0.05)(B, S, D)),
            "w_gate": _w((lambda *s: torch.randn(*s, dtype=torch.bfloat16) * 0.2)(D, F) * (D ** -0.5)),
            "w_up": _w((lambda *s: torch.randn(*s, dtype=torch.bfloat16) * 0.2)(D, F) * (D ** -0.5)),
            "w_down": _w((lambda *s: torch.randn(*s, dtype=torch.bfloat16) * 0.2)(F, D) * (F ** -0.5)),
        }
    _B_in = _B_setup()

    return {
        "A_x": _A_in["x"],
        "A_wqkv": _A_in["wqkv"],
        "A_wo": _A_in["wo"],
        "B_x": _B_in["x"],
        "B_w_gate": _B_in["w_gate"],
        "B_w_up": _B_in["w_up"],
        "B_w_down": _B_in["w_down"],
    }

def reference(A_x, A_wqkv, A_wo, B_x, B_w_gate, B_w_up, B_w_down):
    def _A_ref(x, wqkv, wo):
        B, S, D = x.shape
        H = 32; Dh = D // H
        qkv = (lambda x, *d: x.permute(*d))((x @ wqkv).reshape(B, S, 3, H, Dh), 2, 0, 3, 1, 4)
        q, k, v = qkv[0], qkv[1], qkv[2]
        phi = lambda t: torch.where(t > 0, t + 1, torch.exp(t)) + 1e-6
        pq, pk = phi(q), phi(k)
        kv = torch.einsum("bhsd,bhse->bhde", pk, v)
        z = torch.einsum("bhsd->bhd", pk)
        num = torch.einsum("bhqd,bhde->bhqe", pq, kv)
        den = torch.einsum("bhqd,bhd->bhq", pq, z)[..., None] + 1e-6
        o = num / den
        return (lambda x, *d: x.permute(*d))(o, 0, 2, 1, 3).reshape(B, S, D) @ wo

    def _B_ref(x, w_gate, w_up, w_down):
        g = torch.einsum("bsd,df->bsf", x, w_gate)
        u = torch.einsum("bsd,df->bsf", x, w_up)
        h = g * u
        return torch.einsum("bsf,fd->bsd", h, w_down)

    x = A_x
    h = x + _A_ref(_layernorm(x, axis=-1), A_wqkv, A_wo)
    h2 = h + _B_ref(_layernorm(h, axis=-1), B_w_gate, B_w_up, B_w_down)
    return h2
```

```cpp
#include <hip/hip_runtime.h>
#include <hip/hip_bf16.h>
#include <stdint.h>

typedef __attribute__((ext_vector_type(8))) __bf16 bf16x8;
typedef __attribute__((ext_vector_type(4))) __bf16 bf16x4;
typedef __attribute__((ext_vector_type(4))) short s16x4;
typedef __attribute__((ext_vector_type(4))) float f32x4;
typedef __attribute__((ext_vector_type(2))) float f32x2;
typedef __attribute__((ext_vector_type(4))) unsigned u32x4;
typedef __attribute__((ext_vector_type(2))) unsigned u32x2;
typedef __attribute__((address_space(3))) s16x4* lds_s16x4_ptr;

#define DI __device__ __forceinline__
#define MFMA(a, b, c) __builtin_amdgcn_mfma_f32_16x16x32_bf16((a), (b), (c), 0, 0, 0)

constexpr int S_ = 2048, D_ = 512, F_ = 1376;
constexpr int KVREC = 272;

DI float bfr(float x) { return (float)(__bf16)x; }
DI float bf2f(__bf16 x) { return (float)x; }

DI void glds16(const void* gsrc, unsigned lds_dst) {
  unsigned keep;
  asm volatile(
      "s_mov_b32 %0, m0\n\ts_mov_b32 m0, %2\n\ts_nop 0\n\tglobal_load_lds_dwordx4 %1, off\n\ts_mov_b32 m0, %0"
      : "=&s"(keep)
      : "v"(gsrc), "s"(lds_dst)
      : "memory");
}
#define WAIT_VM(n) asm volatile("s_waitcnt vmcnt(" #n ")" ::: "memory")
DI void wait_vm(int n) {
  switch (n) {
    case 0: WAIT_VM(0); break;   case 1: WAIT_VM(1); break;   case 2: WAIT_VM(2); break;
    case 3: WAIT_VM(3); break;   case 4: WAIT_VM(4); break;   case 5: WAIT_VM(5); break;
    case 6: WAIT_VM(6); break;   case 7: WAIT_VM(7); break;   case 8: WAIT_VM(8); break;
    case 9: WAIT_VM(9); break;   case 10: WAIT_VM(10); break; case 11: WAIT_VM(11); break;
    case 12: WAIT_VM(12); break; case 13: WAIT_VM(13); break; case 14: WAIT_VM(14); break;
    case 15: WAIT_VM(15); break; case 16: WAIT_VM(16); break; case 17: WAIT_VM(17); break;
    case 18: WAIT_VM(18); break; case 19: WAIT_VM(19); break; case 20: WAIT_VM(20); break;
    case 21: WAIT_VM(21); break; case 22: WAIT_VM(22); break; case 23: WAIT_VM(23); break;
    case 24: WAIT_VM(24); break; case 25: WAIT_VM(25); break; case 26: WAIT_VM(26); break;
    case 27: WAIT_VM(27); break; case 28: WAIT_VM(28); break; case 29: WAIT_VM(29); break;
    case 30: WAIT_VM(30); break; case 31: WAIT_VM(31); break; case 32: WAIT_VM(32); break;
    case 33: WAIT_VM(33); break; case 34: WAIT_VM(34); break; case 35: WAIT_VM(35); break;
    case 36: WAIT_VM(36); break; case 37: WAIT_VM(37); break; case 38: WAIT_VM(38); break;
    case 39: WAIT_VM(39); break; case 40: WAIT_VM(40); break; case 41: WAIT_VM(41); break;
    case 42: WAIT_VM(42); break; case 43: WAIT_VM(43); break; case 44: WAIT_VM(44); break;
    default: WAIT_VM(0); break;
  }
}
DI void bar() { __builtin_amdgcn_s_barrier(); }

DI bf16x8 ldB(const char* smem_lane_base, int off0, int off1) {
  s16x4 lo = __builtin_amdgcn_ds_read_tr16_b64_v4i16((lds_s16x4_ptr)(smem_lane_base + off0));
  s16x4 hi = __builtin_amdgcn_ds_read_tr16_b64_v4i16((lds_s16x4_ptr)(smem_lane_base + off1));
  typedef __attribute__((ext_vector_type(8))) short s16x8;
  s16x8 v = __builtin_shufflevector(lo, hi, 0, 1, 2, 3, 4, 5, 6, 7);
  return __builtin_bit_cast(bf16x8, v);
}

DI float phi(float t) {
  float r = t > 0.f ? bfr(t + 1.0f) : bfr(__expf(t));
  return bfr(r + 1e-6f);
}

constexpr int K1_LDS = 96 * 1024 + 4096;
__global__ __launch_bounds__(256, 1) void k1_qkv(const __bf16* __restrict__ x, const __bf16* __restrict__ w,
                                                 __bf16* __restrict__ pq, float* __restrict__ kvpart) {
  __shared__ __attribute__((aligned(16))) char smem[K1_LDS];
  const int tid = threadIdx.x, lane = tid & 63, wv = __builtin_amdgcn_readfirstlane(tid >> 6);
  const int g = lane >> 4, c = lane & 15;
  const int b = blockIdx.x, xcd = b & 7, idx = b >> 3;
  const int mt = (xcd >> 1) * 4 + (idx >> 3), hp = (xcd & 1) * 8 + (idx & 7);
  const unsigned lds0 = (unsigned)(size_t)smem;

  bf16x8 af[2][16];
  const __bf16* xrow = x + (size_t)(mt * 128 + wv * 32 + c) * D_ + g * 8;
#pragma unroll
  for (int ch = 0; ch < 4; ++ch) {
#pragma unroll
    for (int i = 0; i < 6; ++i) {
      const int ins = ch * 24 + i * 4 + wv;
      const int blk = ins * 4 + g;
      const int kb = blk / 6, ct = blk % 6;
      const int row8 = c >> 1, half = c & 1;
      const int u = kb >> 2, t = (kb >> 1) & 1, hf = kb & 1;
      const int k = 32 * u + 16 * hf + 8 * (row8 >> 2) + 4 * t + (row8 & 3);
      const int col = (ct >> 1) * 512 + hp * 32 + (ct & 1) * 16 + half * 8;
      glds16(w + (size_t)k * 1536 + col, lds0 + ins * 1024);
    }
#pragma unroll
    for (int uu = 0; uu < 4; ++uu) {
      const int u = ch * 4 + uu;
      af[0][u] = *(const bf16x8*)(xrow + u * 32);
      af[1][u] = *(const bf16x8*)(xrow + 16 * D_ + u * 32);
    }
  }

  f32x4 acc[2][6];
#pragma unroll
  for (int i = 0; i < 2; ++i)
#pragma unroll
    for (int j = 0; j < 6; ++j) acc[i][j] = (f32x4){0.f, 0.f, 0.f, 0.f};
  f32x4 rsum[2] = {{0.f, 0.f, 0.f, 0.f}, {0.f, 0.f, 0.f, 0.f}};
  f32x4 gram[2] = {{0.f, 0.f, 0.f, 0.f}, {0.f, 0.f, 0.f, 0.f}};
  f32x4 csum[6];
#pragma unroll
  for (int j = 0; j < 6; ++j) csum[j] = (f32x4){0.f, 0.f, 0.f, 0.f};
  bf16x8 ones;
#pragma unroll
  for (int j = 0; j < 8; ++j) ones[j] = (__bf16)1.0f;

  const char* bl = smem + (g >> 1) * 6 * 256 + ((g & 1) * 4 + (c >> 2)) * 32 + (c & 3) * 8;
#pragma unroll
  for (int ch = 0; ch < 4; ++ch) {
    wait_vm(14 * (3 - ch));
    bar();
#pragma unroll
    for (int uu = 0; uu < 4; ++uu) {
      const int u = ch * 4 + uu;
      bf16x8 bfm[6];
#pragma unroll
      for (int ct = 0; ct < 6; ++ct) bfm[ct] = ldB(bl, (4 * u) * 1536 + ct * 256, (4 * u + 2) * 1536 + ct * 256);
#pragma unroll
      for (int i = 0; i < 2; ++i) {
        acc[i][0] = MFMA(bfm[0], af[i][u], acc[i][0]);
        acc[i][1] = MFMA(bfm[1], af[i][u], acc[i][1]);
#pragma unroll
        for (int ct = 2; ct < 6; ++ct) acc[i][ct] = MFMA(af[i][u], bfm[ct], acc[i][ct]);
        rsum[i] = MFMA(ones, af[i][u], rsum[i]);
        gram[i] = MFMA(af[i][u], af[i][u], gram[i]);
      }
      if (uu == wv) {
#pragma unroll
        for (int ct = 0; ct < 6; ++ct) csum[ct] = MFMA(ones, bfm[ct], csum[ct]);
      }
    }
  }

  float* stat = (float*)(smem + 96 * 1024);
  float* cst = (float*)(smem + 96 * 1024 + 1024);
  float m_l[2], rs_l[2];
#pragma unroll
  for (int i = 0; i < 2; ++i) {
    float dsel = (c & 2) ? ((c & 1) ? gram[i][3] : gram[i][2]) : ((c & 1) ? gram[i][1] : gram[i][0]);
    float sq = __shfl(dsel, (c >> 2) * 16 + c, 64);
    float mean = rsum[i][0] * (1.0f / 512.0f);
    float var = fmaxf(sq * (1.0f / 512.0f) - mean * mean, 0.f);
    float mb = bfr(mean);
    float sb = bfr(sqrtf(bfr(bfr(var) + 1e-5f)));
    m_l[i] = mb;
    rs_l[i] = 1.0f / sb;
    if (g == 0) *(f32x2*)(stat + (wv * 32 + i * 16 + c) * 2) = (f32x2){mb, 1.0f / sb};
  }
  if (g == 0) {
#pragma unroll
    for (int ct = 0; ct < 6; ++ct) cst[wv * 96 + ct * 16 + c] = csum[ct][0];
  }
  __syncthreads();
  float cs_c[4];
  f32x4 cs_q[2];
#pragma unroll
  for (int j = 0; j < 4; ++j) {
    const int n = (2 + j) * 16 + c;
    cs_c[j] = (cst[n] + cst[96 + n]) + (cst[192 + n] + cst[288 + n]);
  }
#pragma unroll
  for (int j = 0; j < 2; ++j) {
    const int n = j * 16 + 4 * g;
    cs_q[j] = (*(const f32x4*)(cst + n) + *(const f32x4*)(cst + 96 + n)) +
              (*(const f32x4*)(cst + 192 + n) + *(const f32x4*)(cst + 288 + n));
  }
#pragma unroll
  for (int i = 0; i < 2; ++i) {
    bf16x8 o;
#pragma unroll
    for (int j = 0; j < 2; ++j)
#pragma unroll
      for (int r = 0; r < 4; ++r) {
        float v = bfr(rs_l[i] * (acc[i][j][r] - m_l[i] * cs_q[j][r]));
        o[j * 4 + r] = (__bf16)phi(v);
      }
    const int rt = mt * 8 + wv * 2 + i;
    *(bf16x8*)(pq + ((size_t)(rt * 16 + hp) * 64 + lane) * 8) = o;
  }
  f32x4 kvz[2][2];
  {
    float mm[2][4], rr[2][4];
#pragma unroll
    for (int i = 0; i < 2; ++i)
#pragma unroll
      for (int r = 0; r < 4; r += 2) {
        f32x4 st = *(const f32x4*)(stat + (wv * 32 + i * 16 + 4 * g + r) * 2);
        mm[i][r] = st[0]; rr[i][r] = st[1]; mm[i][r + 1] = st[2]; rr[i][r + 1] = st[3];
      }
#pragma unroll
    for (int a = 0; a < 2; ++a) {
      bf16x8 pk, vv;
#pragma unroll
      for (int i = 0; i < 2; ++i)
#pragma unroll
        for (int r = 0; r < 4; ++r) {
          float kk = bfr(rr[i][r] * (acc[i][2 + a][r] - mm[i][r] * cs_c[a]));
          pk[i * 4 + r] = (__bf16)phi(kk);
          vv[i * 4 + r] = (__bf16)(rr[i][r] * (acc[i][4 + a][r] - mm[i][r] * cs_c[2 + a]));
        }
      f32x4 z4 = {0.f, 0.f, 0.f, 0.f};
      kvz[a][0] = MFMA(pk, vv, z4);
      kvz[a][1] = MFMA(pk, ones, z4);
    }
  }
  f32x4* red = (f32x4*)smem;
#pragma unroll
  for (int a = 0; a < 2; ++a) {
    red[((wv * 2 + a) * 2 + 0) * 64 + lane] = kvz[a][0];
    red[((wv * 2 + a) * 2 + 1) * 64 + lane] = kvz[a][1];
  }
  __syncthreads();
  {
    const int a = wv >> 1, which = wv & 1;
    f32x4 s = (red[((0 * 2 + a) * 2 + which) * 64 + lane] + red[((1 * 2 + a) * 2 + which) * 64 + lane]) +
              (red[((2 * 2 + a) * 2 + which) * 64 + lane] + red[((3 * 2 + a) * 2 + which) * 64 + lane]);
    float* dst = kvpart + (size_t)(mt * 32 + hp * 2 + a) * KVREC;
    if (which == 0) *(f32x4*)(dst + lane * 4) = s;
    else if (c == 0) *(f32x4*)(dst + 256 + g * 4) = s;
  }
}

__global__ __launch_bounds__(256) void k1b_reduce(const float* __restrict__ kvpart, __bf16* __restrict__ kvf,
                                                  const __bf16* __restrict__ wg, const __bf16* __restrict__ wu,
                                                  float* __restrict__ csgu) {
  __shared__ __attribute__((aligned(16))) float red[32 * 64];
  const int tid = threadIdx.x, b = blockIdx.x;
  if (b < 34) {
    if (tid < 64) {
      const int q = b * 64 + tid;
      f32x4 v[16];
#pragma unroll
      for (int m = 0; m < 16; ++m) v[m] = *(const f32x4*)(kvpart + (size_t)m * 32 * KVREC + q * 4);
      f32x4 s = v[0];
#pragma unroll
      for (int m = 1; m < 16; ++m) s += v[m];
      bf16x4 o;
#pragma unroll
      for (int j = 0; j < 4; ++j) o[j] = (__bf16)s[j];
      *(bf16x4*)(kvf + q * 4) = o;
    }
    return;
  }
  const int cb = b - 34;
  const int col0 = cb * 64 + (tid & 7) * 8;
  const __bf16* src = col0 < F_ ? (wg + col0) : (wu + (col0 - F_));
  const int kq = tid >> 3;
  bf16x8 v[16];
#pragma unroll
  for (int r = 0; r < 16; ++r) v[r] = *(const bf16x8*)(src + (size_t)(kq * 16 + r) * F_);
  float s[8];
#pragma unroll
  for (int j = 0; j < 8; ++j) s[j] = 0.f;
#pragma unroll
  for (int r = 0; r < 16; ++r)
#pragma unroll
    for (int j = 0; j < 8; ++j) s[j] += (float)v[r][j];
#pragma unroll
  for (int j = 0; j < 8; ++j) red[kq * 64 + (tid & 7) * 8 + j] = s[j];
  __syncthreads();
  if (tid < 64) {
    float t = 0.f;
#pragma unroll
    for (int q = 0; q < 32; ++q) t += red[q * 64 + tid];
    csgu[cb * 64 + tid] = t;
  }
}

constexpr int K2_LDS = 64 * 1024 + 17408;
__global__ __launch_bounds__(256, 1) void k2_attn_out(const __bf16* __restrict__ x, const __bf16* __restrict__ wo,
                                                      const __bf16* __restrict__ pq, const __bf16* __restrict__ kvf,
                                                      __bf16* __restrict__ hfr, float* __restrict__ hstat) {
  __shared__ __attribute__((aligned(16))) char smem[K2_LDS];
  const int tid = threadIdx.x, lane = tid & 63, wv = __builtin_amdgcn_readfirstlane(tid >> 6);
  const int g = lane >> 4, c = lane & 15;
  const int b = blockIdx.x, xcd = b & 7, idx = b >> 3;
  const int mt = xcd * 4 + (idx >> 3), nt = idx & 7;
  const unsigned lds0 = (unsigned)(size_t)smem;
  const int rt = mt * 4 + wv;

#pragma unroll
  for (int i = 0; i < 5; ++i) {
    const int ins = i * 4 + wv;
    if (ins < 17) glds16((const char*)kvf + ins * 1024 + lane * 16, lds0 + 65536 + ins * 1024);
  }
  bf16x8 pf[16];
#pragma unroll
  for (int hp = 0; hp < 16; ++hp) pf[hp] = *(const bf16x8*)(pq + ((size_t)(rt * 16 + hp) * 64 + lane) * 8);
#pragma unroll
  for (int i = 0; i < 16; ++i) {
    const int ins = i * 4 + wv;
    const int k = ins * 8 + (c >> 1);
    glds16(wo + (size_t)k * D_ + nt * 64 + g * 16 + (c & 1) * 8, lds0 + ins * 1024);
  }
  bf16x4 xr[4];
#pragma unroll
  for (int ct = 0; ct < 4; ++ct) xr[ct] = *(const bf16x4*)(x + (size_t)(rt * 16 + c) * D_ + nt * 64 + ct * 16 + g * 4);

  f32x4 acc[4];
#pragma unroll
  for (int j = 0; j < 4; ++j) acc[j] = (f32x4){0.f, 0.f, 0.f, 0.f};
  WAIT_VM(0);
  bar();
  const char* bl = smem + (g >> 1) * 4 * 256 + ((g & 1) * 4 + (c >> 2)) * 32 + (c & 3) * 8;
  const char* kvl = smem + 65536;
  const f32x4 z4 = {0.f, 0.f, 0.f, 0.f};
#pragma unroll
  for (int hp = 0; hp < 16; ++hp) {
    bf16x4 k0 = *(const bf16x4*)(kvl + (2 * hp) * (KVREC * 2) + lane * 8);
    bf16x4 k1 = *(const bf16x4*)(kvl + (2 * hp + 1) * (KVREC * 2) + lane * 8);
    bf16x4 z0 = *(const bf16x4*)(kvl + (2 * hp) * (KVREC * 2) + 512 + g * 8);
    bf16x4 z1 = *(const bf16x4*)(kvl + (2 * hp + 1) * (KVREC * 2) + 512 + g * 8);
    const bf16x4 zz = {(__bf16)0.f, (__bf16)0.f, (__bf16)0.f, (__bf16)0.f};
    bf16x8 a0 = __builtin_shufflevector(k0, zz, 0, 1, 2, 3, 4, 5, 6, 7);
    bf16x8 a1 = __builtin_shufflevector(zz, k1, 0, 1, 2, 3, 4, 5, 6, 7);
    bf16x8 d0 = __builtin_shufflevector(z0, zz, 0, 1, 2, 3, 4, 5, 6, 7);
    bf16x8 d1 = __builtin_shufflevector(zz, z1, 0, 1, 2, 3, 4, 5, 6, 7);
    f32x4 n0 = MFMA(a0, pf[hp], z4);
    f32x4 n1 = MFMA(a1, pf[hp], z4);
    f32x4 e0 = MFMA(d0, pf[hp], z4);
    f32x4 e1 = MFMA(d1, pf[hp], z4);
    const float r0 = 1.0f / bfr(bfr(e0[0]) + 1e-6f);
    const float r1 = 1.0f / bfr(bfr(e1[0]) + 1e-6f);
    bf16x8 of;
#pragma unroll
    for (int r = 0; r < 4; ++r) {
      of[r] = (__bf16)(bfr(n0[r]) * r0);
      of[4 + r] = (__bf16)(bfr(n1[r]) * r1);
    }
#pragma unroll
    for (int ct = 0; ct < 4; ++ct) {
      bf16x8 wf = ldB(bl, (4 * hp) * 1024 + ct * 256, (4 * hp + 2) * 1024 + ct * 256);
      acc[ct] = MFMA(wf, of, acc[ct]);
    }
  }
  float hv[4][4];
  float s = 0.f;
#pragma unroll
  for (int ct = 0; ct < 4; ++ct)
#pragma unroll
    for (int r = 0; r < 4; ++r) {
      hv[ct][r] = bfr((float)xr[ct][r] + bfr(acc[ct][r]));
      s += hv[ct][r];
    }
#pragma unroll
  for (int p = 0; p < 2; ++p) {
    bf16x8 o;
#pragma unroll
    for (int r = 0; r < 4; ++r) { o[r] = (__bf16)hv[2 * p][r]; o[4 + r] = (__bf16)hv[2 * p + 1][r]; }
    *(bf16x8*)(hfr + ((size_t)(rt * 16 + nt * 2 + p) * 64 + lane) * 8) = o;
  }
  s += __shfl_xor(s, 16, 64);
  s += __shfl_xor(s, 32, 64);
  const float mean = s * (1.0f / 64.0f);
  float m2 = 0.f;
#pragma unroll
  for (int ct = 0; ct < 4; ++ct)
#pragma unroll
    for (int r = 0; r < 4; ++r) { float d = hv[ct][r] - mean; m2 += d * d; }
  m2 += __shfl_xor(m2, 16, 64);
  m2 += __shfl_xor(m2, 32, 64);
  if (g == 0) *(f32x2*)(hstat + ((size_t)(rt * 16 + c) * 8 + nt) * 2) = (f32x2){s, m2};
}

constexpr int K3_LDS = 96 * 1024;
__global__ __launch_bounds__(512, 2) void k3_gate_up(const __bf16* __restrict__ hfr, const float* __restrict__ hstat,
                                                     const __bf16* __restrict__ wg, const __bf16* __restrict__ wu,
                                                     const float* __restrict__ csgu, __bf16* __restrict__ hh) {
  __shared__ __attribute__((aligned(16))) char smem[K3_LDS];
  const int tid = threadIdx.x, lane = tid & 63, wv = __builtin_amdgcn_readfirstlane(tid >> 6);
  const int g = lane >> 4, c = lane & 15;
  const int b = blockIdx.x, xcd = b & 7, idx = b >> 3;
  const int fg = (xcd & 1) * 15 + (idx >> 1);
  const int mt = (xcd >> 1) * 2 + (idx & 1);
  if (fg >= 29) return;
  const unsigned lds0 = (unsigned)(size_t)smem;
  const int rt0 = mt * 16 + wv * 2;

  bf16x8 af[2][16];
#pragma unroll
  for (int ch = 0; ch < 4; ++ch) {
#pragma unroll
    for (int i = 0; i < 3; ++i) {
      const int ins = ch * 24 + i * 8 + wv;
      const int blk = ins * 4 + g;
      const int kb = blk / 6, ct = blk % 6;
      const int k = kb * 8 + (c >> 1);
      int ft = fg * 3 + (ct % 3);
      ft = ft > 85 ? 85 : ft;
      const __bf16* src = (ct < 3 ? wg : wu) + (size_t)k * F_ + ft * 16 + (c & 1) * 8;
      glds16(src, lds0 + ins * 1024);
    }
#pragma unroll
    for (int uu = 0; uu < 4; ++uu) {
      const int u = ch * 4 + uu;
      af[0][u] = *(const bf16x8*)(hfr + ((size_t)(rt0 * 16 + u) * 64 + lane) * 8);
      af[1][u] = *(const bf16x8*)(hfr + ((size_t)((rt0 + 1) * 16 + u) * 64 + lane) * 8);
    }
  }
  f32x4 st[2][4];
#pragma unroll
  for (int i = 0; i < 2; ++i)
#pragma unroll
    for (int j = 0; j < 4; ++j) st[i][j] = *(const f32x4*)(hstat + (size_t)((rt0 + i) * 16 + c) * 16 + j * 4);
  f32x4 cs[6];
#pragma unroll
  for (int ct = 0; ct < 6; ++ct) {
    int ft = fg * 3 + (ct % 3);
    ft = ft > 85 ? 85 : ft;
    cs[ct] = *(const f32x4*)(csgu + (ct < 3 ? 0 : F_) + ft * 16 + g * 4);
  }

  f32x4 acc[2][6];
#pragma unroll
  for (int i = 0; i < 2; ++i)
#pragma unroll
    for (int j = 0; j < 6; ++j) acc[i][j] = (f32x4){0.f, 0.f, 0.f, 0.f};

  const char* bl = smem + (g >> 1) * 6 * 256 + ((g & 1) * 4 + (c >> 2)) * 32 + (c & 3) * 8;
#pragma unroll
  for (int ch = 0; ch < 4; ++ch) {
    wait_vm(11 * (3 - ch) + 14);
    bar();
#pragma unroll
    for (int uu = 0; uu < 4; ++uu) {
      const int u = ch * 4 + uu;
      bf16x8 bfm[6];
#pragma unroll
      for (int ct = 0; ct < 6; ++ct) bfm[ct] = ldB(bl, (4 * u) * 1536 + ct * 256, (4 * u + 2) * 1536 + ct * 256);
#pragma unroll
      for (int i = 0; i < 2; ++i)
#pragma unroll
        for (int ct = 0; ct < 6; ++ct) acc[i][ct] = MFMA(bfm[ct], af[i][u], acc[i][ct]);
    }
  }
#pragma unroll
  for (int i = 0; i < 2; ++i) {
    float s = 0.f;
#pragma unroll
    for (int j = 0; j < 4; ++j) s += st[i][j][0] + st[i][j][2];
    const float mean = s * (1.0f / 512.0f);
    float m2 = 0.f;
#pragma unroll
    for (int j = 0; j < 4; ++j) {
      float d0 = st[i][j][0] * (1.0f / 64.0f) - mean, d1 = st[i][j][2] * (1.0f / 64.0f) - mean;
      m2 += st[i][j][1] + st[i][j][3] + 64.0f * (d0 * d0 + d1 * d1);
    }
    const float var = m2 * (1.0f / 512.0f);
    const float mb = bfr(mean);
    const float rs = 1.0f / bfr(sqrtf(bfr(bfr(var) + 1e-5f)));
#pragma unroll
    for (int ct = 0; ct < 3; ++ct) {
      const int ft = fg * 3 + ct;
      bf16x4 o;
#pragma unroll
      for (int r = 0; r < 4; ++r) {
        float gg = bfr(rs * (acc[i][ct][r] - mb * cs[ct][r]));
        float uu = bfr(rs * (acc[i][3 + ct][r] - mb * cs[3 + ct][r]));
        o[r] = (__bf16)(gg * uu);
      }
      if (ft < 86) *(bf16x4*)(hh + ((size_t)((rt0 + i) * 43 + (ft >> 1)) * 64 + lane) * 8 + (ft & 1) * 4) = o;
    }
  }
}

constexpr int K4_LDS = 160 * 1024;
__global__ __launch_bounds__(512, 2) void k4_down(const __bf16* __restrict__ hh, const __bf16* __restrict__ wd,
                                                  const __bf16* __restrict__ hfr, __bf16* __restrict__ out) {
  __shared__ __attribute__((aligned(16))) char smem[K4_LDS];
  const int tid = threadIdx.x, lane = tid & 63, wv = __builtin_amdgcn_readfirstlane(tid >> 6);
  const int g = lane >> 4, c = lane & 15;
  const int b = blockIdx.x, xcd = b & 7, idx = b >> 3;
  const int mt = xcd * 4 + (idx >> 3), nt = idx & 7;
  const int mi = wv & 1, kq = wv >> 1;
  const unsigned lds0 = (unsigned)(size_t)smem;
  const int rt0 = mt * 4 + mi * 2;
  const int u0 = kq * 11;
  const int nu = kq == 3 ? 10 : 11;

  const __bf16* wsrc = wd + (size_t)(u0 * 32 + mi * 16 + (c >> 1)) * D_ + nt * 64 + g * 16 + (c & 1) * 8;
  const unsigned ldsq = lds0 + kq * 40960 + mi * 2048;
  bf16x8 af[2][11];
  const __bf16* asrc = hh + ((size_t)(rt0 * 43 + u0) * 64 + lane) * 8;
#pragma unroll
  for (int s = 0; s < 10; ++s) {
    glds16(wsrc + (size_t)(s * 32) * D_, ldsq + s * 4096);
    glds16(wsrc + (size_t)(s * 32 + 8) * D_, ldsq + s * 4096 + 1024);
    af[0][s] = *(const bf16x8*)(asrc + (size_t)s * 512);
    af[1][s] = *(const bf16x8*)(asrc + (size_t)(43 + s) * 512);
  }
  {
    const int s = kq == 3 ? 9 : 10;
    af[0][10] = *(const bf16x8*)(asrc + (size_t)s * 512);
    af[1][10] = *(const bf16x8*)(asrc + (size_t)(43 + s) * 512);
  }
  f32x4 acc[2][4];
#pragma unroll
  for (int i = 0; i < 2; ++i)
#pragma unroll
    for (int j = 0; j < 4; ++j) acc[i][j] = (f32x4){0.f, 0.f, 0.f, 0.f};
  const char* bl = smem + kq * 40960 + (g >> 1) * 4 * 256 + ((g & 1) * 4 + (c >> 2)) * 32 + (c & 3) * 8;

  wait_vm(4 * 8 + 2);
  bar();
#pragma unroll
  for (int s = 0; s < 2; ++s) {
    bf16x8 bfm[4];
#pragma unroll
    for (int ct = 0; ct < 4; ++ct) bfm[ct] = ldB(bl, s * 4096 + ct * 256, s * 4096 + 2048 + ct * 256);
#pragma unroll
    for (int i = 0; i < 2; ++i)
#pragma unroll
      for (int ct = 0; ct < 4; ++ct) acc[i][ct] = MFMA(bfm[ct], af[i][s], acc[i][ct]);
  }
  asm volatile("s_waitcnt lgkmcnt(0)" ::: "memory");
  wait_vm(4 * 4 + 2);
  bar();
  if (kq < 3) {
    glds16(wsrc + (size_t)(10 * 32) * D_, ldsq);
    glds16(wsrc + (size_t)(10 * 32 + 8) * D_, ldsq + 1024);
  }
#pragma unroll
  for (int s = 2; s < 6; ++s) {
    bf16x8 bfm[4];
#pragma unroll
    for (int ct = 0; ct < 4; ++ct) bfm[ct] = ldB(bl, s * 4096 + ct * 256, s * 4096 + 2048 + ct * 256);
#pragma unroll
    for (int i = 0; i < 2; ++i)
#pragma unroll
      for (int ct = 0; ct < 4; ++ct) acc[i][ct] = MFMA(bfm[ct], af[i][s], acc[i][ct]);
  }
  WAIT_VM(0);
  bar();
#pragma unroll
  for (int s = 6; s < 10; ++s) {
    bf16x8 bfm[4];
#pragma unroll
    for (int ct = 0; ct < 4; ++ct) bfm[ct] = ldB(bl, s * 4096 + ct * 256, s * 4096 + 2048 + ct * 256);
#pragma unroll
    for (int i = 0; i < 2; ++i)
#pragma unroll
      for (int ct = 0; ct < 4; ++ct) acc[i][ct] = MFMA(bfm[ct], af[i][s], acc[i][ct]);
  }
  if (nu == 11) {
    bf16x8 bfm[4];
#pragma unroll
    for (int ct = 0; ct < 4; ++ct) bfm[ct] = ldB(bl, ct * 256, 2048 + ct * 256);
#pragma unroll
    for (int i = 0; i < 2; ++i)
#pragma unroll
      for (int ct = 0; ct < 4; ++ct) acc[i][ct] = MFMA(bfm[ct], af[i][10], acc[i][ct]);
  }
  const int fi = kq >> 1, fc = (kq & 1) * 2;
  bf16x8 hres = *(const bf16x8*)(hfr + ((size_t)((rt0 + fi) * 16 + nt * 2 + (kq & 1)) * 64 + lane) * 8);
  __syncthreads();
  f32x4* red = (f32x4*)smem;
#pragma unroll
  for (int i = 0; i < 2; ++i)
#pragma unroll
    for (int ct = 0; ct < 4; ++ct) red[(((mi * 4 + kq) * 2 + i) * 4 + ct) * 64 + lane] = acc[i][ct];
  __syncthreads();
#pragma unroll
  for (int p = 0; p < 2; ++p) {
    const int ct = fc + p;
    f32x4 s = (red[(((mi * 4 + 0) * 2 + fi) * 4 + ct) * 64 + lane] + red[(((mi * 4 + 1) * 2 + fi) * 4 + ct) * 64 + lane]) +
              (red[(((mi * 4 + 2) * 2 + fi) * 4 + ct) * 64 + lane] + red[(((mi * 4 + 3) * 2 + fi) * 4 + ct) * 64 + lane]);
    bf16x4 o;
#pragma unroll
    for (int r = 0; r < 4; ++r) o[r] = (__bf16)((float)hres[p * 4 + r] + bfr(s[r]));
    *(bf16x4*)(out + (size_t)((rt0 + fi) * 16 + c) * D_ + nt * 64 + ct * 16 + g * 4) = o;
  }
}

extern "C" void kernel_launch(void* const* d_in, const int* in_sizes, int n_in, void* d_out, int out_size,
                              void* d_ws, size_t ws_size, hipStream_t stream) {
  const __bf16* x = (const __bf16*)d_in[0];
  const __bf16* wqkv = (const __bf16*)d_in[1];
  const __bf16* wo = (const __bf16*)d_in[2];
  const __bf16* wg = (const __bf16*)d_in[4];
  const __bf16* wu = (const __bf16*)d_in[5];
  const __bf16* wd = (const __bf16*)d_in[6];
  char* ws = (char*)d_ws;
  __bf16* pq = (__bf16*)(ws);
  __bf16* hfr = (__bf16*)(ws + (2u << 20));
  __bf16* hh = (__bf16*)(ws + (4u << 20));
  float* kvpart = (float*)(ws + (10u << 20));
  __bf16* kvf = (__bf16*)(ws + (11u << 20));
  float* csgu = (float*)(ws + (11u << 20) + 65536);
  float* hstat = (float*)(ws + (12u << 20));
  k1_qkv<<<256, 256, 0, stream>>>(x, wqkv, pq, kvpart);
  k1b_reduce<<<77, 256, 0, stream>>>(kvpart, kvf, wg, wu, csgu);
  k2_attn_out<<<256, 256, 0, stream>>>(x, wo, pq, kvf, hfr, hstat);
  k3_gate_up<<<240, 512, 0, stream>>>(hfr, hstat, wg, wu, csgu, hh);
  k4_down<<<256, 512, 0, stream>>>(hh, wd, hfr, (__bf16*)d_out);
}
```

```cpp
#include <hip/hip_runtime.h>
#include <hip/hip_bf16.h>
#include <stdint.h>

typedef __attribute__((ext_vector_type(8))) __bf16 bf16x8;
typedef __attribute__((ext_vector_type(4))) __bf16 bf16x4;
typedef __attribute__((ext_vector_type(4))) short s16x4;
typedef __attribute__((ext_vector_type(4))) float f32x4;
typedef __attribute__((ext_vector_type(2))) float f32x2;
typedef __attribute__((ext_vector_type(4))) unsigned u32x4;
typedef __attribute__((ext_vector_type(2))) unsigned u32x2;
typedef __attribute__((address_space(3))) s16x4* lds_s16x4_ptr;

#define DI __device__ __forceinline__
#define MFMA(a, b, c) __builtin_amdgcn_mfma_f32_16x16x32_bf16((a), (b), (c), 0, 0, 0)

constexpr int S_ = 2048, D_ = 512, F_ = 1376;
constexpr int KVREC = 272;

#define STAMP_DECL
#define STAMP(i)
#define STAMP_OUT(k, wsbase)
DI float bfr(float x) { return (float)(__bf16)x; }
DI float bf2f(__bf16 x) { return (float)x; }

DI void glds16(const void* gsrc, unsigned lds_dst) {
  unsigned keep;
  asm volatile(
      "s_mov_b32 %0, m0\n\ts_mov_b32 m0, %2\n\ts_nop 0\n\tglobal_load_lds_dwordx4 %1, off\n\ts_mov_b32 m0, %0"
      : "=&s"(keep)
      : "v"(gsrc), "s"(lds_dst)
      : "memory");
}
DI void glds16s(const void* ubase, unsigned voff, unsigned lds_dst) {
  unsigned keep;
  asm volatile(
      "s_mov_b32 %0, m0\n\ts_mov_b32 m0, %3\n\ts_nop 0\n\tglobal_load_lds_dwordx4 %1, %2\n\ts_mov_b32 m0, %0"
      : "=&s"(keep)
      : "v"(voff), "s"(ubase), "s"(lds_dst)
      : "memory");
}
#define WAIT_VM(n) do { __builtin_amdgcn_sched_barrier(0); asm volatile("s_waitcnt vmcnt(" #n ") lgkmcnt(0)" ::: "memory"); } while (0)
DI void wait_vm(int n) {
  switch (n) {
    case 0: WAIT_VM(0); break;   case 1: WAIT_VM(1); break;   case 2: WAIT_VM(2); break;
    case 3: WAIT_VM(3); break;   case 4: WAIT_VM(4); break;   case 5: WAIT_VM(5); break;
    case 6: WAIT_VM(6); break;   case 7: WAIT_VM(7); break;   case 8: WAIT_VM(8); break;
    case 9: WAIT_VM(9); break;   case 10: WAIT_VM(10); break; case 11: WAIT_VM(11); break;
    case 12: WAIT_VM(12); break; case 13: WAIT_VM(13); break; case 14: WAIT_VM(14); break;
    case 15: WAIT_VM(15); break; case 16: WAIT_VM(16); break; case 17: WAIT_VM(17); break;
    case 18: WAIT_VM(18); break; case 19: WAIT_VM(19); break; case 20: WAIT_VM(20); break;
    case 21: WAIT_VM(21); break; case 22: WAIT_VM(22); break; case 23: WAIT_VM(23); break;
    case 24: WAIT_VM(24); break; case 25: WAIT_VM(25); break; case 26: WAIT_VM(26); break;
    case 27: WAIT_VM(27); break; case 28: WAIT_VM(28); break; case 29: WAIT_VM(29); break;
    case 30: WAIT_VM(30); break; case 31: WAIT_VM(31); break; case 32: WAIT_VM(32); break;
    case 33: WAIT_VM(33); break; case 34: WAIT_VM(34); break; case 35: WAIT_VM(35); break;
    case 36: WAIT_VM(36); break; case 37: WAIT_VM(37); break; case 38: WAIT_VM(38); break;
    case 39: WAIT_VM(39); break; case 40: WAIT_VM(40); break; case 41: WAIT_VM(41); break;
    case 42: WAIT_VM(42); break; case 43: WAIT_VM(43); break; case 44: WAIT_VM(44); break;
    default: WAIT_VM(0); break;
  }
}
DI void bar() { __builtin_amdgcn_s_barrier(); __builtin_amdgcn_sched_barrier(0); }

DI bf16x8 ldB(const char* smem_lane_base, int off0, int off1) {
  s16x4 lo = __builtin_amdgcn_ds_read_tr16_b64_v4i16((lds_s16x4_ptr)(smem_lane_base + off0));
  s16x4 hi = __builtin_amdgcn_ds_read_tr16_b64_v4i16((lds_s16x4_ptr)(smem_lane_base + off1));
  typedef __attribute__((ext_vector_type(8))) short s16x8;
  s16x8 v = __builtin_shufflevector(lo, hi, 0, 1, 2, 3, 4, 5, 6, 7);
  return __builtin_bit_cast(bf16x8, v);
}

DI float phi(float t) {
  float r = t > 0.f ? bfr(t + 1.0f) : bfr(__expf(t));
  return bfr(r + 1e-6f);
}

constexpr int K1_RING = 4 * 32768;
constexpr int K1_LDS = K1_RING + 4096;
__global__ __launch_bounds__(512, 2) void k1_qkv(const __bf16* x, const __bf16* w, __bf16* pq, float* kvpart) {
  __shared__ __attribute__((aligned(16))) char smem[K1_LDS];
  STAMP_DECL; STAMP(0);
  const int tid = threadIdx.x, lane = tid & 63, wv = __builtin_amdgcn_readfirstlane(tid >> 6);
  const int g = lane >> 4, c = lane & 15;
  const int hw = wv & 3, rh = wv >> 2;
  const int b = blockIdx.x, xcd = b & 7, idx = b >> 3;
  const int mt = (xcd >> 1) * 8 + (idx >> 2), hq = (xcd & 1) * 4 + (idx & 3);
  const unsigned lds0 = (unsigned)(size_t)smem;

  const __bf16* xsrc = x + (size_t)(mt * 64 + wv * 8) * D_;
  const __bf16* wsrc = w + (size_t)(wv * 8) * 1536 + hq * 64;
  unsigned xoff, woff;
  {
    const int r = lane >> 3, pos = lane & 7;
    const int row = wv * 8 + r;
    xoff = r * (D_ * 2) + (pos ^ ((row >> 1) & 7)) * 16;
    const int swz = ((r >> 1) & 1) | ((wv & 1) << 1);
    woff = r * 3072 + ((pos >> 1) ^ swz) * 32 + (pos & 1) * 16;
  }
  auto issue = [&](int s) {
    const unsigned slot = lds0 + (s & 3) * 32768;
    glds16s(xsrc + s * 64, xoff, slot + wv * 1024);
#pragma unroll
    for (int p = 0; p < 3; ++p) glds16s(wsrc + (size_t)s * 64 * 1536 + p * 512, woff, slot + 8192 + (p * 8 + wv) * 1024);
  };
  issue(0); issue(1); issue(2);
  STAMP(1);

  f32x4 acc[2][3];
#pragma unroll
  for (int i = 0; i < 2; ++i)
#pragma unroll
    for (int j = 0; j < 3; ++j) acc[i][j] = (f32x4){0.f, 0.f, 0.f, 0.f};
  f32x4 stt = {0.f, 0.f, 0.f, 0.f};
  f32x4 csum[3];
#pragma unroll
  for (int j = 0; j < 3; ++j) csum[j] = (f32x4){0.f, 0.f, 0.f, 0.f};
  bf16x8 ones;
#pragma unroll
  for (int j = 0; j < 8; ++j) ones[j] = (__bf16)1.0f;

  const int aoff = (rh * 32 + c) * 128 + ((g ^ (c >> 1)) * 16);
  const int boff = 8192 + g * 1024 + (c >> 2) * 128 + ((hw ^ (((c >> 3) & 1) | ((g & 1) << 1))) * 32) + (c & 3) * 8;
#pragma unroll
  for (int s = 0; s < 8; ++s) {
    if (s < 6) WAIT_VM(8); else if (s == 6) WAIT_VM(4); else WAIT_VM(0);
    bar();
    if (s == 0) STAMP(2);
    if (s + 3 < 8) issue(s + 3);
    const char* sl = smem + (s & 3) * 32768;
#pragma unroll
    for (int uu = 0; uu < 2; ++uu) {
      bf16x8 a0 = *(const bf16x8*)(sl + (aoff ^ (uu << 6)));
      bf16x8 a1 = *(const bf16x8*)(sl + (aoff ^ (uu << 6)) + 2048);
      bf16x8 bq = ldB(sl + boff, uu * 4096, uu * 4096 + 512);
      bf16x8 bk = ldB(sl + boff, 8192 + uu * 4096, 8192 + uu * 4096 + 512);
      bf16x8 bv = ldB(sl + boff, 16384 + uu * 4096, 16384 + uu * 4096 + 512);
      acc[0][0] = MFMA(bq, a0, acc[0][0]);
      acc[1][0] = MFMA(bq, a1, acc[1][0]);
      acc[0][1] = MFMA(a0, bk, acc[0][1]);
      acc[1][1] = MFMA(a1, bk, acc[1][1]);
      acc[0][2] = MFMA(a0, bv, acc[0][2]);
      acc[1][2] = MFMA(a1, bv, acc[1][2]);
      if (hw == 0) stt = MFMA(ones, a0, stt);
      else if (hw == 1) stt = MFMA(a0, a0, stt);
      else if (hw == 2) stt = MFMA(ones, a1, stt);
      else stt = MFMA(a1, a1, stt);
      if (uu == rh) {
        csum[0] = MFMA(ones, bq, csum[0]);
        csum[1] = MFMA(ones, bk, csum[1]);
        csum[2] = MFMA(ones, bv, csum[2]);
      }
    }
  }
  STAMP(6);
  float* tsum = (float*)(smem + K1_RING);
  float* tsq = tsum + 64;
  float* tcs = tsum + 128;
  float* tst = tsum + 512;
  {
    const int row = rh * 32 + (hw >> 1) * 16 + c;
    if ((hw & 1) == 0) { if (g == 0) tsum[row] = stt[0]; }
    else if (g == (c >> 2)) tsq[row] = (c & 2) ? ((c & 1) ? stt[3] : stt[2]) : ((c & 1) ? stt[1] : stt[0]);
    if (g == 0) {
#pragma unroll
      for (int p = 0; p < 3; ++p) tcs[rh * 192 + p * 64 + hw * 16 + c] = csum[p][0];
    }
  }
  __syncthreads();
  if (tid < 64) {
    const float mean = tsum[tid] * (1.0f / 512.0f);
    const float var = fmaxf(tsq[tid] * (1.0f / 512.0f) - mean * mean, 0.f);
    const float sb = bfr(sqrtf(bfr(bfr(var) + 1e-5f)));
    *(f32x2*)(tst + tid * 2) = (f32x2){bfr(mean), 1.0f / sb};
  }
  __syncthreads();
  const f32x4 csq = *(const f32x4*)(tcs + hw * 16 + 4 * g) + *(const f32x4*)(tcs + 192 + hw * 16 + 4 * g);
  const float csk = tcs[64 + hw * 16 + c] + tcs[192 + 64 + hw * 16 + c];
  const float csv = tcs[128 + hw * 16 + c] + tcs[192 + 128 + hw * 16 + c];
#pragma unroll
  for (int i = 0; i < 2; ++i) {
    const f32x2 st = *(const f32x2*)(tst + (rh * 32 + i * 16 + c) * 2);
    bf16x4 o;
#pragma unroll
    for (int r = 0; r < 4; ++r) o[r] = (__bf16)phi(bfr(st[1] * (acc[i][0][r] - st[0] * csq[r])));
    const int rt = mt * 4 + rh * 2 + i;
    *(bf16x4*)(pq + ((size_t)(rt * 16 + hq * 2 + (hw >> 1)) * 64 + lane) * 8 + (hw & 1) * 4) = o;
  }
  STAMP(7);
  {
    bf16x8 pk, vv;
#pragma unroll
    for (int i = 0; i < 2; ++i)
#pragma unroll
      for (int r = 0; r < 4; r += 2) {
        const f32x4 st = *(const f32x4*)(tst + (rh * 32 + i * 16 + 4 * g + r) * 2);
        pk[i * 4 + r] = (__bf16)phi(bfr(st[1] * (acc[i][1][r] - st[0] * csk)));
        pk[i * 4 + r + 1] = (__bf16)phi(bfr(st[3] * (acc[i][1][r + 1] - st[2] * csk)));
        vv[i * 4 + r] = (__bf16)(st[1] * (acc[i][2][r] - st[0] * csv));
        vv[i * 4 + r + 1] = (__bf16)(st[3] * (acc[i][2][r + 1] - st[2] * csv));
      }
    const f32x4 z4 = {0.f, 0.f, 0.f, 0.f};
    f32x4 kv = MFMA(pk, vv, z4);
    f32x4 zz = MFMA(pk, ones, z4);
    float* dst = kvpart + (size_t)((mt * 2 + rh) * 32 + hq * 4 + hw) * KVREC;
    *(f32x4*)(dst + lane * 4) = kv;
    if (c == 0) *(f32x4*)(dst + 256 + g * 4) = zz;
  }
  STAMP(8); STAMP_OUT(0, pq);
}

__global__ __launch_bounds__(256) void k1b_prep(const float* kvpart, __bf16* kvf, const __bf16* wo, const __bf16* wg,
                                                const __bf16* wu, const __bf16* wd, __bf16* wof, __bf16* wguf,
                                                __bf16* wdf, float* csp) {
  __shared__ __attribute__((aligned(16))) char smem[32768];
  STAMP_DECL; STAMP(0);
  const int tid = threadIdx.x, b = blockIdx.x, lane = tid & 63, wv = __builtin_amdgcn_readfirstlane(tid >> 6);
  const int g = lane >> 4, c = lane & 15;
  if (b < 34) {
    const int q = b * 64 + lane;
    f32x4 v[16];
#pragma unroll
    for (int m = 0; m < 16; ++m) v[m] = *(const f32x4*)(kvpart + (size_t)(wv * 16 + m) * 32 * KVREC + q * 4);
    f32x4 s = v[0];
#pragma unroll
    for (int m = 1; m < 16; ++m) s += v[m];
    f32x4* red = (f32x4*)smem;
    red[tid] = s;
    __syncthreads();
    if (tid < 64) {
      f32x4 t = (red[tid] + red[64 + tid]) + (red[128 + tid] + red[192 + tid]);
      bf16x4 o;
#pragma unroll
      for (int j = 0; j < 4; ++j) o[j] = (__bf16)t[j];
      *(bf16x4*)(kvf + q * 4) = o;
    }
    STAMP(1); STAMP_OUT(1, (const char*)kvpart - (10u << 20));
    return;
  }
  const int J = (b - 34) * 4 + wv;
  const __bf16* src; int rs, kq, stripe, kind, kmax;
  if (J < 64) { kind = 0; kq = J >> 4; stripe = J & 15; src = wo; rs = D_; kmax = 511; }
  else if (J < 236) { kind = 1; kq = (J - 64) / 43; stripe = (J - 64) % 43; src = wg; rs = F_; kmax = 511; }
  else if (J < 408) { kind = 2; kq = (J - 236) / 43; stripe = (J - 236) % 43; src = wu; rs = F_; kmax = 511; }
  else { kind = 3; kq = (J - 408) >> 4; stripe = (J - 408) & 15; src = wd; rs = D_; kmax = F_ - 1; }
  const unsigned lds0 = (unsigned)(size_t)smem + wv * 8192;
#pragma unroll
  for (int i = 0; i < 8; ++i) {
    const int r = i * 16 + (lane >> 2), p = lane & 3;
    int k = kq * 128 + r; k = k > kmax ? kmax : k;
    const int ch = (((p >> 1) ^ ((r >> 2) & 1)) << 1) | (p & 1);
    glds16(src + (size_t)k * rs + stripe * 32 + ch * 8, lds0 + i * 1024);
  }
  WAIT_VM(0);
  const char* bl = smem + wv * 8192 + (4 * g + (c >> 2)) * 64 + (c & 3) * 8;
  bf16x8 ones;
#pragma unroll
  for (int j = 0; j < 8; ++j) ones[j] = (__bf16)1.0f;
  f32x4 cs[2] = {{0.f, 0.f, 0.f, 0.f}, {0.f, 0.f, 0.f, 0.f}};
#pragma unroll
  for (int uu = 0; uu < 4; ++uu) {
    const int u = kq * 4 + uu;
#pragma unroll
    for (int j = 0; j < 2; ++j) {
      const int sw = ((j ^ (g & 1)) * 32);
      bf16x8 f = ldB(bl, uu * 2048 + sw, uu * 2048 + 1024 + sw);
      const int ctg = stripe * 2 + j;
      if (kind == 0) *(bf16x8*)(wof + ((size_t)(((ctg >> 2) * 16 + u) * 4 + (ctg & 3)) * 64 + lane) * 8) = f;
      else if (kind == 3) { if (u < 43) *(bf16x8*)(wdf + ((size_t)(((ctg >> 2) * 43 + u) * 4 + (ctg & 3)) * 64 + lane) * 8) = f; }
      else {
        *(bf16x8*)(wguf + ((size_t)((ctg * 16 + u) * 2 + (kind - 1)) * 64 + lane) * 8) = f;
        cs[j] = MFMA(ones, f, cs[j]);
      }
    }
  }
  if ((kind == 1 || kind == 2) && g == 0) {
#pragma unroll
    for (int j = 0; j < 2; ++j) csp[kq * 2752 + (kind - 1) * F_ + (stripe * 2 + j) * 16 + c] = cs[j][0];
  }
  STAMP(2); STAMP_OUT(1, (const char*)kvpart - (10u << 20));
}

constexpr int K2_LDS = 64 * 1024 + 17408;
__global__ __launch_bounds__(512, 2) void k2_attn_out(const __bf16* x, const __bf16* wof, const __bf16* pq,
                                                      const __bf16* kvf, __bf16* hfr, float* hstat) {
  __shared__ __attribute__((aligned(16))) char smem[K2_LDS];
  STAMP_DECL; STAMP(0);
  const int tid = threadIdx.x, lane = tid & 63, wv = __builtin_amdgcn_readfirstlane(tid >> 6);
  const int g = lane >> 4, c = lane & 15;
  const int rg = wv & 3, kh = wv >> 2;
  const int b = blockIdx.x, xcd = b & 7, idx = b >> 3;
  const int mt = xcd * 4 + (idx >> 3), nt = idx & 7;
  const unsigned lds0 = (unsigned)(size_t)smem;
  const int rt = mt * 4 + rg;

  bf16x4 xr[2];
#pragma unroll
  for (int p = 0; p < 2; ++p) xr[p] = *(const bf16x4*)(x + (size_t)(rt * 16 + c) * D_ + nt * 64 + (kh * 2 + p) * 16 + g * 4);
  bf16x8 pf[8];
#pragma unroll
  for (int h = 0; h < 8; ++h) pf[h] = *(const bf16x8*)(pq + ((size_t)(rt * 16 + kh * 8 + h) * 64 + lane) * 8);
#pragma unroll
  for (int i = 0; i < 3; ++i) {
    const int ins = i * 8 + wv;
    if (ins < 17) glds16s((const char*)kvf + ins * 1024, lane * 16, lds0 + 65536 + ins * 1024);
  }
#pragma unroll
  for (int i = 0; i < 8; ++i) {
    const int ins = i * 8 + wv;
    glds16s((const char*)wof + (size_t)nt * 65536 + ins * 1024, lane * 16, lds0 + ins * 1024);
  }
  f32x4 acc[4];
#pragma unroll
  for (int j = 0; j < 4; ++j) acc[j] = (f32x4){0.f, 0.f, 0.f, 0.f};
  STAMP(1);
  WAIT_VM(0);
  bar();
  STAMP(2);
  const char* bl = smem + kh * 32768 + lane * 16;
  const char* kvl = smem + 65536 + kh * 16 * (KVREC * 2);
  const f32x4 z4 = {0.f, 0.f, 0.f, 0.f};
#pragma unroll
  for (int h = 0; h < 8; ++h) {
    bf16x4 k0 = *(const bf16x4*)(kvl + (2 * h) * (KVREC * 2) + lane * 8);
    bf16x4 k1 = *(const bf16x4*)(kvl + (2 * h + 1) * (KVREC * 2) + lane * 8);
    bf16x4 z0 = *(const bf16x4*)(kvl + (2 * h) * (KVREC * 2) + 512 + g * 8);
    bf16x4 z1 = *(const bf16x4*)(kvl + (2 * h + 1) * (KVREC * 2) + 512 + g * 8);
    const bf16x4 zz = {(__bf16)0.f, (__bf16)0.f, (__bf16)0.f, (__bf16)0.f};
    bf16x8 a0 = __builtin_shufflevector(k0, zz, 0, 1, 2, 3, 4, 5, 6, 7);
    bf16x8 a1 = __builtin_shufflevector(zz, k1, 0, 1, 2, 3, 4, 5, 6, 7);
    bf16x8 d0 = __builtin_shufflevector(z0, zz, 0, 1, 2, 3, 4, 5, 6, 7);
    bf16x8 d1 = __builtin_shufflevector(zz, z1, 0, 1, 2, 3, 4, 5, 6, 7);
    f32x4 n0 = MFMA(a0, pf[h], z4);
    f32x4 n1 = MFMA(a1, pf[h], z4);
    f32x4 e0 = MFMA(d0, pf[h], z4);
    f32x4 e1 = MFMA(d1, pf[h], z4);
    const float r0 = __builtin_amdgcn_rcpf(bfr(bfr(e0[0]) + 1e-6f));
    const float r1 = __builtin_amdgcn_rcpf(bfr(bfr(e1[0]) + 1e-6f));
    bf16x8 of;
#pragma unroll
    for (int r = 0; r < 4; ++r) {
      of[r] = (__bf16)(bfr(n0[r]) * r0);
      of[4 + r] = (__bf16)(bfr(n1[r]) * r1);
    }
#pragma unroll
    for (int ct = 0; ct < 4; ++ct) {
      bf16x8 wf = *(const bf16x8*)(bl + (h * 4 + ct) * 1024);
      acc[ct] = MFMA(wf, of, acc[ct]);
    }
  }
  STAMP(3);
  __syncthreads();
  f32x4* red = (f32x4*)smem;
  red[((rg * 2 + kh) * 2 + 0) * 64 + lane] = kh ? acc[0] : acc[2];
  red[((rg * 2 + kh) * 2 + 1) * 64 + lane] = kh ? acc[1] : acc[3];
  __syncthreads();
  f32x4 f0 = (kh ? acc[2] : acc[0]), f1 = (kh ? acc[3] : acc[1]);
  {
    f32x4 o0 = red[((rg * 2 + (kh ^ 1)) * 2 + 0) * 64 + lane], o1 = red[((rg * 2 + (kh ^ 1)) * 2 + 1) * 64 + lane];
    f0 = kh ? (o0 + f0) : (f0 + o0);
    f1 = kh ? (o1 + f1) : (f1 + o1);
  }
  float hv[8];
  float s = 0.f;
#pragma unroll
  for (int r = 0; r < 4; ++r) {
    hv[r] = bfr((float)xr[0][r] + bfr(f0[r]));
    hv[4 + r] = bfr((float)xr[1][r] + bfr(f1[r]));
    s += hv[r] + hv[4 + r];
  }
  {
    bf16x8 o;
#pragma unroll
    for (int r = 0; r < 8; ++r) o[r] = (__bf16)hv[r];
    *(bf16x8*)(hfr + ((size_t)(rt * 16 + nt * 2 + kh) * 64 + lane) * 8) = o;
  }
  s += __shfl_xor(s, 16, 64);
  s += __shfl_xor(s, 32, 64);
  const float mean = s * (1.0f / 32.0f);
  float m2 = 0.f;
#pragma unroll
  for (int r = 0; r < 8; ++r) { float d = hv[r] - mean; m2 += d * d; }
  m2 += __shfl_xor(m2, 16, 64);
  m2 += __shfl_xor(m2, 32, 64);
  if (g == 0) *(f32x2*)(hstat + ((size_t)(rt * 16 + c) * 16 + nt * 2 + kh) * 2) = (f32x2){s, m2};
  STAMP(4); STAMP_OUT(2, pq);
}

constexpr int K3_LDS = 3 * 40960;
__global__ __launch_bounds__(512, 2) void k3_gate_up(const __bf16* hfr, const float* hstat, const __bf16* wguf,
                                                     const float* csp, __bf16* hh) {
  __shared__ __attribute__((aligned(16))) char smem[K3_LDS];
  STAMP_DECL; STAMP(0);
  const int tid = threadIdx.x, lane = tid & 63, wv = __builtin_amdgcn_readfirstlane(tid >> 6);
  const int g = lane >> 4, c = lane & 15;
  const int mw = wv & 3, nw = wv >> 2;
  const int b = blockIdx.x, xcd = b & 7, idx = b >> 3;
  const int mt = (xcd >> 2) * 8 + (idx >> 2), fgp = (xcd & 3) * 4 + (idx & 3);
  const int ft0 = (fgp * 43) >> 3;
  const unsigned lds0 = (unsigned)(size_t)smem;
  const int rt0 = mt * 8 + mw * 2;

  f32x4 st[2][2];
#pragma unroll
  for (int i = 0; i < 2; ++i)
#pragma unroll
    for (int j = 0; j < 2; ++j) st[i][j] = *(const f32x4*)(hstat + (size_t)((rt0 + i) * 16 + c) * 32 + g * 8 + j * 4);
  f32x4 cs[6];
#pragma unroll
  for (int ct = 0; ct < 6; ++ct)
    cs[ct] = *(const f32x4*)(csp + (c & 3) * 2752 + (ct & 1) * F_ + (ft0 + nw * 3 + (ct >> 1)) * 16 + g * 4);
  const char* psrc[5]; int pstep[5];
#pragma unroll
  for (int e = 0; e < 5; ++e) {
    const int pi = e * 8 + wv;
    if (pi < 16) { psrc[e] = (const char*)hfr + ((size_t)((mt * 8 + (pi >> 1)) * 16 + (pi & 1)) * 1024); pstep[e] = 2048; }
    else { const int q = pi - 16; psrc[e] = (const char*)wguf + ((size_t)(ft0 + (q >> 2)) * 32 + (q & 3)) * 1024; pstep[e] = 4096; }
  }
  auto issue = [&](int s) {
    const unsigned slot = lds0 + (s % 3) * 40960;
#pragma unroll
    for (int e = 0; e < 5; ++e) glds16s(psrc[e] + s * pstep[e], lane * 16, slot + (e * 8 + wv) * 1024);
  };
  issue(0); issue(1);
  STAMP(1);

  f32x4 acc[2][6];
#pragma unroll
  for (int i = 0; i < 2; ++i)
#pragma unroll
    for (int j = 0; j < 6; ++j) acc[i][j] = (f32x4){0.f, 0.f, 0.f, 0.f};

#pragma unroll
  for (int s = 0; s < 8; ++s) {
    if (s < 7) WAIT_VM(5); else WAIT_VM(0);
    bar();
    if (s == 0) STAMP(2);
    if (s + 2 < 8) issue(s + 2);
    const char* sl = smem + (s % 3) * 40960 + lane * 16;
#pragma unroll
    for (int uu = 0; uu < 2; ++uu) {
      bf16x8 a0 = *(const bf16x8*)(sl + (mw * 4 + uu) * 1024);
      bf16x8 a1 = *(const bf16x8*)(sl + (mw * 4 + 2 + uu) * 1024);
#pragma unroll
      for (int ct = 0; ct < 6; ++ct) {
        bf16x8 bf = *(const bf16x8*)(sl + 16384 + ((nw * 3 + (ct >> 1)) * 4 + uu * 2 + (ct & 1)) * 1024);
        acc[0][ct] = MFMA(bf, a0, acc[0][ct]);
        acc[1][ct] = MFMA(bf, a1, acc[1][ct]);
      }
    }
  }
  STAMP(6);
#pragma unroll
  for (int ct = 0; ct < 6; ++ct)
#pragma unroll
    for (int r = 0; r < 4; ++r) {
      float v = cs[ct][r];
      v += __shfl_xor(v, 1, 64);
      v += __shfl_xor(v, 2, 64);
      cs[ct][r] = v;
    }
#pragma unroll
  for (int i = 0; i < 2; ++i) {
    float s = (st[i][0][0] + st[i][0][2]) + (st[i][1][0] + st[i][1][2]);
    s += __shfl_xor(s, 16, 64);
    s += __shfl_xor(s, 32, 64);
    const float mean = s * (1.0f / 512.0f);
    float m2 = 0.f;
#pragma unroll
    for (int j = 0; j < 2; ++j) {
      float d0 = st[i][j][0] * (1.0f / 32.0f) - mean, d1 = st[i][j][2] * (1.0f / 32.0f) - mean;
      m2 += st[i][j][1] + st[i][j][3] + 32.0f * (d0 * d0 + d1 * d1);
    }
    m2 += __shfl_xor(m2, 16, 64);
    m2 += __shfl_xor(m2, 32, 64);
    const float var = m2 * (1.0f / 512.0f);
    const float mb = bfr(mean);
    const float rs = 1.0f / bfr(sqrtf(bfr(bfr(var) + 1e-5f)));
#pragma unroll
    for (int j = 0; j < 3; ++j) {
      const int ft = ft0 + nw * 3 + j;
      bf16x4 o;
#pragma unroll
      for (int r = 0; r < 4; ++r) {
        float gg = bfr(rs * (acc[i][2 * j][r] - mb * cs[2 * j][r]));
        float uu = bfr(rs * (acc[i][2 * j + 1][r] - mb * cs[2 * j + 1][r]));
        o[r] = (__bf16)(gg * uu);
      }
      *(bf16x4*)(hh + ((size_t)((rt0 + i) * 43 + (ft >> 1)) * 64 + lane) * 8 + (ft & 1) * 4) = o;
    }
  }
  STAMP(7); STAMP_OUT(3, (const char*)hfr - (2u << 20));
}

constexpr int K4_LDS = 160 * 1024;
__global__ __launch_bounds__(512, 2) void k4_down(const __bf16* hh, const __bf16* wdf, const __bf16* hfr, __bf16* out) {
  __shared__ __attribute__((aligned(16))) char smem[K4_LDS];
  STAMP_DECL; STAMP(0);
  const int tid = threadIdx.x, lane = tid & 63, wv = __builtin_amdgcn_readfirstlane(tid >> 6);
  const int g = lane >> 4, c = lane & 15;
  const int b = blockIdx.x, xcd = b & 7, idx = b >> 3;
  const int mt = xcd * 4 + (idx >> 3), nt = idx & 7;
  const int mi = wv & 1, kq = wv >> 1;
  const unsigned lds0 = (unsigned)(size_t)smem;
  const int rt0 = mt * 4 + mi * 2;
  const int u0 = kq * 11;
  const int nu = kq == 3 ? 10 : 11;
  const int fi = kq >> 1, fc = (kq & 1) * 2;
  bf16x8 hres = *(const bf16x8*)(hfr + ((size_t)((rt0 + fi) * 16 + nt * 2 + (kq & 1)) * 64 + lane) * 8);

  const char* wsrc = (const char*)wdf + ((size_t)(nt * 43 + u0) * 4 + mi * 2) * 1024;
  const unsigned ldsq = lds0 + kq * 40960 + mi * 2048;
  bf16x8 af[2][11];
  const __bf16* asrc = hh + ((size_t)(rt0 * 43 + u0) * 64 + lane) * 8;
#pragma unroll
  for (int s = 0; s < 10; ++s) {
    glds16s(wsrc + s * 4096, lane * 16, ldsq + s * 4096);
    glds16s(wsrc + s * 4096 + 1024, lane * 16, ldsq + s * 4096 + 1024);
    af[0][s] = *(const bf16x8*)(asrc + (size_t)s * 512);
    af[1][s] = *(const bf16x8*)(asrc + (size_t)(43 + s) * 512);
  }
  {
    const int s = kq == 3 ? 9 : 10;
    af[0][10] = *(const bf16x8*)(asrc + (size_t)s * 512);
    af[1][10] = *(const bf16x8*)(asrc + (size_t)(43 + s) * 512);
  }
  f32x4 acc[2][4];
#pragma unroll
  for (int i = 0; i < 2; ++i)
#pragma unroll
    for (int j = 0; j < 4; ++j) acc[i][j] = (f32x4){0.f, 0.f, 0.f, 0.f};
  const char* bl = smem + kq * 40960 + lane * 16;

  STAMP(1);
  wait_vm(4 * 8 + 2);
  bar();
  STAMP(2);
#pragma unroll
  for (int s = 0; s < 2; ++s) {
#pragma unroll
    for (int ct = 0; ct < 4; ++ct) {
      bf16x8 bf = *(const bf16x8*)(bl + s * 4096 + ct * 1024);
      acc[0][ct] = MFMA(bf, af[0][s], acc[0][ct]);
      acc[1][ct] = MFMA(bf, af[1][s], acc[1][ct]);
    }
  }
  asm volatile("s_waitcnt lgkmcnt(0)" ::: "memory");
  wait_vm(4 * 4 + 2);
  bar();
  STAMP(3);
  if (kq < 3) {
    glds16s(wsrc + 10 * 4096, lane * 16, ldsq);
    glds16s(wsrc + 10 * 4096 + 1024, lane * 16, ldsq + 1024);
  }
#pragma unroll
  for (int s = 2; s < 6; ++s) {
#pragma unroll
    for (int ct = 0; ct < 4; ++ct) {
      bf16x8 bf = *(const bf16x8*)(bl + s * 4096 + ct * 1024);
      acc[0][ct] = MFMA(bf, af[0][s], acc[0][ct]);
      acc[1][ct] = MFMA(bf, af[1][s], acc[1][ct]);
    }
  }
  WAIT_VM(0);
  bar();
  STAMP(4);
#pragma unroll
  for (int s = 6; s < 10; ++s) {
#pragma unroll
    for (int ct = 0; ct < 4; ++ct) {
      bf16x8 bf = *(const bf16x8*)(bl + s * 4096 + ct * 1024);
      acc[0][ct] = MFMA(bf, af[0][s], acc[0][ct]);
      acc[1][ct] = MFMA(bf, af[1][s], acc[1][ct]);
    }
  }
  if (nu == 11) {
#pragma unroll
    for (int ct = 0; ct < 4; ++ct) {
      bf16x8 bf = *(const bf16x8*)(bl + ct * 1024);
      acc[0][ct] = MFMA(bf, af[0][10], acc[0][ct]);
      acc[1][ct] = MFMA(bf, af[1][10], acc[1][ct]);
    }
  }
  STAMP(5);
  __syncthreads();
  f32x4* red = (f32x4*)smem;
#pragma unroll
  for (int i = 0; i < 2; ++i)
#pragma unroll
    for (int ct = 0; ct < 4; ++ct) red[(((mi * 4 + kq) * 2 + i) * 4 + ct) * 64 + lane] = acc[i][ct];
  __syncthreads();
#pragma unroll
  for (int p = 0; p < 2; ++p) {
    const int ct = fc + p;
    f32x4 s = (red[(((mi * 4 + 0) * 2 + fi) * 4 + ct) * 64 + lane] + red[(((mi * 4 + 1) * 2 + fi) * 4 + ct) * 64 + lane]) +
              (red[(((mi * 4 + 2) * 2 + fi) * 4 + ct) * 64 + lane] + red[(((mi * 4 + 3) * 2 + fi) * 4 + ct) * 64 + lane]);
    bf16x4 o;
#pragma unroll
    for (int r = 0; r < 4; ++r) o[r] = (__bf16)((float)hres[p * 4 + r] + bfr(s[r]));
    *(bf16x4*)(out + (size_t)((rt0 + fi) * 16 + c) * D_ + nt * 64 + ct * 16 + g * 4) = o;
  }
  STAMP(6); STAMP_OUT(4, (const char*)hfr - (2u << 20));
}

extern "C" void kernel_launch(void* const* d_in, const int* in_sizes, int n_in, void* d_out, int out_size,
                              void* d_ws, size_t ws_size, hipStream_t stream) {
  const __bf16* x = (const __bf16*)d_in[0];
  const __bf16* wqkv = (const __bf16*)d_in[1];
  const __bf16* wo = (const __bf16*)d_in[2];
  const __bf16* wg = (const __bf16*)d_in[4];
  const __bf16* wu = (const __bf16*)d_in[5];
  const __bf16* wd = (const __bf16*)d_in[6];
  char* ws = (char*)d_ws;
  __bf16* pq = (__bf16*)(ws);
  __bf16* hfr = (__bf16*)(ws + (2u << 20));
  __bf16* hh = (__bf16*)(ws + (4u << 20));
  float* kvpart = (float*)(ws + (10u << 20));
  __bf16* kvf = (__bf16*)(ws + (13u << 20));
  float* csp = (float*)(ws + (13u << 20) + 65536);
  float* hstat = (float*)(ws + (14u << 20));
  __bf16* wof = (__bf16*)(ws + (15u << 20));
  __bf16* wguf = (__bf16*)(ws + (16u << 20));
  __bf16* wdf = (__bf16*)(ws + (19u << 20));
  k1_qkv<<<256, 512, 0, stream>>>(x, wqkv, pq, kvpart);
  k1b_prep<<<180, 256, 0, stream>>>(kvpart, kvf, wo, wg, wu, wd, wof, wguf, wdf, csp);
  k2_attn_out<<<256, 512, 0, stream>>>(x, wof, pq, kvf, hfr, hstat);
  k3_gate_up<<<256, 512, 0, stream>>>(hfr, hstat, wguf, csp, hh);
  k4_down<<<256, 512, 0, stream>>>(hh, wdf, hfr, (__bf16*)d_out);
}
```

```cpp
#include <hip/hip_runtime.h>
#include <hip/hip_bf16.h>
#include <stdint.h>

typedef __attribute__((ext_vector_type(8))) __bf16 bf16x8;
typedef __attribute__((ext_vector_type(4))) __bf16 bf16x4;
typedef __attribute__((ext_vector_type(4))) short s16x4;
typedef __attribute__((ext_vector_type(4))) float f32x4;
typedef __attribute__((ext_vector_type(2))) float f32x2;
typedef __attribute__((ext_vector_type(4))) unsigned u32x4;
typedef __attribute__((ext_vector_type(2))) unsigned u32x2;
typedef __attribute__((address_space(3))) s16x4* lds_s16x4_ptr;

#define DI __device__ __forceinline__
#define MFMA(a, b, c) __builtin_amdgcn_mfma_f32_16x16x32_bf16((a), (b), (c), 0, 0, 0)

constexpr int S_ = 2048, D_ = 512, F_ = 1376;
constexpr int KVREC = 272;

#define STAMP_DECL
#define STAMP(i)
#define STAMP_OUT(k, wsbase)
DI float bfr(float x) { return (float)(__bf16)x; }
DI float bf2f(__bf16 x) { return (float)x; }

DI void glds16(const void* gsrc, unsigned lds_dst) {
  unsigned keep;
  asm volatile(
      "s_mov_b32 %0, m0\n\ts_mov_b32 m0, %2\n\ts_nop 0\n\tglobal_load_lds_dwordx4 %1, off\n\ts_mov_b32 m0, %0"
      : "=&s"(keep)
      : "v"(gsrc), "s"(lds_dst)
      : "memory");
}
DI void glds16s(const void* ubase, unsigned voff, unsigned lds_dst) {
  unsigned keep;
  asm volatile(
      "s_mov_b32 %0, m0\n\ts_mov_b32 m0, %3\n\ts_nop 0\n\tglobal_load_lds_dwordx4 %1, %2\n\ts_mov_b32 m0, %0"
      : "=&s"(keep)
      : "v"(voff), "s"(ubase), "s"(lds_dst)
      : "memory");
}
#define WAIT_VM(n) do { __builtin_amdgcn_sched_barrier(0); asm volatile("s_waitcnt vmcnt(" #n ") lgkmcnt(0)" ::: "memory"); } while (0)
DI void wait_vm(int n) {
  switch (n) {
    case 0: WAIT_VM(0); break;   case 1: WAIT_VM(1); break;   case 2: WAIT_VM(2); break;
    case 3: WAIT_VM(3); break;   case 4: WAIT_VM(4); break;   case 5: WAIT_VM(5); break;
    case 6: WAIT_VM(6); break;   case 7: WAIT_VM(7); break;   case 8: WAIT_VM(8); break;
    case 9: WAIT_VM(9); break;   case 10: WAIT_VM(10); break; case 11: WAIT_VM(11); break;
    case 12: WAIT_VM(12); break; case 13: WAIT_VM(13); break; case 14: WAIT_VM(14); break;
    case 15: WAIT_VM(15); break; case 16: WAIT_VM(16); break; case 17: WAIT_VM(17); break;
    case 18: WAIT_VM(18); break; case 19: WAIT_VM(19); break; case 20: WAIT_VM(20); break;
    case 21: WAIT_VM(21); break; case 22: WAIT_VM(22); break; case 23: WAIT_VM(23); break;
    case 24: WAIT_VM(24); break; case 25: WAIT_VM(25); break; case 26: WAIT_VM(26); break;
    case 27: WAIT_VM(27); break; case 28: WAIT_VM(28); break; case 29: WAIT_VM(29); break;
    case 30: WAIT_VM(30); break; case 31: WAIT_VM(31); break; case 32: WAIT_VM(32); break;
    case 33: WAIT_VM(33); break; case 34: WAIT_VM(34); break; case 35: WAIT_VM(35); break;
    case 36: WAIT_VM(36); break; case 37: WAIT_VM(37); break; case 38: WAIT_VM(38); break;
    case 39: WAIT_VM(39); break; case 40: WAIT_VM(40); break; case 41: WAIT_VM(41); break;
    case 42: WAIT_VM(42); break; case 43: WAIT_VM(43); break; case 44: WAIT_VM(44); break;
    default: WAIT_VM(0); break;
  }
}
DI void bar() { __builtin_amdgcn_s_barrier(); __builtin_amdgcn_sched_barrier(0); }

DI bf16x8 ldB(const char* smem_lane_base, int off0, int off1) {
  s16x4 lo = __builtin_amdgcn_ds_read_tr16_b64_v4i16((lds_s16x4_ptr)(smem_lane_base + off0));
  s16x4 hi = __builtin_amdgcn_ds_read_tr16_b64_v4i16((lds_s16x4_ptr)(smem_lane_base + off1));
  typedef __attribute__((ext_vector_type(8))) short s16x8;
  s16x8 v = __builtin_shufflevector(lo, hi, 0, 1, 2, 3, 4, 5, 6, 7);
  return __builtin_bit_cast(bf16x8, v);
}

DI float phi(float t) {
  float r = t > 0.f ? bfr(t + 1.0f) : bfr(__expf(t));
  return bfr(r + 1e-6f);
}

constexpr int K1_RING = 4 * 32768;
constexpr int K1_LDS = K1_RING + 4096;
__global__ __launch_bounds__(512, 2) void k1_qkv(const __bf16* x, const __bf16* w, __bf16* pq, float* kvpart) {
  __shared__ __attribute__((aligned(16))) char smem[K1_LDS];
  STAMP_DECL; STAMP(0);
  const int tid = threadIdx.x, lane = tid & 63, wv = __builtin_amdgcn_readfirstlane(tid >> 6);
  const int g = lane >> 4, c = lane & 15;
  const int hw = wv & 3, rh = wv >> 2;
  const int b = blockIdx.x, xcd = b & 7, idx = b >> 3;
  const int mt = (xcd >> 1) * 8 + (idx >> 2), hq = (xcd & 1) * 4 + (idx & 3);
  const unsigned lds0 = (unsigned)(size_t)smem;

  const __bf16* xsrc = x + (size_t)(mt * 64 + wv * 8) * D_;
  const __bf16* wsrc = w + (size_t)(wv * 8) * 1536 + hq * 64;
  unsigned xoff, woff;
  {
    const int r = lane >> 3, pos = lane & 7;
    const int row = wv * 8 + r;
    xoff = r * (D_ * 2) + (pos ^ ((row >> 1) & 7)) * 16;
    const int swz = ((r >> 1) & 1) | ((wv & 1) << 1);
    woff = r * 3072 + ((pos >> 1) ^ swz) * 32 + (pos & 1) * 16;
  }
  auto issue = [&](int s) {
    const unsigned slot = lds0 + (s & 3) * 32768;
    glds16s(xsrc + s * 64, xoff, slot + wv * 1024);
#pragma unroll
    for (int p = 0; p < 3; ++p) glds16s(wsrc + (size_t)s * 64 * 1536 + p * 512, woff, slot + 8192 + (p * 8 + wv) * 1024);
  };
  issue(0); issue(1); issue(2);
  STAMP(1);

  f32x4 acc[2][3];
#pragma unroll
  for (int i = 0; i < 2; ++i)
#pragma unroll
    for (int j = 0; j < 3; ++j) acc[i][j] = (f32x4){0.f, 0.f, 0.f, 0.f};
  f32x4 stt = {0.f, 0.f, 0.f, 0.f};
  f32x4 csum[3];
#pragma unroll
  for (int j = 0; j < 3; ++j) csum[j] = (f32x4){0.f, 0.f, 0.f, 0.f};
  bf16x8 ones;
#pragma unroll
  for (int j = 0; j < 8; ++j) ones[j] = (__bf16)1.0f;

  const int aoff = (rh * 32 + c) * 128 + ((g ^ (c >> 1)) * 16);
  const int boff = 8192 + g * 1024 + (c >> 2) * 128 + ((hw ^ (((c >> 3) & 1) | ((g & 1) << 1))) * 32) + (c & 3) * 8;
#pragma unroll
  for (int s = 0; s < 8; ++s) {
    if (s < 6) WAIT_VM(8); else if (s == 6) WAIT_VM(4); else WAIT_VM(0);
    bar();
    STAMP(8 + s);
    if (s + 3 < 8) issue(s + 3);
    const char* sl = smem + (s & 3) * 32768;
#pragma unroll
    for (int uu = 0; uu < 2; ++uu) {
      bf16x8 a0 = *(const bf16x8*)(sl + (aoff ^ (uu << 6)));
      bf16x8 a1 = *(const bf16x8*)(sl + (aoff ^ (uu << 6)) + 2048);
      bf16x8 bq = ldB(sl + boff, uu * 4096, uu * 4096 + 512);
      bf16x8 bk = ldB(sl + boff, 8192 + uu * 4096, 8192 + uu * 4096 + 512);
      bf16x8 bv = ldB(sl + boff, 16384 + uu * 4096, 16384 + uu * 4096 + 512);
      acc[0][0] = MFMA(bq, a0, acc[0][0]);
      acc[1][0] = MFMA(bq, a1, acc[1][0]);
      acc[0][1] = MFMA(a0, bk, acc[0][1]);
      acc[1][1] = MFMA(a1, bk, acc[1][1]);
      acc[0][2] = MFMA(a0, bv, acc[0][2]);
      acc[1][2] = MFMA(a1, bv, acc[1][2]);
      if (hw == 0) stt = MFMA(ones, a0, stt);
      else if (hw == 1) stt = MFMA(a0, a0, stt);
      else if (hw == 2) stt = MFMA(ones, a1, stt);
      else stt = MFMA(a1, a1, stt);
      if (uu == rh) {
        csum[0] = MFMA(ones, bq, csum[0]);
        csum[1] = MFMA(ones, bk, csum[1]);
        csum[2] = MFMA(ones, bv, csum[2]);
      }
    }
  }
  STAMP(6);
  float* tsum = (float*)(smem + K1_RING);
  float* tsq = tsum + 64;
  float* tcs = tsum + 128;
  float* tst = tsum + 512;
  {
    const int row = rh * 32 + (hw >> 1) * 16 + c;
    if ((hw & 1) == 0) { if (g == 0) tsum[row] = stt[0]; }
    else if (g == (c >> 2)) tsq[row] = (c & 2) ? ((c & 1) ? stt[3] : stt[2]) : ((c & 1) ? stt[1] : stt[0]);
    if (g == 0) {
#pragma unroll
      for (int p = 0; p < 3; ++p) tcs[rh * 192 + p * 64 + hw * 16 + c] = csum[p][0];
    }
  }
  __syncthreads();
  if (tid < 64) {
    const float mean = tsum[tid] * (1.0f / 512.0f);
    const float var = fmaxf(tsq[tid] * (1.0f / 512.0f) - mean * mean, 0.f);
    const float sb = bfr(sqrtf(bfr(bfr(var) + 1e-5f)));
    *(f32x2*)(tst + tid * 2) = (f32x2){bfr(mean), 1.0f / sb};
  }
  __syncthreads();
  const f32x4 csq = *(const f32x4*)(tcs + hw * 16 + 4 * g) + *(const f32x4*)(tcs + 192 + hw * 16 + 4 * g);
  const float csk = tcs[64 + hw * 16 + c] + tcs[192 + 64 + hw * 16 + c];
  const float csv = tcs[128 + hw * 16 + c] + tcs[192 + 128 + hw * 16 + c];
#pragma unroll
  for (int i = 0; i < 2; ++i) {
    const f32x2 st = *(const f32x2*)(tst + (rh * 32 + i * 16 + c) * 2);
    bf16x4 o;
#pragma unroll
    for (int r = 0; r < 4; ++r) o[r] = (__bf16)phi(bfr(st[1] * (acc[i][0][r] - st[0] * csq[r])));
    const int rt = mt * 4 + rh * 2 + i;
    *(bf16x4*)(pq + ((size_t)(rt * 16 + hq * 2 + (hw >> 1)) * 64 + lane) * 8 + (hw & 1) * 4) = o;
  }
  STAMP(7);
  {
    bf16x8 pk, vv;
#pragma unroll
    for (int i = 0; i < 2; ++i)
#pragma unroll
      for (int r = 0; r < 4; r += 2) {
        const f32x4 st = *(const f32x4*)(tst + (rh * 32 + i * 16 + 4 * g + r) * 2);
        pk[i * 4 + r] = (__bf16)phi(bfr(st[1] * (acc[i][1][r] - st[0] * csk)));
        pk[i * 4 + r + 1] = (__bf16)phi(bfr(st[3] * (acc[i][1][r + 1] - st[2] * csk)));
        vv[i * 4 + r] = (__bf16)(st[1] * (acc[i][2][r] - st[0] * csv));
        vv[i * 4 + r + 1] = (__bf16)(st[3] * (acc[i][2][r + 1] - st[2] * csv));
      }
    const f32x4 z4 = {0.f, 0.f, 0.f, 0.f};
    f32x4 kv = MFMA(pk, vv, z4);
    f32x4 zz = MFMA(pk, ones, z4);
    float* dst = kvpart + (size_t)((mt * 2 + rh) * 32 + hq * 4 + hw) * KVREC;
    *(f32x4*)(dst + lane * 4) = kv;
    if (c == 0) *(f32x4*)(dst + 256 + g * 4) = zz;
  }
  STAMP(2); STAMP_OUT(0, pq);
}

__global__ __launch_bounds__(256) void k1b_prep(const float* kvpart, __bf16* kvf, const __bf16* wo, const __bf16* wg,
                                                const __bf16* wu, const __bf16* wd, __bf16* wof, __bf16* wguf,
                                                __bf16* wdf, float* csp) {
  __shared__ __attribute__((aligned(16))) char smem[32768];
  STAMP_DECL; STAMP(0);
  const int tid = threadIdx.x, b = blockIdx.x, lane = tid & 63, wv = __builtin_amdgcn_readfirstlane(tid >> 6);
  const int g = lane >> 4, c = lane & 15;
  if (b < 34) {
    const int q = b * 64 + lane;
    f32x4 v[16];
#pragma unroll
    for (int m = 0; m < 16; ++m) v[m] = *(const f32x4*)(kvpart + (size_t)(wv * 16 + m) * 32 * KVREC + q * 4);
    f32x4 s = v[0];
#pragma unroll
    for (int m = 1; m < 16; ++m) s += v[m];
    f32x4* red = (f32x4*)smem;
    red[tid] = s;
    __syncthreads();
    if (tid < 64) {
      f32x4 t = (red[tid] + red[64 + tid]) + (red[128 + tid] + red[192 + tid]);
      bf16x4 o;
#pragma unroll
      for (int j = 0; j < 4; ++j) o[j] = (__bf16)t[j];
      *(bf16x4*)(kvf + q * 4) = o;
    }
    STAMP(1); STAMP_OUT(1, (const char*)kvpart - (10u << 20));
    return;
  }
  const int J = (b - 34) * 4 + wv;
  const __bf16* src; int rs, kq, stripe, kind, kmax;
  if (J < 64) { kind = 0; kq = J >> 4; stripe = J & 15; src = wo; rs = D_; kmax = 511; }
  else if (J < 236) { kind = 1; kq = wv; stripe = (J - 64) >> 2; src = wg; rs = F_; kmax = 511; }
  else if (J < 408) { kind = 2; kq = wv; stripe = (J - 236) >> 2; src = wu; rs = F_; kmax = 511; }
  else { kind = 3; kq = (J - 408) >> 4; stripe = (J - 408) & 15; src = wd; rs = D_; kmax = F_ - 1; }
  const unsigned lds0 = (unsigned)(size_t)smem + wv * 8192;
#pragma unroll
  for (int i = 0; i < 8; ++i) {
    const int r = i * 16 + (lane >> 2), p = lane & 3;
    int k = kq * 128 + r; k = k > kmax ? kmax : k;
    const int ch = (((p >> 1) ^ ((r >> 2) & 1)) << 1) | (p & 1);
    glds16(src + (size_t)k * rs + stripe * 32 + ch * 8, lds0 + i * 1024);
  }
  WAIT_VM(0);
  const char* bl = smem + wv * 8192 + (4 * g + (c >> 2)) * 64 + (c & 3) * 8;
  bf16x8 ones;
#pragma unroll
  for (int j = 0; j < 8; ++j) ones[j] = (__bf16)1.0f;
  f32x4 cs[2] = {{0.f, 0.f, 0.f, 0.f}, {0.f, 0.f, 0.f, 0.f}};
#pragma unroll
  for (int uu = 0; uu < 4; ++uu) {
    const int u = kq * 4 + uu;
#pragma unroll
    for (int j = 0; j < 2; ++j) {
      const int sw = ((j ^ (g & 1)) * 32);
      bf16x8 f = ldB(bl, uu * 2048 + sw, uu * 2048 + 1024 + sw);
      const int ctg = stripe * 2 + j;
      if (kind == 0) *(bf16x8*)(wof + ((size_t)(((ctg >> 2) * 16 + u) * 4 + (ctg & 3)) * 64 + lane) * 8) = f;
      else if (kind == 3) { if (u < 43) *(bf16x8*)(wdf + ((size_t)(((ctg >> 2) * 43 + u) * 4 + (ctg & 3)) * 64 + lane) * 8) = f; }
      else {
        *(bf16x8*)(wguf + ((size_t)((ctg * 16 + u) * 2 + (kind - 1)) * 64 + lane) * 8) = f;
        cs[j] = MFMA(ones, f, cs[j]);
      }
    }
  }
  if (kind == 1 || kind == 2) {
    __syncthreads();
    float* red = (float*)smem;
    if (g == 0) { red[wv * 32 + c] = cs[0][0]; red[wv * 32 + 16 + c] = cs[1][0]; }
    __syncthreads();
    if (tid < 32) csp[(kind - 1) * F_ + stripe * 32 + tid] = (red[tid] + red[32 + tid]) + (red[64 + tid] + red[96 + tid]);
  }
  STAMP(2); STAMP_OUT(1, (const char*)kvpart - (10u << 20));
}

constexpr int K2_LDS = 64 * 1024 + 17408;
__global__ __launch_bounds__(512, 2) void k2_attn_out(const __bf16* x, const __bf16* wof, const __bf16* pq,
                                                      const __bf16* kvf, __bf16* hfr, float* hstat) {
  __shared__ __attribute__((aligned(16))) char smem[K2_LDS];
  STAMP_DECL; STAMP(0);
  const int tid = threadIdx.x, lane = tid & 63, wv = __builtin_amdgcn_readfirstlane(tid >> 6);
  const int g = lane >> 4, c = lane & 15;
  const int rg = wv & 3, kh = wv >> 2;
  const int b = blockIdx.x, xcd = b & 7, idx = b >> 3;
  const int mt = xcd * 4 + (idx >> 3), nt = idx & 7;
  const unsigned lds0 = (unsigned)(size_t)smem;
  const int rt = mt * 4 + rg;

  bf16x4 xr[2];
#pragma unroll
  for (int p = 0; p < 2; ++p) xr[p] = *(const bf16x4*)(x + (size_t)(rt * 16 + c) * D_ + nt * 64 + (kh * 2 + p) * 16 + g * 4);
  bf16x8 pf[8];
#pragma unroll
  for (int h = 0; h < 8; ++h) pf[h] = *(const bf16x8*)(pq + ((size_t)(rt * 16 + kh * 8 + h) * 64 + lane) * 8);
#pragma unroll
  for (int i = 0; i < 3; ++i) {
    const int ins = i * 8 + wv;
    if (ins < 17) glds16s((const char*)kvf + ins * 1024, lane * 16, lds0 + 65536 + ins * 1024);
  }
#pragma unroll
  for (int i = 0; i < 8; ++i) {
    const int ins = i * 8 + wv;
    glds16s((const char*)wof + (size_t)nt * 65536 + ins * 1024, lane * 16, lds0 + ins * 1024);
  }
  f32x4 acc[4];
#pragma unroll
  for (int j = 0; j < 4; ++j) acc[j] = (f32x4){0.f, 0.f, 0.f, 0.f};
  STAMP(1);
  WAIT_VM(0);
  bar();
  STAMP(2);
  const char* bl = smem + kh * 32768 + lane * 16;
  const char* kvl = smem + 65536 + kh * 16 * (KVREC * 2);
  const f32x4 z4 = {0.f, 0.f, 0.f, 0.f};
#pragma unroll
  for (int h = 0; h < 8; ++h) {
    bf16x4 k0 = *(const bf16x4*)(kvl + (2 * h) * (KVREC * 2) + lane * 8);
    bf16x4 k1 = *(const bf16x4*)(kvl + (2 * h + 1) * (KVREC * 2) + lane * 8);
    bf16x4 z0 = *(const bf16x4*)(kvl + (2 * h) * (KVREC * 2) + 512 + g * 8);
    bf16x4 z1 = *(const bf16x4*)(kvl + (2 * h + 1) * (KVREC * 2) + 512 + g * 8);
    const bf16x4 zz = {(__bf16)0.f, (__bf16)0.f, (__bf16)0.f, (__bf16)0.f};
    bf16x8 a0 = __builtin_shufflevector(k0, zz, 0, 1, 2, 3, 4, 5, 6, 7);
    bf16x8 a1 = __builtin_shufflevector(zz, k1, 0, 1, 2, 3, 4, 5, 6, 7);
    bf16x8 d0 = __builtin_shufflevector(z0, zz, 0, 1, 2, 3, 4, 5, 6, 7);
    bf16x8 d1 = __builtin_shufflevector(zz, z1, 0, 1, 2, 3, 4, 5, 6, 7);
    f32x4 n0 = MFMA(a0, pf[h], z4);
    f32x4 n1 = MFMA(a1, pf[h], z4);
    f32x4 e0 = MFMA(d0, pf[h], z4);
    f32x4 e1 = MFMA(d1, pf[h], z4);
    const float r0 = __builtin_amdgcn_rcpf(bfr(bfr(e0[0]) + 1e-6f));
    const float r1 = __builtin_amdgcn_rcpf(bfr(bfr(e1[0]) + 1e-6f));
    bf16x8 of;
#pragma unroll
    for (int r = 0; r < 4; ++r) {
      of[r] = (__bf16)(bfr(n0[r]) * r0);
      of[4 + r] = (__bf16)(bfr(n1[r]) * r1);
    }
#pragma unroll
    for (int ct = 0; ct < 4; ++ct) {
      bf16x8 wf = *(const bf16x8*)(bl + (h * 4 + ct) * 1024);
      acc[ct] = MFMA(wf, of, acc[ct]);
    }
  }
  STAMP(3);
  __syncthreads();
  f32x4* red = (f32x4*)smem;
  red[((rg * 2 + kh) * 2 + 0) * 64 + lane] = kh ? acc[0] : acc[2];
  red[((rg * 2 + kh) * 2 + 1) * 64 + lane] = kh ? acc[1] : acc[3];
  __syncthreads();
  f32x4 f0 = (kh ? acc[2] : acc[0]), f1 = (kh ? acc[3] : acc[1]);
  {
    f32x4 o0 = red[((rg * 2 + (kh ^ 1)) * 2 + 0) * 64 + lane], o1 = red[((rg * 2 + (kh ^ 1)) * 2 + 1) * 64 + lane];
    f0 = kh ? (o0 + f0) : (f0 + o0);
    f1 = kh ? (o1 + f1) : (f1 + o1);
  }
  float hv[8];
  float s = 0.f;
#pragma unroll
  for (int r = 0; r < 4; ++r) {
    hv[r] = bfr((float)xr[0][r] + bfr(f0[r]));
    hv[4 + r] = bfr((float)xr[1][r] + bfr(f1[r]));
    s += hv[r] + hv[4 + r];
  }
  {
    bf16x8 o;
#pragma unroll
    for (int r = 0; r < 8; ++r) o[r] = (__bf16)hv[r];
    *(bf16x8*)(hfr + ((size_t)(rt * 16 + nt * 2 + kh) * 64 + lane) * 8) = o;
  }
  s += __shfl_xor(s, 16, 64);
  s += __shfl_xor(s, 32, 64);
  const float mean = s * (1.0f / 32.0f);
  float m2 = 0.f;
#pragma unroll
  for (int r = 0; r < 8; ++r) { float d = hv[r] - mean; m2 += d * d; }
  m2 += __shfl_xor(m2, 16, 64);
  m2 += __shfl_xor(m2, 32, 64);
  if (g == 0) *(f32x2*)(hstat + ((size_t)(rt * 16 + c) * 16 + nt * 2 + kh) * 2) = (f32x2){s, m2};
  STAMP(4); STAMP_OUT(2, pq);
}

constexpr int K3_RING = 7 * 20480;
constexpr int K3_LDS = K3_RING + 16384;
__global__ __launch_bounds__(512, 2) void k3_gate_up(const __bf16* hfr, const float* hstat, const __bf16* wguf,
                                                     const float* csp, __bf16* hh) {
  __shared__ __attribute__((aligned(16))) char smem[K3_LDS];
  STAMP_DECL; STAMP(0);
  const int tid = threadIdx.x, lane = tid & 63, wv = __builtin_amdgcn_readfirstlane(tid >> 6);
  const int g = lane >> 4, c = lane & 15;
  const int mw = wv & 3, nw = wv >> 2;
  const int b = blockIdx.x, xcd = b & 7, idx = b >> 3;
  const int mt = (xcd >> 2) * 8 + (idx >> 2), fgp = (xcd & 3) * 4 + (idx & 3);
  const int ft0 = (fgp * 43) >> 3;
  const unsigned lds0 = (unsigned)(size_t)smem;
  const int rt0 = mt * 8 + mw * 2;

  f32x4 cs[6];
#pragma unroll
  for (int ct = 0; ct < 6; ++ct)
    cs[ct] = *(const f32x4*)(csp + (ct & 1) * F_ + (ft0 + nw * 3 + (ct >> 1)) * 16 + g * 4);
  glds16s((const char*)hstat + (size_t)mt * 16384 + wv * 2048, lane * 16, lds0 + K3_RING + wv * 2048);
  glds16s((const char*)hstat + (size_t)mt * 16384 + wv * 2048 + 1024, lane * 16, lds0 + K3_RING + wv * 2048 + 1024);
  const char* pA = (const char*)hfr + (size_t)(mt * 8 + wv) * 16384;
  const char* pB0 = (const char*)wguf + ((size_t)(ft0 + (wv >> 1)) * 32 + (wv & 1)) * 1024;
  const char* pB1 = (const char*)wguf + ((size_t)(ft0 + 4 + ((wv & 3) >> 1)) * 32 + (wv & 1)) * 1024;
  auto issue = [&](int n) {
    const unsigned slot = lds0 + (n % 7) * 20480;
    glds16s(pA + n * 1024, lane * 16, slot + wv * 1024);
    glds16s(pB0 + n * 2048, lane * 16, slot + 8192 + wv * 1024);
    if (wv < 4) glds16s(pB1 + n * 2048, lane * 16, slot + 16384 + wv * 1024);
  };
#pragma unroll
  for (int n = 0; n < 6; ++n) issue(n);
  STAMP(1);

  f32x4 acc[2][6];
#pragma unroll
  for (int i = 0; i < 2; ++i)
#pragma unroll
    for (int j = 0; j < 6; ++j) acc[i][j] = (f32x4){0.f, 0.f, 0.f, 0.f};
  bf16x8 fa[2][2], fb[2][6];
  auto rd = [&](int n, int bu) {
    const char* sl = smem + (n % 7) * 20480 + lane * 16;
    fa[bu][0] = *(const bf16x8*)(sl + (mw * 2) * 1024);
    fa[bu][1] = *(const bf16x8*)(sl + (mw * 2 + 1) * 1024);
#pragma unroll
    for (int ct = 0; ct < 6; ++ct) fb[bu][ct] = *(const bf16x8*)(sl + 8192 + (nw * 6 + ct) * 1024);
  };
  if (wv < 4) WAIT_VM(15); else WAIT_VM(10);
  bar();
  STAMP(2);
  rd(0, 0);
  float mb[2], rs[2];
#pragma unroll
  for (int i = 0; i < 2; ++i) {
    const char* sp = smem + K3_RING + ((mw * 2 + i) * 16 + c) * 128 + g * 32;
    const f32x4 s0 = *(const f32x4*)(sp), s1 = *(const f32x4*)(sp + 16);
    float s = (s0[0] + s0[2]) + (s1[0] + s1[2]);
    s += __shfl_xor(s, 16, 64);
    s += __shfl_xor(s, 32, 64);
    const float mean = s * (1.0f / 512.0f);
    const float d0 = s0[0] * (1.0f / 32.0f) - mean, d1 = s0[2] * (1.0f / 32.0f) - mean;
    const float d2 = s1[0] * (1.0f / 32.0f) - mean, d3 = s1[2] * (1.0f / 32.0f) - mean;
    float m2 = (s0[1] + s0[3]) + (s1[1] + s1[3]) + 32.0f * ((d0 * d0 + d1 * d1) + (d2 * d2 + d3 * d3));
    m2 += __shfl_xor(m2, 16, 64);
    m2 += __shfl_xor(m2, 32, 64);
    mb[i] = bfr(mean);
    rs[i] = 1.0f / bfr(sqrtf(bfr(bfr(m2 * (1.0f / 512.0f)) + 1e-5f)));
  }
#pragma unroll
  for (int n = 0; n < 16; ++n) {
    if (n < 15) {
      const int y = (n + 5 < 15 ? n + 5 : 15) - (n + 1);
      if (wv < 4) wait_vm(3 * y); else wait_vm(2 * y);
      bar();
      if (n + 6 < 16) issue(n + 6);
      rd(n + 1, (n + 1) & 1);
    }
#pragma unroll
    for (int ct = 0; ct < 6; ++ct) {
      acc[0][ct] = MFMA(fb[n & 1][ct], fa[n & 1][0], acc[0][ct]);
      acc[1][ct] = MFMA(fb[n & 1][ct], fa[n & 1][1], acc[1][ct]);
    }
  }
  STAMP(6);
#pragma unroll
  for (int i = 0; i < 2; ++i) {
#pragma unroll
    for (int j = 0; j < 3; ++j) {
      const int ft = ft0 + nw * 3 + j;
      bf16x4 o;
#pragma unroll
      for (int r = 0; r < 4; ++r) {
        float gg = bfr(rs[i] * (acc[i][2 * j][r] - mb[i] * cs[2 * j][r]));
        float uu = bfr(rs[i] * (acc[i][2 * j + 1][r] - mb[i] * cs[2 * j + 1][r]));
        o[r] = (__bf16)(gg * uu);
      }
      *(bf16x4*)(hh + ((size_t)((rt0 + i) * 43 + (ft >> 1)) * 64 + lane) * 8 + (ft & 1) * 4) = o;
    }
  }
  STAMP(7); STAMP_OUT(3, (const char*)hfr - (2u << 20));
}

constexpr int K4_LDS = 160 * 1024;
__global__ __launch_bounds__(512, 2) void k4_down(const __bf16* hh, const __bf16* wdf, const __bf16* hfr, __bf16* out) {
  __shared__ __attribute__((aligned(16))) char smem[K4_LDS];
  STAMP_DECL; STAMP(0);
  const int tid = threadIdx.x, lane = tid & 63, wv = __builtin_amdgcn_readfirstlane(tid >> 6);
  const int g = lane >> 4, c = lane & 15;
  const int b = blockIdx.x, xcd = b & 7, idx = b >> 3;
  const int mt = xcd * 4 + (idx >> 3), nt = idx & 7;
  const int mi = wv & 1, kq = wv >> 1;
  const unsigned lds0 = (unsigned)(size_t)smem;
  const int rt0 = mt * 4 + mi * 2;
  const int u0 = kq * 11;
  const int nu = kq == 3 ? 10 : 11;
  const int fi = kq >> 1, fc = (kq & 1) * 2;
  bf16x8 hres = *(const bf16x8*)(hfr + ((size_t)((rt0 + fi) * 16 + nt * 2 + (kq & 1)) * 64 + lane) * 8);

  const char* wsrc = (const char*)wdf + ((size_t)(nt * 43 + u0) * 4 + mi * 2) * 1024;
  const unsigned ldsq = lds0 + kq * 40960 + mi * 2048;
  bf16x8 af[2][11];
  const __bf16* asrc = hh + ((size_t)(rt0 * 43 + u0) * 64 + lane) * 8;
#pragma unroll
  for (int s = 0; s < 10; ++s) {
    glds16s(wsrc + s * 4096, lane * 16, ldsq + s * 4096);
    glds16s(wsrc + s * 4096 + 1024, lane * 16, ldsq + s * 4096 + 1024);
    af[0][s] = *(const bf16x8*)(asrc + (size_t)s * 512);
    af[1][s] = *(const bf16x8*)(asrc + (size_t)(43 + s) * 512);
  }
  {
    const int s = kq == 3 ? 9 : 10;
    af[0][10] = *(const bf16x8*)(asrc + (size_t)s * 512);
    af[1][10] = *(const bf16x8*)(asrc + (size_t)(43 + s) * 512);
  }
  f32x4 acc[2][4];
#pragma unroll
  for (int i = 0; i < 2; ++i)
#pragma unroll
    for (int j = 0; j < 4; ++j) acc[i][j] = (f32x4){0.f, 0.f, 0.f, 0.f};
  const char* bl = smem + kq * 40960 + lane * 16;

  STAMP(1);
  wait_vm(4 * 8 + 2);
  bar();
  STAMP(2);
#pragma unroll
  for (int s = 0; s < 2; ++s) {
#pragma unroll
    for (int ct = 0; ct < 4; ++ct) {
      bf16x8 bf = *(const bf16x8*)(bl + s * 4096 + ct * 1024);
      acc[0][ct] = MFMA(bf, af[0][s], acc[0][ct]);
      acc[1][ct] = MFMA(bf, af[1][s], acc[1][ct]);
    }
  }
  asm volatile("s_waitcnt lgkmcnt(0)" ::: "memory");
  wait_vm(4 * 4 + 2);
  bar();
  STAMP(3);
  if (kq < 3) {
    glds16s(wsrc + 10 * 4096, lane * 16, ldsq);
    glds16s(wsrc + 10 * 4096 + 1024, lane * 16, ldsq + 1024);
  }
#pragma unroll
  for (int s = 2; s < 6; ++s) {
#pragma unroll
    for (int ct = 0; ct < 4; ++ct) {
      bf16x8 bf = *(const bf16x8*)(bl + s * 4096 + ct * 1024);
      acc[0][ct] = MFMA(bf, af[0][s], acc[0][ct]);
      acc[1][ct] = MFMA(bf, af[1][s], acc[1][ct]);
    }
  }
  WAIT_VM(0);
  bar();
  STAMP(4);
#pragma unroll
  for (int s = 6; s < 10; ++s) {
#pragma unroll
    for (int ct = 0; ct < 4; ++ct) {
      bf16x8 bf = *(const bf16x8*)(bl + s * 4096 + ct * 1024);
      acc[0][ct] = MFMA(bf, af[0][s], acc[0][ct]);
      acc[1][ct] = MFMA(bf, af[1][s], acc[1][ct]);
    }
  }
  if (nu == 11) {
#pragma unroll
    for (int ct = 0; ct < 4; ++ct) {
      bf16x8 bf = *(const bf16x8*)(bl + ct * 1024);
      acc[0][ct] = MFMA(bf, af[0][10], acc[0][ct]);
      acc[1][ct] = MFMA(bf, af[1][10], acc[1][ct]);
    }
  }
  STAMP(5);
  __syncthreads();
  f32x4* red = (f32x4*)smem;
#pragma unroll
  for (int i = 0; i < 2; ++i)
#pragma unroll
    for (int ct = 0; ct < 4; ++ct) red[(((mi * 4 + kq) * 2 + i) * 4 + ct) * 64 + lane] = acc[i][ct];
  __syncthreads();
#pragma unroll
  for (int p = 0; p < 2; ++p) {
    const int ct = fc + p;
    f32x4 s = (red[(((mi * 4 + 0) * 2 + fi) * 4 + ct) * 64 + lane] + red[(((mi * 4 + 1) * 2 + fi) * 4 + ct) * 64 + lane]) +
              (red[(((mi * 4 + 2) * 2 + fi) * 4 + ct) * 64 + lane] + red[(((mi * 4 + 3) * 2 + fi) * 4 + ct) * 64 + lane]);
    bf16x4 o;
#pragma unroll
    for (int r = 0; r < 4; ++r) o[r] = (__bf16)((float)hres[p * 4 + r] + bfr(s[r]));
    *(bf16x4*)(out + (size_t)((rt0 + fi) * 16 + c) * D_ + nt * 64 + ct * 16 + g * 4) = o;
  }
  STAMP(6); STAMP_OUT(4, (const char*)hfr - (2u << 20));
}

extern "C" void kernel_launch(void* const* d_in, const int* in_sizes, int n_in, void* d_out, int out_size,
                              void* d_ws, size_t ws_size, hipStream_t stream) {
  const __bf16* x = (const __bf16*)d_in[0];
  const __bf16* wqkv = (const __bf16*)d_in[1];
  const __bf16* wo = (const __bf16*)d_in[2];
  const __bf16* wg = (const __bf16*)d_in[4];
  const __bf16* wu = (const __bf16*)d_in[5];
  const __bf16* wd = (const __bf16*)d_in[6];
  char* ws = (char*)d_ws;
  __bf16* pq = (__bf16*)(ws);
  __bf16* hfr = (__bf16*)(ws + (2u << 20));
  __bf16* hh = (__bf16*)(ws + (4u << 20));
  float* kvpart = (float*)(ws + (10u << 20));
  __bf16* kvf = (__bf16*)(ws + (13u << 20));
  float* csp = (float*)(ws + (13u << 20) + 65536);
  float* hstat = (float*)(ws + (14u << 20));
  __bf16* wof = (__bf16*)(ws + (15u << 20));
  __bf16* wguf = (__bf16*)(ws + (16u << 20));
  __bf16* wdf = (__bf16*)(ws + (19u << 20));
  k1_qkv<<<256, 512, 0, stream>>>(x, wqkv, pq, kvpart);
  k1b_prep<<<180, 256, 0, stream>>>(kvpart, kvf, wo, wg, wu, wd, wof, wguf, wdf, csp);
  k2_attn_out<<<256, 512, 0, stream>>>(x, wof, pq, kvf, hfr, hstat);
  k3_gate_up<<<256, 512, 0, stream>>>(hfr, hstat, wguf, csp, hh);
  k4_down<<<256, 512, 0, stream>>>(hh, wdf, hfr, (__bf16*)d_out);
}
```

```cpp
#include <hip/hip_runtime.h>
#include <hip/hip_bf16.h>
#include <stdint.h>

typedef __attribute__((ext_vector_type(8))) __bf16 bf16x8;
typedef __attribute__((ext_vector_type(4))) __bf16 bf16x4;
typedef __attribute__((ext_vector_type(4))) short s16x4;
typedef __attribute__((ext_vector_type(4))) float f32x4;
typedef __attribute__((ext_vector_type(2))) float f32x2;
typedef __attribute__((ext_vector_type(4))) unsigned u32x4;
typedef __attribute__((ext_vector_type(2))) unsigned u32x2;
typedef __attribute__((address_space(3))) s16x4* lds_s16x4_ptr;

#define DI __device__ __forceinline__
#define MFMA(a, b, c) __builtin_amdgcn_mfma_f32_16x16x32_bf16((a), (b), (c), 0, 0, 0)

constexpr int S_ = 2048, D_ = 512, F_ = 1376;
constexpr int KVREC = 272;

#define STAMP_DECL
#define STAMP(i)
#define STAMP_OUT(k, wsbase)
DI float bfr(float x) { return (float)(__bf16)x; }
DI float bf2f(__bf16 x) { return (float)x; }

DI void glds16(const void* gsrc, unsigned lds_dst) {
  unsigned keep;
  asm volatile(
      "s_mov_b32 %0, m0\n\ts_mov_b32 m0, %2\n\ts_nop 0\n\tglobal_load_lds_dwordx4 %1, off\n\ts_mov_b32 m0, %0"
      : "=&s"(keep)
      : "v"(gsrc), "s"(lds_dst)
      : "memory");
}
DI void glds16s(const void* ubase, unsigned voff, unsigned lds_dst) {
  unsigned keep;
  asm volatile(
      "s_mov_b32 %0, m0\n\ts_mov_b32 m0, %3\n\ts_nop 0\n\tglobal_load_lds_dwordx4 %1, %2\n\ts_mov_b32 m0, %0"
      : "=&s"(keep)
      : "v"(voff), "s"(ubase), "s"(lds_dst)
      : "memory");
}
#define WAIT_VM(n) do { __builtin_amdgcn_sched_barrier(0); asm volatile("s_waitcnt vmcnt(" #n ") lgkmcnt(0)" ::: "memory"); } while (0)
DI void wait_vm(int n) {
  switch (n) {
    case 0: WAIT_VM(0); break;   case 1: WAIT_VM(1); break;   case 2: WAIT_VM(2); break;
    case 3: WAIT_VM(3); break;   case 4: WAIT_VM(4); break;   case 5: WAIT_VM(5); break;
    case 6: WAIT_VM(6); break;   case 7: WAIT_VM(7); break;   case 8: WAIT_VM(8); break;
    case 9: WAIT_VM(9); break;   case 10: WAIT_VM(10); break; case 11: WAIT_VM(11); break;
    case 12: WAIT_VM(12); break; case 13: WAIT_VM(13); break; case 14: WAIT_VM(14); break;
    case 15: WAIT_VM(15); break; case 16: WAIT_VM(16); break; case 17: WAIT_VM(17); break;
    case 18: WAIT_VM(18); break; case 19: WAIT_VM(19); break; case 20: WAIT_VM(20); break;
    case 21: WAIT_VM(21); break; case 22: WAIT_VM(22); break; case 23: WAIT_VM(23); break;
    case 24: WAIT_VM(24); break; case 25: WAIT_VM(25); break; case 26: WAIT_VM(26); break;
    case 27: WAIT_VM(27); break; case 28: WAIT_VM(28); break; case 29: WAIT_VM(29); break;
    case 30: WAIT_VM(30); break; case 31: WAIT_VM(31); break; case 32: WAIT_VM(32); break;
    case 33: WAIT_VM(33); break; case 34: WAIT_VM(34); break; case 35: WAIT_VM(35); break;
    case 36: WAIT_VM(36); break; case 37: WAIT_VM(37); break; case 38: WAIT_VM(38); break;
    case 39: WAIT_VM(39); break; case 40: WAIT_VM(40); break; case 41: WAIT_VM(41); break;
    case 42: WAIT_VM(42); break; case 43: WAIT_VM(43); break; case 44: WAIT_VM(44); break;
    default: WAIT_VM(0); break;
  }
}
DI void bar() { __builtin_amdgcn_s_barrier(); __builtin_amdgcn_sched_barrier(0); }

DI bf16x8 ldB(const char* smem_lane_base, int off0, int off1) {
  s16x4 lo = __builtin_amdgcn_ds_read_tr16_b64_v4i16((lds_s16x4_ptr)(smem_lane_base + off0));
  s16x4 hi = __builtin_amdgcn_ds_read_tr16_b64_v4i16((lds_s16x4_ptr)(smem_lane_base + off1));
  typedef __attribute__((ext_vector_type(8))) short s16x8;
  s16x8 v = __builtin_shufflevector(lo, hi, 0, 1, 2, 3, 4, 5, 6, 7);
  return __builtin_bit_cast(bf16x8, v);
}

DI float phi(float t) {
  float r = t > 0.f ? bfr(t + 1.0f) : bfr(__expf(t));
  return bfr(r + 1e-6f);
}

constexpr int K1_RING = 4 * 32768;
constexpr int K1_LDS = K1_RING + 4096;
__global__ __launch_bounds__(512, 2) void k1_qkv(const __bf16* x, const __bf16* w, __bf16* pq, float* kvpart) {
  __shared__ __attribute__((aligned(16))) char smem[K1_LDS];
  STAMP_DECL; STAMP(0);
  const int tid = threadIdx.x, lane = tid & 63, wv = __builtin_amdgcn_readfirstlane(tid >> 6);
  const int g = lane >> 4, c = lane & 15;
  const int hw = wv & 3, rh = wv >> 2;
  const int b = blockIdx.x, xcd = b & 7, idx = b >> 3;
  const int mt = (xcd >> 1) * 8 + (idx >> 2), hq = (xcd & 1) * 4 + (idx & 3);
  const unsigned lds0 = (unsigned)(size_t)smem;

  const __bf16* xsrc = x + (size_t)(mt * 64 + wv * 8) * D_;
  const __bf16* wsrc = w + (size_t)(wv * 8) * 1536 + hq * 64;
  unsigned xoff, woff;
  {
    const int r = lane >> 3, pos = lane & 7;
    const int row = wv * 8 + r;
    xoff = r * (D_ * 2) + (pos ^ ((row >> 1) & 7)) * 16;
    const int swz = ((r >> 1) & 1) | ((wv & 1) << 1);
    woff = r * 3072 + ((pos >> 1) ^ swz) * 32 + (pos & 1) * 16;
  }
  auto issue = [&](int s) {
    const unsigned slot = lds0 + (s & 3) * 32768;
    glds16s(xsrc + s * 64, xoff, slot + wv * 1024);
#pragma unroll
    for (int p = 0; p < 3; ++p) glds16s(wsrc + (size_t)s * 64 * 1536 + p * 512, woff, slot + 8192 + (p * 8 + wv) * 1024);
  };
  issue(0); issue(1); issue(2);
  STAMP(1);

  f32x4 acc[2][3];
#pragma unroll
  for (int i = 0; i < 2; ++i)
#pragma unroll
    for (int j = 0; j < 3; ++j) acc[i][j] = (f32x4){0.f, 0.f, 0.f, 0.f};
  f32x4 stt = {0.f, 0.f, 0.f, 0.f};
  f32x4 csum[3];
#pragma unroll
  for (int j = 0; j < 3; ++j) csum[j] = (f32x4){0.f, 0.f, 0.f, 0.f};
  bf16x8 ones;
#pragma unroll
  for (int j = 0; j < 8; ++j) ones[j] = (__bf16)1.0f;

  const int aoff = (rh * 32 + c) * 128 + ((g ^ (c >> 1)) * 16);
  const int boff = 8192 + g * 1024 + (c >> 2) * 128 + ((hw ^ (((c >> 3) & 1) | ((g & 1) << 1))) * 32) + (c & 3) * 8;
#pragma unroll
  for (int s = 0; s < 8; ++s) {
    if (s < 6) WAIT_VM(8); else if (s == 6) WAIT_VM(4); else WAIT_VM(0);
    bar();
    STAMP(8 + s);
    if (s + 3 < 8) issue(s + 3);
    const char* sl = smem + (s & 3) * 32768;
#pragma unroll
    for (int uu = 0; uu < 2; ++uu) {
      bf16x8 a0 = *(const bf16x8*)(sl + (aoff ^ (uu << 6)));
      bf16x8 a1 = *(const bf16x8*)(sl + (aoff ^ (uu << 6)) + 2048);
      bf16x8 bq = ldB(sl + boff, uu * 4096, uu * 4096 + 512);
      bf16x8 bk = ldB(sl + boff, 8192 + uu * 4096, 8192 + uu * 4096 + 512);
      bf16x8 bv = ldB(sl + boff, 16384 + uu * 4096, 16384 + uu * 4096 + 512);
      acc[0][0] = MFMA(bq, a0, acc[0][0]);
      acc[1][0] = MFMA(bq, a1, acc[1][0]);
      acc[0][1] = MFMA(a0, bk, acc[0][1]);
      acc[1][1] = MFMA(a1, bk, acc[1][1]);
      acc[0][2] = MFMA(a0, bv, acc[0][2]);
      acc[1][2] = MFMA(a1, bv, acc[1][2]);
      if (hw == 0) stt = MFMA(ones, a0, stt);
      else if (hw == 1) stt = MFMA(a0, a0, stt);
      else if (hw == 2) stt = MFMA(ones, a1, stt);
      else stt = MFMA(a1, a1, stt);
      if (uu == rh) {
        csum[0] = MFMA(ones, bq, csum[0]);
        csum[1] = MFMA(ones, bk, csum[1]);
        csum[2] = MFMA(ones, bv, csum[2]);
      }
    }
  }
  STAMP(6);
  float* tsum = (float*)(smem + K1_RING);
  float* tsq = tsum + 64;
  float* tcs = tsum + 128;
  float* tst = tsum + 512;
  {
    const int row = rh * 32 + (hw >> 1) * 16 + c;
    if ((hw & 1) == 0) { if (g == 0) tsum[row] = stt[0]; }
    else if (g == (c >> 2)) tsq[row] = (c & 2) ? ((c & 1) ? stt[3] : stt[2]) : ((c & 1) ? stt[1] : stt[0]);
    if (g == 0) {
#pragma unroll
      for (int p = 0; p < 3; ++p) tcs[rh * 192 + p * 64 + hw * 16 + c] = csum[p][0];
    }
  }
  __syncthreads();
  if (tid < 64) {
    const float mean = tsum[tid] * (1.0f / 512.0f);
    const float var = fmaxf(tsq[tid] * (1.0f / 512.0f) - mean * mean, 0.f);
    const float sb = bfr(sqrtf(bfr(bfr(var) + 1e-5f)));
    *(f32x2*)(tst + tid * 2) = (f32x2){bfr(mean), 1.0f / sb};
  }
  __syncthreads();
  const f32x4 csq = *(const f32x4*)(tcs + hw * 16 + 4 * g) + *(const f32x4*)(tcs + 192 + hw * 16 + 4 * g);
  const float csk = tcs[64 + hw * 16 + c] + tcs[192 + 64 + hw * 16 + c];
  const float csv = tcs[128 + hw * 16 + c] + tcs[192 + 128 + hw * 16 + c];
#pragma unroll
  for (int i = 0; i < 2; ++i) {
    const f32x2 st = *(const f32x2*)(tst + (rh * 32 + i * 16 + c) * 2);
    bf16x4 o;
#pragma unroll
    for (int r = 0; r < 4; ++r) o[r] = (__bf16)phi(bfr(st[1] * (acc[i][0][r] - st[0] * csq[r])));
    const int rt = mt * 4 + rh * 2 + i;
    *(bf16x4*)(pq + ((size_t)(rt * 16 + hq * 2 + (hw >> 1)) * 64 + lane) * 8 + (hw & 1) * 4) = o;
  }
  STAMP(7);
  {
    bf16x8 pk, vv;
#pragma unroll
    for (int i = 0; i < 2; ++i)
#pragma unroll
      for (int r = 0; r < 4; r += 2) {
        const f32x4 st = *(const f32x4*)(tst + (rh * 32 + i * 16 + 4 * g + r) * 2);
        pk[i * 4 + r] = (__bf16)phi(bfr(st[1] * (acc[i][1][r] - st[0] * csk)));
        pk[i * 4 + r + 1] = (__bf16)phi(bfr(st[3] * (acc[i][1][r + 1] - st[2] * csk)));
        vv[i * 4 + r] = (__bf16)(st[1] * (acc[i][2][r] - st[0] * csv));
        vv[i * 4 + r + 1] = (__bf16)(st[3] * (acc[i][2][r + 1] - st[2] * csv));
      }
    const f32x4 z4 = {0.f, 0.f, 0.f, 0.f};
    f32x4 kv = MFMA(pk, vv, z4);
    f32x4 zz = MFMA(pk, ones, z4);
    float* dst = kvpart + (size_t)((mt * 2 + rh) * 32 + hq * 4 + hw) * KVREC;
    *(f32x4*)(dst + lane * 4) = kv;
    if (c == 0) *(f32x4*)(dst + 256 + g * 4) = zz;
  }
  STAMP(2); STAMP_OUT(0, pq);
}

__global__ __launch_bounds__(256) void k1b_reduce(const float* kvpart, __bf16* kvf, const __bf16* wo, const __bf16* wg,
                                                  const __bf16* wu, const __bf16* wd, float* csp) {
  __shared__ __attribute__((aligned(16))) float red[32 * 64];
  STAMP_DECL; STAMP(0);
  const int tid = threadIdx.x, b = blockIdx.x, lane = tid & 63, wv = __builtin_amdgcn_readfirstlane(tid >> 6);
  if (b < 34) {
    const int q = b * 64 + lane;
    f32x4 v[16];
#pragma unroll
    for (int m = 0; m < 16; ++m) v[m] = *(const f32x4*)(kvpart + (size_t)(wv * 16 + m) * 32 * KVREC + q * 4);
    f32x4 s = v[0];
#pragma unroll
    for (int m = 1; m < 16; ++m) s += v[m];
    f32x4* r4 = (f32x4*)red;
    r4[tid] = s;
    __syncthreads();
    if (tid < 64) {
      f32x4 t = (r4[tid] + r4[64 + tid]) + (r4[128 + tid] + r4[192 + tid]);
      bf16x4 o;
#pragma unroll
      for (int j = 0; j < 4; ++j) o[j] = (__bf16)t[j];
      *(bf16x4*)(kvf + q * 4) = o;
    }
    STAMP(1); STAMP_OUT(1, (const char*)kvpart - (10u << 20));
    return;
  }
  if (b >= 77) {
    const int pb = b - 77;
    const u32x4* src = pb < 32 ? (const u32x4*)wo + (size_t)pb * 1024 : (const u32x4*)wd + (size_t)(pb - 32) * 1024;
    u32x4 v0 = src[tid], v1 = src[256 + tid], v2 = src[512 + tid], v3 = src[768 + tid];
    asm volatile("" :: "v"(v0), "v"(v1), "v"(v2), "v"(v3));
    return;
  }
  const int cb = b - 34;
  const int col0 = cb * 64 + (tid & 7) * 8;
  const __bf16* src = col0 < F_ ? (wg + col0) : (wu + (col0 - F_));
  const int kq = tid >> 3;
  bf16x8 v[16];
#pragma unroll
  for (int r = 0; r < 16; ++r) v[r] = *(const bf16x8*)(src + (size_t)(kq * 16 + r) * F_);
  float s[8];
#pragma unroll
  for (int j = 0; j < 8; ++j) s[j] = 0.f;
#pragma unroll
  for (int r = 0; r < 16; ++r)
#pragma unroll
    for (int j = 0; j < 8; ++j) s[j] += (float)v[r][j];
#pragma unroll
  for (int j = 0; j < 8; ++j) red[kq * 64 + (tid & 7) * 8 + j] = s[j];
  __syncthreads();
  if (tid < 64) {
    float t = 0.f;
#pragma unroll
    for (int q = 0; q < 32; ++q) t += red[q * 64 + tid];
    csp[cb * 64 + tid] = t;
  }
  STAMP(2); STAMP_OUT(1, (const char*)kvpart - (10u << 20));
}

constexpr int K2_LDS = 64 * 1024 + 17408;
__global__ __launch_bounds__(512, 2) void k2_attn_out(const __bf16* x, const __bf16* wo, const __bf16* pq,
                                                      const __bf16* kvf, __bf16* hfr, float* hstat) {
  __shared__ __attribute__((aligned(16))) char smem[K2_LDS];
  STAMP_DECL; STAMP(0);
  const int tid = threadIdx.x, lane = tid & 63, wv = __builtin_amdgcn_readfirstlane(tid >> 6);
  const int g = lane >> 4, c = lane & 15;
  const int rg = wv & 3, kh = wv >> 2;
  const int b = blockIdx.x, xcd = b & 7, idx = b >> 3;
  const int mt = xcd * 4 + (idx >> 3), nt = idx & 7;
  const unsigned lds0 = (unsigned)(size_t)smem;
  const int rt = mt * 4 + rg;

  bf16x4 xr[2];
#pragma unroll
  for (int p = 0; p < 2; ++p) xr[p] = *(const bf16x4*)(x + (size_t)(rt * 16 + c) * D_ + nt * 64 + (kh * 2 + p) * 16 + g * 4);
  bf16x8 pf[8];
#pragma unroll
  for (int h = 0; h < 8; ++h) pf[h] = *(const bf16x8*)(pq + ((size_t)(rt * 16 + kh * 8 + h) * 64 + lane) * 8);
#pragma unroll
  for (int i = 0; i < 3; ++i) {
    const int ins = i * 8 + wv;
    if (ins < 17) glds16s((const char*)kvf + ins * 1024, lane * 16, lds0 + 65536 + ins * 1024);
  }
  {
    const int r = lane >> 3, pos = lane & 7;
    const unsigned woff = r * (D_ * 2) + (((pos >> 1) ^ ((r >> 1) & 3)) * 32) + (pos & 1) * 16;
#pragma unroll
    for (int i = 0; i < 8; ++i) {
      const int kb = i * 8 + wv;
      glds16s(wo + (size_t)kb * 8 * D_ + nt * 64, woff, lds0 + kb * 1024);
    }
  }
  f32x4 acc[4];
#pragma unroll
  for (int j = 0; j < 4; ++j) acc[j] = (f32x4){0.f, 0.f, 0.f, 0.f};
  STAMP(1);
  WAIT_VM(0);
  bar();
  STAMP(2);
  const int brow = (g & 1) * 4 + (c >> 2);
  const char* bl = smem + kh * 32768 + (g >> 1) * 1024 + brow * 128 + (c & 3) * 8;
  const int bsw = (brow >> 1) & 3;
  const char* kvl = smem + 65536 + kh * 16 * (KVREC * 2);
  const f32x4 z4 = {0.f, 0.f, 0.f, 0.f};
#pragma unroll
  for (int h = 0; h < 8; ++h) {
    bf16x4 k0 = *(const bf16x4*)(kvl + (2 * h) * (KVREC * 2) + lane * 8);
    bf16x4 k1 = *(const bf16x4*)(kvl + (2 * h + 1) * (KVREC * 2) + lane * 8);
    bf16x4 z0 = *(const bf16x4*)(kvl + (2 * h) * (KVREC * 2) + 512 + g * 8);
    bf16x4 z1 = *(const bf16x4*)(kvl + (2 * h + 1) * (KVREC * 2) + 512 + g * 8);
    const bf16x4 zz = {(__bf16)0.f, (__bf16)0.f, (__bf16)0.f, (__bf16)0.f};
    bf16x8 a0 = __builtin_shufflevector(k0, zz, 0, 1, 2, 3, 4, 5, 6, 7);
    bf16x8 a1 = __builtin_shufflevector(zz, k1, 0, 1, 2, 3, 4, 5, 6, 7);
    bf16x8 d0 = __builtin_shufflevector(z0, zz, 0, 1, 2, 3, 4, 5, 6, 7);
    bf16x8 d1 = __builtin_shufflevector(zz, z1, 0, 1, 2, 3, 4, 5, 6, 7);
    f32x4 n0 = MFMA(a0, pf[h], z4);
    f32x4 n1 = MFMA(a1, pf[h], z4);
    f32x4 e0 = MFMA(d0, pf[h], z4);
    f32x4 e1 = MFMA(d1, pf[h], z4);
    const float r0 = __builtin_amdgcn_rcpf(bfr(bfr(e0[0]) + 1e-6f));
    const float r1 = __builtin_amdgcn_rcpf(bfr(bfr(e1[0]) + 1e-6f));
    bf16x8 of;
#pragma unroll
    for (int r = 0; r < 4; ++r) {
      of[r] = (__bf16)(bfr(n0[r]) * r0);
      of[4 + r] = (__bf16)(bfr(n1[r]) * r1);
    }
#pragma unroll
    for (int ct = 0; ct < 4; ++ct) {
      bf16x8 wf = ldB(bl + ((ct ^ bsw) * 32), h * 4096, h * 4096 + 2048);
      acc[ct] = MFMA(wf, of, acc[ct]);
    }
  }
  STAMP(3);
  __syncthreads();
  f32x4* red = (f32x4*)smem;
  red[((rg * 2 + kh) * 2 + 0) * 64 + lane] = kh ? acc[0] : acc[2];
  red[((rg * 2 + kh) * 2 + 1) * 64 + lane] = kh ? acc[1] : acc[3];
  __syncthreads();
  f32x4 f0 = (kh ? acc[2] : acc[0]), f1 = (kh ? acc[3] : acc[1]);
  {
    f32x4 o0 = red[((rg * 2 + (kh ^ 1)) * 2 + 0) * 64 + lane], o1 = red[((rg * 2 + (kh ^ 1)) * 2 + 1) * 64 + lane];
    f0 = kh ? (o0 + f0) : (f0 + o0);
    f1 = kh ? (o1 + f1) : (f1 + o1);
  }
  float hv[8];
  float s = 0.f;
#pragma unroll
  for (int r = 0; r < 4; ++r) {
    hv[r] = bfr((float)xr[0][r] + bfr(f0[r]));
    hv[4 + r] = bfr((float)xr[1][r] + bfr(f1[r]));
    s += hv[r] + hv[4 + r];
  }
  {
    bf16x8 o;
#pragma unroll
    for (int r = 0; r < 8; ++r) o[r] = (__bf16)hv[r];
    *(bf16x8*)(hfr + ((size_t)(rt * 16 + nt * 2 + kh) * 64 + lane) * 8) = o;
  }
  s += __shfl_xor(s, 16, 64);
  s += __shfl_xor(s, 32, 64);
  const float mean = s * (1.0f / 32.0f);
  float m2 = 0.f;
#pragma unroll
  for (int r = 0; r < 8; ++r) { float d = hv[r] - mean; m2 += d * d; }
  m2 += __shfl_xor(m2, 16, 64);
  m2 += __shfl_xor(m2, 32, 64);
  if (g == 0) *(f32x2*)(hstat + ((size_t)(rt * 16 + c) * 16 + nt * 2 + kh) * 2) = (f32x2){s, m2};
  STAMP(4); STAMP_OUT(2, pq);
}

constexpr int K3_RING = 7 * 20480;
constexpr int K3_LDS = K3_RING + 16384;
__global__ __launch_bounds__(512, 2) void k3_gate_up(const __bf16* hfr, const float* hstat, const __bf16* wg,
                                                     const __bf16* wu, const float* csp, __bf16* hh) {
  __shared__ __attribute__((aligned(16))) char smem[K3_LDS];
  STAMP_DECL; STAMP(0);
  const int tid = threadIdx.x, lane = tid & 63, wv = __builtin_amdgcn_readfirstlane(tid >> 6);
  const int g = lane >> 4, c = lane & 15;
  const int mw = wv & 3, nw = wv >> 2;
  const int b = blockIdx.x, xcd = b & 7, idx = b >> 3;
  const int mt = (xcd >> 2) * 8 + (idx >> 2), fgp = (xcd & 3) * 4 + (idx & 3);
  const int ft0 = 2 * ((fgp * 43) >> 4);
  const unsigned lds0 = (unsigned)(size_t)smem;
  const int rt0 = mt * 8 + mw * 2;

  const char* pA = (const char*)hfr + (size_t)(mt * 8 + wv) * 16384;
  const unsigned boffl = (lane >> 2) * (F_ * 2) + ((((lane >> 1) & 1) ^ ((lane >> 4) & 1)) * 32) + (lane & 1) * 16;
  const int q0 = wv, q1 = 8 + (wv & 3);
  const __bf16* pB0 = (q0 < 6 ? wg : wu) + (size_t)((q0 & 1) * 16) * F_ + (ft0 + ((q0 % 6) >> 1) * 2) * 16;
  const __bf16* pB1 = wu + (size_t)((q1 & 1) * 16) * F_ + (ft0 + ((q1 - 6) >> 1) * 2) * 16;
  auto issue = [&](int n) {
    const unsigned slot = lds0 + (n % 7) * 20480;
    glds16s(pA + n * 1024, lane * 16, slot + wv * 1024);
    glds16s(pB0 + (size_t)n * 32 * F_, boffl, slot + 8192 + wv * 1024);
    if (wv < 4) glds16s(pB1 + (size_t)n * 32 * F_, boffl, slot + 16384 + wv * 1024);
  };
  issue(0); issue(1);
  glds16s((const char*)hstat + (size_t)mt * 16384 + wv * 2048, lane * 16, lds0 + K3_RING + wv * 2048);
  glds16s((const char*)hstat + (size_t)mt * 16384 + wv * 2048 + 1024, lane * 16, lds0 + K3_RING + wv * 2048 + 1024);
#pragma unroll
  for (int n = 2; n < 6; ++n) issue(n);
  STAMP(1);

  f32x4 acc[2][6];
#pragma unroll
  for (int i = 0; i < 2; ++i)
#pragma unroll
    for (int j = 0; j < 6; ++j) acc[i][j] = (f32x4){0.f, 0.f, 0.f, 0.f};
  bf16x8 fa[2][2], fb[2][6];
  const int bl0 = 8192 + (4 * g + (c >> 2)) * 64 + (c & 3) * 8;
  auto rd = [&](int n, int bu) {
    const char* sl = smem + (n % 7) * 20480;
    fa[bu][0] = *(const bf16x8*)(sl + lane * 16 + (mw * 2) * 1024);
    fa[bu][1] = *(const bf16x8*)(sl + lane * 16 + (mw * 2 + 1) * 1024);
#pragma unroll
    for (int ct = 0; ct < 6; ++ct) {
      const int ftl = nw * 3 + (ct >> 1);
      const char* bp = sl + bl0 + ((ct & 1) * 6 + (ftl >> 1) * 2) * 1024 + (((ftl & 1) ^ (g & 1)) * 32);
      fb[bu][ct] = ldB(bp, 0, 1024);
    }
  };
  if (wv < 4) WAIT_VM(17); else WAIT_VM(12);
  bar();
  STAMP(2);
  rd(0, 0);
  float mb[2], rs[2];
  f32x4 cs[6];
#pragma unroll
  for (int n = 0; n < 16; ++n) {
    if (n < 15) {
      const int y = (n + 5 < 15 ? n + 5 : 15) - (n + 1);
      if (wv < 4) wait_vm(3 * y + (n == 0 ? 2 : 0)); else wait_vm(2 * y + (n == 0 ? 2 : 0));
      bar();
      if (n + 6 < 16) issue(n + 6);
      rd(n + 1, (n + 1) & 1);
    }
    if (n == 2) {
#pragma unroll
      for (int i = 0; i < 2; ++i) {
        const char* sp = smem + K3_RING + ((mw * 2 + i) * 16 + c) * 128 + g * 32;
        const f32x4 s0 = *(const f32x4*)(sp), s1 = *(const f32x4*)(sp + 16);
        float s = (s0[0] + s0[2]) + (s1[0] + s1[2]);
        s += __shfl_xor(s, 16, 64);
        s += __shfl_xor(s, 32, 64);
        const float mean = s * (1.0f / 512.0f);
        const float d0 = s0[0] * (1.0f / 32.0f) - mean, d1 = s0[2] * (1.0f / 32.0f) - mean;
        const float d2 = s1[0] * (1.0f / 32.0f) - mean, d3 = s1[2] * (1.0f / 32.0f) - mean;
        float m2 = (s0[1] + s0[3]) + (s1[1] + s1[3]) + 32.0f * ((d0 * d0 + d1 * d1) + (d2 * d2 + d3 * d3));
        m2 += __shfl_xor(m2, 16, 64);
        m2 += __shfl_xor(m2, 32, 64);
        mb[i] = bfr(mean);
        rs[i] = 1.0f / bfr(sqrtf(bfr(bfr(m2 * (1.0f / 512.0f)) + 1e-5f)));
      }
    }
    if (n == 10) {
#pragma unroll
      for (int ct = 0; ct < 6; ++ct)
        cs[ct] = *(const f32x4*)(csp + (ct & 1) * F_ + (ft0 + nw * 3 + (ct >> 1)) * 16 + g * 4);
    }
#pragma unroll
    for (int ct = 0; ct < 6; ++ct) {
      acc[0][ct] = MFMA(fb[n & 1][ct], fa[n & 1][0], acc[0][ct]);
      acc[1][ct] = MFMA(fb[n & 1][ct], fa[n & 1][1], acc[1][ct]);
    }
  }
  STAMP(6);
#pragma unroll
  for (int i = 0; i < 2; ++i) {
#pragma unroll
    for (int j = 0; j < 3; ++j) {
      const int ft = ft0 + nw * 3 + j;
      bf16x4 o;
#pragma unroll
      for (int r = 0; r < 4; ++r) {
        float gg = bfr(rs[i] * (acc[i][2 * j][r] - mb[i] * cs[2 * j][r]));
        float uu = bfr(rs[i] * (acc[i][2 * j + 1][r] - mb[i] * cs[2 * j + 1][r]));
        o[r] = (__bf16)(gg * uu);
      }
      *(bf16x4*)(hh + ((size_t)((rt0 + i) * 43 + (ft >> 1)) * 64 + lane) * 8 + (ft & 1) * 4) = o;
    }
  }
  STAMP(7); STAMP_OUT(3, (const char*)hfr - (2u << 20));
}

constexpr int K4_LDS = 160 * 1024;
__global__ __launch_bounds__(512, 2) void k4_down(const __bf16* hh, const __bf16* wd, const __bf16* hfr, __bf16* out) {
  __shared__ __attribute__((aligned(16))) char smem[K4_LDS];
  STAMP_DECL; STAMP(0);
  const int tid = threadIdx.x, lane = tid & 63, wv = __builtin_amdgcn_readfirstlane(tid >> 6);
  const int g = lane >> 4, c = lane & 15;
  const int b = blockIdx.x, xcd = b & 7, idx = b >> 3;
  const int mt = xcd * 4 + (idx >> 3), nt = idx & 7;
  const int mi = wv & 1, kq = wv >> 1;
  const unsigned lds0 = (unsigned)(size_t)smem;
  const int rt0 = mt * 4 + mi * 2;
  const int u0 = kq * 11;
  const int nu = kq == 3 ? 10 : 11;
  const int fi = kq >> 1, fc = (kq & 1) * 2;
  bf16x8 hres = *(const bf16x8*)(hfr + ((size_t)((rt0 + fi) * 16 + nt * 2 + (kq & 1)) * 64 + lane) * 8);

  const __bf16* wsrc = wd + (size_t)(u0 * 32 + mi * 16) * D_ + nt * 64;
  const unsigned woff = (lane >> 3) * (D_ * 2) + ((((lane & 7) >> 1) ^ ((lane >> 4) & 3)) * 32) + (lane & 1) * 16;
  const unsigned ldsq = lds0 + kq * 40960 + mi * 2048;
  bf16x8 af[2][11];
  const __bf16* asrc = hh + ((size_t)(rt0 * 43 + u0) * 64 + lane) * 8;
#pragma unroll
  for (int s = 0; s < 10; ++s) {
    glds16s(wsrc + (size_t)(s * 32) * D_, woff, ldsq + s * 4096);
    glds16s(wsrc + (size_t)(s * 32 + 8) * D_, woff, ldsq + s * 4096 + 1024);
    af[0][s] = *(const bf16x8*)(asrc + (size_t)s * 512);
    af[1][s] = *(const bf16x8*)(asrc + (size_t)(43 + s) * 512);
  }
  {
    const int s = kq == 3 ? 9 : 10;
    af[0][10] = *(const bf16x8*)(asrc + (size_t)s * 512);
    af[1][10] = *(const bf16x8*)(asrc + (size_t)(43 + s) * 512);
  }
  f32x4 acc[2][4];
#pragma unroll
  for (int i = 0; i < 2; ++i)
#pragma unroll
    for (int j = 0; j < 4; ++j) acc[i][j] = (f32x4){0.f, 0.f, 0.f, 0.f};
  const int brow = (g & 1) * 4 + (c >> 2);
  const char* bl = smem + kq * 40960 + (g >> 1) * 1024 + brow * 128 + (c & 3) * 8;
  const int bsw = (brow >> 1) & 3;

  STAMP(1);
  wait_vm(4 * 8 + 2);
  bar();
  STAMP(2);
#pragma unroll
  for (int s = 0; s < 2; ++s) {
#pragma unroll
    for (int ct = 0; ct < 4; ++ct) {
      bf16x8 bf = ldB(bl + ((ct ^ bsw) * 32), s * 4096, s * 4096 + 2048);
      acc[0][ct] = MFMA(bf, af[0][s], acc[0][ct]);
      acc[1][ct] = MFMA(bf, af[1][s], acc[1][ct]);
    }
  }
  wait_vm(4 * 4 + 2);
  bar();
  STAMP(3);
  if (kq < 3) {
    glds16s(wsrc + (size_t)(10 * 32) * D_, woff, ldsq);
    glds16s(wsrc + (size_t)(10 * 32 + 8) * D_, woff, ldsq + 1024);
  }
#pragma unroll
  for (int s = 2; s < 6; ++s) {
#pragma unroll
    for (int ct = 0; ct < 4; ++ct) {
      bf16x8 bf = ldB(bl + ((ct ^ bsw) * 32), s * 4096, s * 4096 + 2048);
      acc[0][ct] = MFMA(bf, af[0][s], acc[0][ct]);
      acc[1][ct] = MFMA(bf, af[1][s], acc[1][ct]);
    }
  }
  WAIT_VM(0);
  bar();
  STAMP(4);
#pragma unroll
  for (int s = 6; s < 10; ++s) {
#pragma unroll
    for (int ct = 0; ct < 4; ++ct) {
      bf16x8 bf = ldB(bl + ((ct ^ bsw) * 32), s * 4096, s * 4096 + 2048);
      acc[0][ct] = MFMA(bf, af[0][s], acc[0][ct]);
      acc[1][ct] = MFMA(bf, af[1][s], acc[1][ct]);
    }
  }
  if (nu == 11) {
#pragma unroll
    for (int ct = 0; ct < 4; ++ct) {
      bf16x8 bf = ldB(bl + ((ct ^ bsw) * 32), 0, 2048);
      acc[0][ct] = MFMA(bf, af[0][10], acc[0][ct]);
      acc[1][ct] = MFMA(bf, af[1][10], acc[1][ct]);
    }
  }
  STAMP(5);
  __syncthreads();
  f32x4* red = (f32x4*)smem;
#pragma unroll
  for (int i = 0; i < 2; ++i)
#pragma unroll
    for (int ct = 0; ct < 4; ++ct) red[(((mi * 4 + kq) * 2 + i) * 4 + ct) * 64 + lane] = acc[i][ct];
  __syncthreads();
#pragma unroll
  for (int p = 0; p < 2; ++p) {
    const int ct = fc + p;
    f32x4 s = (red[(((mi * 4 + 0) * 2 + fi) * 4 + ct) * 64 + lane] + red[(((mi * 4 + 1) * 2 + fi) * 4 + ct) * 64 + lane]) +
              (red[(((mi * 4 + 2) * 2 + fi) * 4 + ct) * 64 + lane] + red[(((mi * 4 + 3) * 2 + fi) * 4 + ct) * 64 + lane]);
    bf16x4 o;
#pragma unroll
    for (int r = 0; r < 4; ++r) o[r] = (__bf16)((float)hres[p * 4 + r] + bfr(s[r]));
    *(bf16x4*)(out + (size_t)((rt0 + fi) * 16 + c) * D_ + nt * 64 + ct * 16 + g * 4) = o;
  }
  STAMP(6); STAMP_OUT(4, (const char*)hfr - (2u << 20));
}

extern "C" void kernel_launch(void* const* d_in, const int* in_sizes, int n_in, void* d_out, int out_size,
                              void* d_ws, size_t ws_size, hipStream_t stream) {
  const __bf16* x = (const __bf16*)d_in[0];
  const __bf16* wqkv = (const __bf16*)d_in[1];
  const __bf16* wo = (const __bf16*)d_in[2];
  const __bf16* wg = (const __bf16*)d_in[4];
  const __bf16* wu = (const __bf16*)d_in[5];
  const __bf16* wd = (const __bf16*)d_in[6];
  char* ws = (char*)d_ws;
  __bf16* pq = (__bf16*)(ws);
  __bf16* hfr = (__bf16*)(ws + (2u << 20));
  __bf16* hh = (__bf16*)(ws + (4u << 20));
  float* kvpart = (float*)(ws + (10u << 20));
  __bf16* kvf = (__bf16*)(ws + (13u << 20));
  float* csp = (float*)(ws + (13u << 20) + 65536);
  float* hstat = (float*)(ws + (14u << 20));
  k1_qkv<<<256, 512, 0, stream>>>(x, wqkv, pq, kvpart);
  k1b_reduce<<<77 + 118, 256, 0, stream>>>(kvpart, kvf, wo, wg, wu, wd, csp);
  k2_attn_out<<<256, 512, 0, stream>>>(x, wo, pq, kvf, hfr, hstat);
  k3_gate_up<<<256, 512, 0, stream>>>(hfr, hstat, wg, wu, csp, hh);
  k4_down<<<256, 512, 0, stream>>>(hh, wd, hfr, (__bf16*)d_out);
}
```

```cpp
#include <hip/hip_runtime.h>
#include <hip/hip_bf16.h>
#include <stdint.h>

typedef __attribute__((ext_vector_type(8))) __bf16 bf16x8;
typedef __attribute__((ext_vector_type(4))) __bf16 bf16x4;
typedef __attribute__((ext_vector_type(4))) short s16x4;
typedef __attribute__((ext_vector_type(4))) float f32x4;
typedef __attribute__((ext_vector_type(2))) float f32x2;
typedef __attribute__((ext_vector_type(4))) unsigned u32x4;
typedef __attribute__((ext_vector_type(2))) unsigned u32x2;
typedef __attribute__((address_space(3))) s16x4* lds_s16x4_ptr;

#define DI __device__ __forceinline__
#define MFMA(a, b, c) __builtin_amdgcn_mfma_f32_16x16x32_bf16((a), (b), (c), 0, 0, 0)

constexpr int S_ = 2048, D_ = 512, F_ = 1376;
constexpr int KVREC = 272;

#define STAMP_DECL
#define STAMP(i)
#define STAMP_OUT(k, wsbase)
DI float bfr(float x) { return (float)(__bf16)x; }
DI float bf2f(__bf16 x) { return (float)x; }

DI void glds16(const void* gsrc, unsigned lds_dst) {
  unsigned keep;
  asm volatile(
      "s_mov_b32 %0, m0\n\ts_mov_b32 m0, %2\n\ts_nop 0\n\tglobal_load_lds_dwordx4 %1, off\n\ts_mov_b32 m0, %0"
      : "=&s"(keep)
      : "v"(gsrc), "s"(lds_dst)
      : "memory");
}
DI void glds16s(const void* ubase, unsigned voff, unsigned lds_dst) {
  unsigned keep;
  asm volatile(
      "s_mov_b32 %0, m0\n\ts_mov_b32 m0, %3\n\ts_nop 0\n\tglobal_load_lds_dwordx4 %1, %2\n\ts_mov_b32 m0, %0"
      : "=&s"(keep)
      : "v"(voff), "s"(ubase), "s"(lds_dst)
      : "memory");
}
#define WAIT_VM(n) do { __builtin_amdgcn_sched_barrier(0); asm volatile("s_waitcnt vmcnt(" #n ") lgkmcnt(0)" ::: "memory"); } while (0)
DI void wait_vm(int n) {
  switch (n) {
    case 0: WAIT_VM(0); break;   case 1: WAIT_VM(1); break;   case 2: WAIT_VM(2); break;
    case 3: WAIT_VM(3); break;   case 4: WAIT_VM(4); break;   case 5: WAIT_VM(5); break;
    case 6: WAIT_VM(6); break;   case 7: WAIT_VM(7); break;   case 8: WAIT_VM(8); break;
    case 9: WAIT_VM(9); break;   case 10: WAIT_VM(10); break; case 11: WAIT_VM(11); break;
    case 12: WAIT_VM(12); break; case 13: WAIT_VM(13); break; case 14: WAIT_VM(14); break;
    case 15: WAIT_VM(15); break; case 16: WAIT_VM(16); break; case 17: WAIT_VM(17); break;
    case 18: WAIT_VM(18); break; case 19: WAIT_VM(19); break; case 20: WAIT_VM(20); break;
    case 21: WAIT_VM(21); break; case 22: WAIT_VM(22); break; case 23: WAIT_VM(23); break;
    case 24: WAIT_VM(24); break; case 25: WAIT_VM(25); break; case 26: WAIT_VM(26); break;
    case 27: WAIT_VM(27); break; case 28: WAIT_VM(28); break; case 29: WAIT_VM(29); break;
    case 30: WAIT_VM(30); break; case 31: WAIT_VM(31); break; case 32: WAIT_VM(32); break;
    case 33: WAIT_VM(33); break; case 34: WAIT_VM(34); break; case 35: WAIT_VM(35); break;
    case 36: WAIT_VM(36); break; case 37: WAIT_VM(37); break; case 38: WAIT_VM(38); break;
    case 39: WAIT_VM(39); break; case 40: WAIT_VM(40); break; case 41: WAIT_VM(41); break;
    case 42: WAIT_VM(42); break; case 43: WAIT_VM(43); break; case 44: WAIT_VM(44); break;
    default: WAIT_VM(0); break;
  }
}
#define SB __builtin_amdgcn_sched_barrier(0)
DI void bar() { __builtin_amdgcn_s_barrier(); __builtin_amdgcn_sched_barrier(0); }

DI bf16x8 ldB(const char* smem_lane_base, int off0, int off1) {
  s16x4 lo = __builtin_amdgcn_ds_read_tr16_b64_v4i16((lds_s16x4_ptr)(smem_lane_base + off0));
  s16x4 hi = __builtin_amdgcn_ds_read_tr16_b64_v4i16((lds_s16x4_ptr)(smem_lane_base + off1));
  typedef __attribute__((ext_vector_type(8))) short s16x8;
  s16x8 v = __builtin_shufflevector(lo, hi, 0, 1, 2, 3, 4, 5, 6, 7);
  return __builtin_bit_cast(bf16x8, v);
}

DI float phi(float t) {
  float r = t > 0.f ? bfr(t + 1.0f) : bfr(__expf(t));
  return bfr(r + 1e-6f);
}

constexpr int K1_RING = 65536 + 7 * 12288;
constexpr int K1_LDS = K1_RING + 4096;
constexpr int k1_gcnt(int n, int lo) { return n > 15 ? 0 : ((lo ? 2 : 1) + ((n & 1) == 0 ? 1 : 0)); }
constexpr int k1_younger(int n, int lo) {
  return k1_gcnt(n + 1, lo) + k1_gcnt(n + 2, lo) + k1_gcnt(n + 3, lo) + k1_gcnt(n + 4, lo);
}
__global__ __launch_bounds__(512, 2) void k1_qkv(const __bf16* x, const __bf16* w, __bf16* pq, float* kvpart) {
  __shared__ __attribute__((aligned(16))) char smem[K1_LDS];
  STAMP_DECL; STAMP(0);
  const int tid = threadIdx.x, lane = tid & 63, wv = __builtin_amdgcn_readfirstlane(tid >> 6);
  const int g = lane >> 4, c = lane & 15;
  const int hw = wv & 3, rh = wv >> 2;
  const int b = blockIdx.x, xcd = b & 7, idx = b >> 3;
  const int mt = (xcd >> 1) * 8 + (idx >> 2), hq = (xcd & 1) * 4 + (idx & 3);
  const unsigned lds0 = (unsigned)(size_t)smem;

  const __bf16* xsrc = x + (size_t)(mt * 64 + wv * 8) * D_;
  const __bf16* wsrc0 = w + (size_t)((wv & 3) * 8) * 1536 + (wv >> 2) * 512 + hq * 64;
  const __bf16* wsrc1 = w + (size_t)((wv & 3) * 8) * 1536 + 1024 + hq * 64;
  unsigned xoff, woff;
  {
    const int r = lane >> 3, pos = lane & 7;
    const int row = wv * 8 + r;
    xoff = r * (D_ * 2) + (pos ^ ((row >> 1) & 7)) * 16;
    const int swz = ((r >> 1) & 1) | ((wv & 1) << 1);
    woff = r * 3072 + ((pos >> 1) ^ swz) * 32 + (pos & 1) * 16;
  }
  auto issue1 = [&](int n, int e) {
    const unsigned slot = lds0 + 65536 + (n % 7) * 12288;
    if (e == 0) glds16s(wsrc0 + (size_t)n * 32 * 1536, woff, slot + wv * 1024);
    if (e == 1) { if (wv < 4) glds16s(wsrc1 + (size_t)n * 32 * 1536, woff, slot + 8192 + wv * 1024); }
    if (e == 2) { if ((n & 1) == 0) glds16s(xsrc + (n >> 1) * 64, xoff, lds0 + (n >> 1) * 8192 + wv * 1024); }
  };
#pragma unroll
  for (int n = 0; n < 6; ++n) { issue1(n, 0); issue1(n, 1); issue1(n, 2); }
  STAMP(1);

  f32x4 acc[2][3];
#pragma unroll
  for (int i = 0; i < 2; ++i)
#pragma unroll
    for (int j = 0; j < 3; ++j) acc[i][j] = (f32x4){0.f, 0.f, 0.f, 0.f};
  f32x4 stt = {0.f, 0.f, 0.f, 0.f};
  f32x4 csum[3];
#pragma unroll
  for (int j = 0; j < 3; ++j) csum[j] = (f32x4){0.f, 0.f, 0.f, 0.f};
  bf16x8 ones;
#pragma unroll
  for (int j = 0; j < 8; ++j) ones[j] = (__bf16)1.0f;

  const int aoff = (rh * 32 + c) * 128 + ((g ^ (c >> 1)) * 16);
  const int boff = 65536 + g * 1024 + (c >> 2) * 128 + ((hw ^ (((c >> 3) & 1) | ((g & 1) << 1))) * 32) + (c & 3) * 8;
  bf16x8 fa[2][2], fb[2][3];
  auto rdA = [&](int n, int bu) {
    const char* sl = smem + (n >> 1) * 8192 + (aoff ^ ((n & 1) << 6));
    fa[bu][0] = *(const bf16x8*)(sl);
    fa[bu][1] = *(const bf16x8*)(sl + 2048);
  };
  auto rdB = [&](int n, int bu, int p) { fb[bu][p] = ldB(smem + boff + (n % 7) * 12288 + p * 4096, 0, 512); };
  if (wv < 4) wait_vm(k1_gcnt(5, 1) + k1_younger(0, 1)); else wait_vm(k1_gcnt(5, 0) + k1_younger(0, 0));
  bar();
  STAMP(8);
  rdA(0, 0); rdB(0, 0, 0); rdB(0, 0, 1); rdB(0, 0, 2);
#pragma unroll
  for (int n = 0; n < 16; ++n) {
    const int cu = n & 1, nx = cu ^ 1;
    if (n < 15) {
      if (wv < 4) wait_vm(k1_younger(n + 1, 1)); else wait_vm(k1_younger(n + 1, 0));
      bar();
    }
    acc[0][0] = MFMA(fb[cu][0], fa[cu][0], acc[0][0]); SB;
    if (n < 15) rdA(n + 1, nx);
    SB;
    acc[1][0] = MFMA(fb[cu][0], fa[cu][1], acc[1][0]); SB;
    if (n < 15) rdB(n + 1, nx, 0);
    SB;
    acc[0][1] = MFMA(fa[cu][0], fb[cu][1], acc[0][1]); SB;
    if (n < 15) rdB(n + 1, nx, 1);
    SB;
    acc[1][1] = MFMA(fa[cu][1], fb[cu][1], acc[1][1]); SB;
    if (n < 15) rdB(n + 1, nx, 2);
    SB;
    acc[0][2] = MFMA(fa[cu][0], fb[cu][2], acc[0][2]); SB;
    if (n + 6 < 16) issue1(n + 6, 0);
    SB;
    acc[1][2] = MFMA(fa[cu][1], fb[cu][2], acc[1][2]); SB;
    if (n + 6 < 16) issue1(n + 6, 1);
    SB;
    if (hw == 0) stt = MFMA(ones, fa[cu][0], stt);
    else if (hw == 1) stt = MFMA(fa[cu][0], fa[cu][0], stt);
    else if (hw == 2) stt = MFMA(ones, fa[cu][1], stt);
    else stt = MFMA(fa[cu][1], fa[cu][1], stt);
    SB;
    if (n + 6 < 16) issue1(n + 6, 2);
    SB;
    if (cu == rh) {
      csum[0] = MFMA(ones, fb[cu][0], csum[0]);
      csum[1] = MFMA(ones, fb[cu][1], csum[1]);
      csum[2] = MFMA(ones, fb[cu][2], csum[2]);
    }
  }
  STAMP(6);
  float* tsum = (float*)(smem + K1_RING);
  float* tsq = tsum + 64;
  float* tcs = tsum + 128;
  float* tst = tsum + 512;
  {
    const int row = rh * 32 + (hw >> 1) * 16 + c;
    if ((hw & 1) == 0) { if (g == 0) tsum[row] = stt[0]; }
    else if (g == (c >> 2)) tsq[row] = (c & 2) ? ((c & 1) ? stt[3] : stt[2]) : ((c & 1) ? stt[1] : stt[0]);
    if (g == 0) {
#pragma unroll
      for (int p = 0; p < 3; ++p) tcs[rh * 192 + p * 64 + hw * 16 + c] = csum[p][0];
    }
  }
  __syncthreads();
  if (tid < 64) {
    const float mean = tsum[tid] * (1.0f / 512.0f);
    const float var = fmaxf(tsq[tid] * (1.0f / 512.0f) - mean * mean, 0.f);
    const float sb = bfr(sqrtf(bfr(bfr(var) + 1e-5f)));
    *(f32x2*)(tst + tid * 2) = (f32x2){bfr(mean), 1.0f / sb};
  }
  __syncthreads();
  const f32x4 csq = *(const f32x4*)(tcs + hw * 16 + 4 * g) + *(const f32x4*)(tcs + 192 + hw * 16 + 4 * g);
  const float csk = tcs[64 + hw * 16 + c] + tcs[192 + 64 + hw * 16 + c];
  const float csv = tcs[128 + hw * 16 + c] + tcs[192 + 128 + hw * 16 + c];
#pragma unroll
  for (int i = 0; i < 2; ++i) {
    const f32x2 st = *(const f32x2*)(tst + (rh * 32 + i * 16 + c) * 2);
    bf16x4 o;
#pragma unroll
    for (int r = 0; r < 4; ++r) o[r] = (__bf16)phi(bfr(st[1] * (acc[i][0][r] - st[0] * csq[r])));
    const int rt = mt * 4 + rh * 2 + i;
    *(bf16x4*)(pq + ((size_t)(rt * 16 + hq * 2 + (hw >> 1)) * 64 + lane) * 8 + (hw & 1) * 4) = o;
  }
  STAMP(7);
  {
    bf16x8 pk, vv;
#pragma unroll
    for (int i = 0; i < 2; ++i)
#pragma unroll
      for (int r = 0; r < 4; r += 2) {
        const f32x4 st = *(const f32x4*)(tst + (rh * 32 + i * 16 + 4 * g + r) * 2);
        pk[i * 4 + r] = (__bf16)phi(bfr(st[1] * (acc[i][1][r] - st[0] * csk)));
        pk[i * 4 + r + 1] = (__bf16)phi(bfr(st[3] * (acc[i][1][r + 1] - st[2] * csk)));
        vv[i * 4 + r] = (__bf16)(st[1] * (acc[i][2][r] - st[0] * csv));
        vv[i * 4 + r + 1] = (__bf16)(st[3] * (acc[i][2][r + 1] - st[2] * csv));
      }
    const f32x4 z4 = {0.f, 0.f, 0.f, 0.f};
    f32x4 kv = MFMA(pk, vv, z4);
    f32x4 zz = MFMA(pk, ones, z4);
    float* dst = kvpart + (size_t)((mt * 2 + rh) * 32 + hq * 4 + hw) * KVREC;
    *(f32x4*)(dst + lane * 4) = kv;
    if (c == 0) *(f32x4*)(dst + 256 + g * 4) = zz;
  }
  STAMP(2); STAMP_OUT(0, pq);
}

__global__ __launch_bounds__(256) void k1b_reduce(const float* kvpart, __bf16* kvf, const __bf16* wo, const __bf16* wg,
                                                  const __bf16* wu, const __bf16* wd, float* csp) {
  __shared__ __attribute__((aligned(16))) float red[32 * 64];
  STAMP_DECL; STAMP(0);
  const int tid = threadIdx.x, b = blockIdx.x, lane = tid & 63, wv = __builtin_amdgcn_readfirstlane(tid >> 6);
  if (b < 34) {
    const int q = b * 64 + lane;
    f32x4 v[16];
#pragma unroll
    for (int m = 0; m < 16; ++m) v[m] = *(const f32x4*)(kvpart + (size_t)(wv * 16 + m) * 32 * KVREC + q * 4);
    f32x4 s = v[0];
#pragma unroll
    for (int m = 1; m < 16; ++m) s += v[m];
    f32x4* r4 = (f32x4*)red;
    r4[tid] = s;
    __syncthreads();
    if (tid < 64) {
      f32x4 t = (r4[tid] + r4[64 + tid]) + (r4[128 + tid] + r4[192 + tid]);
      bf16x4 o;
#pragma unroll
      for (int j = 0; j < 4; ++j) o[j] = (__bf16)t[j];
      *(bf16x4*)(kvf + q * 4) = o;
    }
    STAMP(1); STAMP_OUT(1, (const char*)kvpart - (10u << 20));
    return;
  }
  if (b >= 77) {
    const int pb = b - 77;
    const u32x4* src = pb < 32 ? (const u32x4*)wo + (size_t)pb * 1024 : (const u32x4*)wd + (size_t)(pb - 32) * 1024;
    u32x4 v0 = src[tid], v1 = src[256 + tid], v2 = src[512 + tid], v3 = src[768 + tid];
    asm volatile("" :: "v"(v0), "v"(v1), "v"(v2), "v"(v3));
    return;
  }
  const int cb = b - 34;
  const int col0 = cb * 64 + (tid & 7) * 8;
  const __bf16* src = col0 < F_ ? (wg + col0) : (wu + (col0 - F_));
  const int kq = tid >> 3;
  bf16x8 v[16];
#pragma unroll
  for (int r = 0; r < 16; ++r) v[r] = *(const bf16x8*)(src + (size_t)(kq * 16 + r) * F_);
  float s[8];
#pragma unroll
  for (int j = 0; j < 8; ++j) s[j] = 0.f;
#pragma unroll
  for (int r = 0; r < 16; ++r)
#pragma unroll
    for (int j = 0; j < 8; ++j) s[j] += (float)v[r][j];
#pragma unroll
  for (int j = 0; j < 8; ++j) red[kq * 64 + (tid & 7) * 8 + j] = s[j];
  __syncthreads();
  if (tid < 64) {
    float t = 0.f;
#pragma unroll
    for (int q = 0; q < 32; ++q) t += red[q * 64 + tid];
    csp[cb * 64 + tid] = t;
  }
  STAMP(2); STAMP_OUT(1, (const char*)kvpart - (10u << 20));
}

constexpr int K2_LDS = 64 * 1024 + 17408;
__global__ __launch_bounds__(512, 2) void k2_attn_out(const __bf16* x, const __bf16* wo, const __bf16* pq,
                                                      const __bf16* kvf, __bf16* hfr, float* hstat) {
  __shared__ __attribute__((aligned(16))) char smem[K2_LDS];
  STAMP_DECL; STAMP(0);
  const int tid = threadIdx.x, lane = tid & 63, wv = __builtin_amdgcn_readfirstlane(tid >> 6);
  const int g = lane >> 4, c = lane & 15;
  const int rg = wv & 3, kh = wv >> 2;
  const int b = blockIdx.x, xcd = b & 7, idx = b >> 3;
  const int mt = xcd * 4 + (idx >> 3), nt = idx & 7;
  const unsigned lds0 = (unsigned)(size_t)smem;
  const int rt = mt * 4 + rg;

  bf16x4 xr[2];
#pragma unroll
  for (int p = 0; p < 2; ++p) xr[p] = *(const bf16x4*)(x + (size_t)(rt * 16 + c) * D_ + nt * 64 + (kh * 2 + p) * 16 + g * 4);
  bf16x8 pf[8];
#pragma unroll
  for (int h = 0; h < 8; ++h) pf[h] = *(const bf16x8*)(pq + ((size_t)(rt * 16 + kh * 8 + h) * 64 + lane) * 8);
#pragma unroll
  for (int i = 0; i < 3; ++i) {
    const int ins = i * 8 + wv;
    if (ins < 17) glds16s((const char*)kvf + ins * 1024, lane * 16, lds0 + 65536 + ins * 1024);
  }
  {
    const int r = lane >> 3, pos = lane & 7;
    const unsigned woff = r * (D_ * 2) + (((pos >> 1) ^ ((r >> 1) & 3)) * 32) + (pos & 1) * 16;
#pragma unroll
    for (int i = 0; i < 8; ++i) {
      const int kb = i * 8 + wv;
      glds16s(wo + (size_t)kb * 8 * D_ + nt * 64, woff, lds0 + kb * 1024);
    }
  }
  f32x4 acc[4];
#pragma unroll
  for (int j = 0; j < 4; ++j) acc[j] = (f32x4){0.f, 0.f, 0.f, 0.f};
  STAMP(1);
  WAIT_VM(0);
  bar();
  STAMP(2);
  const int brow = (g & 1) * 4 + (c >> 2);
  const char* bl = smem + kh * 32768 + (g >> 1) * 1024 + brow * 128 + (c & 3) * 8;
  const int bsw = (brow >> 1) & 3;
  const char* kvl = smem + 65536 + kh * 16 * (KVREC * 2);
  const f32x4 z4 = {0.f, 0.f, 0.f, 0.f};
#pragma unroll
  for (int h = 0; h < 8; ++h) {
    bf16x4 k0 = *(const bf16x4*)(kvl + (2 * h) * (KVREC * 2) + lane * 8);
    bf16x4 k1 = *(const bf16x4*)(kvl + (2 * h + 1) * (KVREC * 2) + lane * 8);
    bf16x4 z0 = *(const bf16x4*)(kvl + (2 * h) * (KVREC * 2) + 512 + g * 8);
    bf16x4 z1 = *(const bf16x4*)(kvl + (2 * h + 1) * (KVREC * 2) + 512 + g * 8);
    const bf16x4 zz = {(__bf16)0.f, (__bf16)0.f, (__bf16)0.f, (__bf16)0.f};
    bf16x8 a0 = __builtin_shufflevector(k0, zz, 0, 1, 2, 3, 4, 5, 6, 7);
    bf16x8 a1 = __builtin_shufflevector(zz, k1, 0, 1, 2, 3, 4, 5, 6, 7);
    bf16x8 d0 = __builtin_shufflevector(z0, zz, 0, 1, 2, 3, 4, 5, 6, 7);
    bf16x8 d1 = __builtin_shufflevector(zz, z1, 0, 1, 2, 3, 4, 5, 6, 7);
    f32x4 n0 = MFMA(a0, pf[h], z4);
    f32x4 n1 = MFMA(a1, pf[h], z4);
    f32x4 e0 = MFMA(d0, pf[h], z4);
    f32x4 e1 = MFMA(d1, pf[h], z4);
    const float r0 = __builtin_amdgcn_rcpf(bfr(bfr(e0[0]) + 1e-6f));
    const float r1 = __builtin_amdgcn_rcpf(bfr(bfr(e1[0]) + 1e-6f));
    bf16x8 of;
#pragma unroll
    for (int r = 0; r < 4; ++r) {
      of[r] = (__bf16)(bfr(n0[r]) * r0);
      of[4 + r] = (__bf16)(bfr(n1[r]) * r1);
    }
#pragma unroll
    for (int ct = 0; ct < 4; ++ct) {
      bf16x8 wf = ldB(bl + ((ct ^ bsw) * 32), h * 4096, h * 4096 + 2048);
      acc[ct] = MFMA(wf, of, acc[ct]);
    }
  }
  STAMP(3);
  __syncthreads();
  f32x4* red = (f32x4*)smem;
  red[((rg * 2 + kh) * 2 + 0) * 64 + lane] = kh ? acc[0] : acc[2];
  red[((rg * 2 + kh) * 2 + 1) * 64 + lane] = kh ? acc[1] : acc[3];
  __syncthreads();
  f32x4 f0 = (kh ? acc[2] : acc[0]), f1 = (kh ? acc[3] : acc[1]);
  {
    f32x4 o0 = red[((rg * 2 + (kh ^ 1)) * 2 + 0) * 64 + lane], o1 = red[((rg * 2 + (kh ^ 1)) * 2 + 1) * 64 + lane];
    f0 = kh ? (o0 + f0) : (f0 + o0);
    f1 = kh ? (o1 + f1) : (f1 + o1);
  }
  float hv[8];
  float s = 0.f;
#pragma unroll
  for (int r = 0; r < 4; ++r) {
    hv[r] = bfr((float)xr[0][r] + bfr(f0[r]));
    hv[4 + r] = bfr((float)xr[1][r] + bfr(f1[r]));
    s += hv[r] + hv[4 + r];
  }
  {
    bf16x8 o;
#pragma unroll
    for (int r = 0; r < 8; ++r) o[r] = (__bf16)hv[r];
    *(bf16x8*)(hfr + ((size_t)(rt * 16 + nt * 2 + kh) * 64 + lane) * 8) = o;
  }
  s += __shfl_xor(s, 16, 64);
  s += __shfl_xor(s, 32, 64);
  const float mean = s * (1.0f / 32.0f);
  float m2 = 0.f;
#pragma unroll
  for (int r = 0; r < 8; ++r) { float d = hv[r] - mean; m2 += d * d; }
  m2 += __shfl_xor(m2, 16, 64);
  m2 += __shfl_xor(m2, 32, 64);
  if (g == 0) *(f32x2*)(hstat + ((size_t)(rt * 16 + c) * 16 + nt * 2 + kh) * 2) = (f32x2){s, m2};
  STAMP(4); STAMP_OUT(2, pq);
}

constexpr int K3_RING = 7 * 20480;
constexpr int K3_LDS = K3_RING + 16384;
__global__ __launch_bounds__(512, 2) void k3_gate_up(const __bf16* hfr, const float* hstat, const __bf16* wg,
                                                     const __bf16* wu, const float* csp, __bf16* hh) {
  __shared__ __attribute__((aligned(16))) char smem[K3_LDS];
  STAMP_DECL; STAMP(0);
  const int tid = threadIdx.x, lane = tid & 63, wv = __builtin_amdgcn_readfirstlane(tid >> 6);
  const int g = lane >> 4, c = lane & 15;
  const int mw = wv & 3, nw = wv >> 2;
  const int b = blockIdx.x, xcd = b & 7, idx = b >> 3;
  const int mt = (xcd >> 2) * 8 + (idx >> 2), fgp = (xcd & 3) * 4 + (idx & 3);
  const int ft0 = 2 * ((fgp * 43) >> 4);
  const unsigned lds0 = (unsigned)(size_t)smem;
  const int rt0 = mt * 8 + mw * 2;

  const char* pA = (const char*)hfr + (size_t)(mt * 8 + wv) * 16384;
  const unsigned boffl = (lane >> 2) * (F_ * 2) + ((((lane >> 1) & 1) ^ ((lane >> 4) & 1)) * 32) + (lane & 1) * 16;
  const int q0 = wv, q1 = 8 + (wv & 3);
  const __bf16* pB0 = (q0 < 6 ? wg : wu) + (size_t)((q0 & 1) * 16) * F_ + (ft0 + ((q0 % 6) >> 1) * 2) * 16;
  const __bf16* pB1 = wu + (size_t)((q1 & 1) * 16) * F_ + (ft0 + ((q1 - 6) >> 1) * 2) * 16;
  auto issue1 = [&](int n, int e) {
    const unsigned slot = lds0 + (n % 7) * 20480;
    if (e == 0) glds16s(pA + n * 1024, lane * 16, slot + wv * 1024);
    if (e == 1) glds16s(pB0 + (size_t)n * 32 * F_, boffl, slot + 8192 + wv * 1024);
    if (e == 2) { if (wv < 4) glds16s(pB1 + (size_t)n * 32 * F_, boffl, slot + 16384 + wv * 1024); }
  };
  auto issue = [&](int n) { issue1(n, 0); issue1(n, 1); issue1(n, 2); };
  issue(0); issue(1);
  glds16s((const char*)hstat + (size_t)mt * 16384 + wv * 2048, lane * 16, lds0 + K3_RING + wv * 2048);
  glds16s((const char*)hstat + (size_t)mt * 16384 + wv * 2048 + 1024, lane * 16, lds0 + K3_RING + wv * 2048 + 1024);
#pragma unroll
  for (int n = 2; n < 6; ++n) issue(n);
  STAMP(1);

  f32x4 acc[2][6];
#pragma unroll
  for (int i = 0; i < 2; ++i)
#pragma unroll
    for (int j = 0; j < 6; ++j) acc[i][j] = (f32x4){0.f, 0.f, 0.f, 0.f};
  bf16x8 fa[2][2], fb[2][6];
  const int bl0 = 8192 + (4 * g + (c >> 2)) * 64 + (c & 3) * 8;
  auto rdA = [&](int n, int bu) {
    const char* sl = smem + (n % 7) * 20480;
    fa[bu][0] = *(const bf16x8*)(sl + lane * 16 + (mw * 2) * 1024);
    fa[bu][1] = *(const bf16x8*)(sl + lane * 16 + (mw * 2 + 1) * 1024);
  };
  auto rdB = [&](int n, int bu, int ct) {
    const char* sl = smem + (n % 7) * 20480;
    const int ftl = nw * 3 + (ct >> 1);
    const char* bp = sl + bl0 + ((ct & 1) * 6 + (ftl >> 1) * 2) * 1024 + (((ftl & 1) ^ (g & 1)) * 32);
    fb[bu][ct] = ldB(bp, 0, 1024);
  };
  auto rd = [&](int n, int bu) {
    rdA(n, bu);
#pragma unroll
    for (int ct = 0; ct < 6; ++ct) rdB(n, bu, ct);
  };
  if (wv < 4) WAIT_VM(17); else WAIT_VM(12);
  bar();
  STAMP(2);
  rd(0, 0);
  float mb[2], rs[2];
  f32x4 cs[6];
#pragma unroll
  for (int n = 0; n < 16; ++n) {
    if (n < 15) {
      const int y = (n + 5 < 15 ? n + 5 : 15) - (n + 1);
      if (wv < 4) wait_vm(3 * y + (n == 0 ? 2 : 0)); else wait_vm(2 * y + (n == 0 ? 2 : 0));
      bar();
    }
    if (n == 2) {
#pragma unroll
      for (int i = 0; i < 2; ++i) {
        const char* sp = smem + K3_RING + ((mw * 2 + i) * 16 + c) * 128 + g * 32;
        const f32x4 s0 = *(const f32x4*)(sp), s1 = *(const f32x4*)(sp + 16);
        float s = (s0[0] + s0[2]) + (s1[0] + s1[2]);
        s += __shfl_xor(s, 16, 64);
        s += __shfl_xor(s, 32, 64);
        const float mean = s * (1.0f / 512.0f);
        const float d0 = s0[0] * (1.0f / 32.0f) - mean, d1 = s0[2] * (1.0f / 32.0f) - mean;
        const float d2 = s1[0] * (1.0f / 32.0f) - mean, d3 = s1[2] * (1.0f / 32.0f) - mean;
        float m2 = (s0[1] + s0[3]) + (s1[1] + s1[3]) + 32.0f * ((d0 * d0 + d1 * d1) + (d2 * d2 + d3 * d3));
        m2 += __shfl_xor(m2, 16, 64);
        m2 += __shfl_xor(m2, 32, 64);
        mb[i] = bfr(mean);
        rs[i] = 1.0f / bfr(sqrtf(bfr(bfr(m2 * (1.0f / 512.0f)) + 1e-5f)));
      }
    }
    if (n == 10) {
#pragma unroll
      for (int ct = 0; ct < 6; ++ct)
        cs[ct] = *(const f32x4*)(csp + (ct & 1) * F_ + (ft0 + nw * 3 + (ct >> 1)) * 16 + g * 4);
    }
#pragma unroll
    for (int ct = 0; ct < 6; ++ct) {
      acc[0][ct] = MFMA(fb[n & 1][ct], fa[n & 1][0], acc[0][ct]);
      SB;
      if (n < 15) { if (ct == 0) rdA(n + 1, (n + 1) & 1); rdB(n + 1, (n + 1) & 1, ct); }
      SB;
      acc[1][ct] = MFMA(fb[n & 1][ct], fa[n & 1][1], acc[1][ct]);
      SB;
      if (n + 6 < 16 && ct >= 1 && ct <= 3) issue1(n + 6, ct - 1);
      SB;
    }
  }
  STAMP(6);
#pragma unroll
  for (int i = 0; i < 2; ++i) {
#pragma unroll
    for (int j = 0; j < 3; ++j) {
      const int ft = ft0 + nw * 3 + j;
      bf16x4 o;
#pragma unroll
      for (int r = 0; r < 4; ++r) {
        float gg = bfr(rs[i] * (acc[i][2 * j][r] - mb[i] * cs[2 * j][r]));
        float uu = bfr(rs[i] * (acc[i][2 * j + 1][r] - mb[i] * cs[2 * j + 1][r]));
        o[r] = (__bf16)(gg * uu);
      }
      *(bf16x4*)(hh + ((size_t)((rt0 + i) * 43 + (ft >> 1)) * 64 + lane) * 8 + (ft & 1) * 4) = o;
    }
  }
  STAMP(7); STAMP_OUT(3, (const char*)hfr - (2u << 20));
}

constexpr int K4_LDS = 160 * 1024;
__global__ __launch_bounds__(512, 2) void k4_down(const __bf16* hh, const __bf16* wd, const __bf16* hfr, __bf16* out) {
  __shared__ __attribute__((aligned(16))) char smem[K4_LDS];
  STAMP_DECL; STAMP(0);
  const int tid = threadIdx.x, lane = tid & 63, wv = __builtin_amdgcn_readfirstlane(tid >> 6);
  const int g = lane >> 4, c = lane & 15;
  const int b = blockIdx.x, xcd = b & 7, idx = b >> 3;
  const int mt = xcd * 4 + (idx >> 3), nt = idx & 7;
  const int mi = wv & 1, kq = wv >> 1;
  const unsigned lds0 = (unsigned)(size_t)smem;
  const int rt0 = mt * 4 + mi * 2;
  const int u0 = kq * 11;
  const int nu = kq == 3 ? 10 : 11;
  const int fi = kq >> 1, fc = (kq & 1) * 2;
  bf16x8 hres = *(const bf16x8*)(hfr + ((size_t)((rt0 + fi) * 16 + nt * 2 + (kq & 1)) * 64 + lane) * 8);

  const __bf16* wsrc = wd + (size_t)(u0 * 32 + mi * 16) * D_ + nt * 64;
  const unsigned woff = (lane >> 3) * (D_ * 2) + ((((lane & 7) >> 1) ^ ((lane >> 4) & 3)) * 32) + (lane & 1) * 16;
  const unsigned ldsq = lds0 + kq * 40960 + mi * 2048;
  bf16x8 af[2][11];
  const __bf16* asrc = hh + ((size_t)(rt0 * 43 + u0) * 64 + lane) * 8;
#pragma unroll
  for (int s = 0; s < 10; ++s) {
    glds16s(wsrc + (size_t)(s * 32) * D_, woff, ldsq + s * 4096);
    glds16s(wsrc + (size_t)(s * 32 + 8) * D_, woff, ldsq + s * 4096 + 1024);
    af[0][s] = *(const bf16x8*)(asrc + (size_t)s * 512);
    af[1][s] = *(const bf16x8*)(asrc + (size_t)(43 + s) * 512);
  }
  {
    const int s = kq == 3 ? 9 : 10;
    af[0][10] = *(const bf16x8*)(asrc + (size_t)s * 512);
    af[1][10] = *(const bf16x8*)(asrc + (size_t)(43 + s) * 512);
  }
  f32x4 acc[2][4];
#pragma unroll
  for (int i = 0; i < 2; ++i)
#pragma unroll
    for (int j = 0; j < 4; ++j) acc[i][j] = (f32x4){0.f, 0.f, 0.f, 0.f};
  const int brow = (g & 1) * 4 + (c >> 2);
  const char* bl = smem + kq * 40960 + (g >> 1) * 1024 + brow * 128 + (c & 3) * 8;
  const int bsw = (brow >> 1) & 3;

  STAMP(1);
  wait_vm(4 * 8 + 2);
  bar();
  STAMP(2);
#pragma unroll
  for (int s = 0; s < 2; ++s) {
#pragma unroll
    for (int ct = 0; ct < 4; ++ct) {
      bf16x8 bf = ldB(bl + ((ct ^ bsw) * 32), s * 4096, s * 4096 + 2048);
      acc[0][ct] = MFMA(bf, af[0][s], acc[0][ct]);
      acc[1][ct] = MFMA(bf, af[1][s], acc[1][ct]);
    }
  }
  wait_vm(4 * 4 + 2);
  bar();
  STAMP(3);
  if (kq < 3) {
    glds16s(wsrc + (size_t)(10 * 32) * D_, woff, ldsq);
    glds16s(wsrc + (size_t)(10 * 32 + 8) * D_, woff, ldsq + 1024);
  }
#pragma unroll
  for (int s = 2; s < 6; ++s) {
#pragma unroll
    for (int ct = 0; ct < 4; ++ct) {
      bf16x8 bf = ldB(bl + ((ct ^ bsw) * 32), s * 4096, s * 4096 + 2048);
      acc[0][ct] = MFMA(bf, af[0][s], acc[0][ct]);
      acc[1][ct] = MFMA(bf, af[1][s], acc[1][ct]);
    }
  }
  WAIT_VM(0);
  bar();
  STAMP(4);
#pragma unroll
  for (int s = 6; s < 10; ++s) {
#pragma unroll
    for (int ct = 0; ct < 4; ++ct) {
      bf16x8 bf = ldB(bl + ((ct ^ bsw) * 32), s * 4096, s * 4096 + 2048);
      acc[0][ct] = MFMA(bf, af[0][s], acc[0][ct]);
      acc[1][ct] = MFMA(bf, af[1][s], acc[1][ct]);
    }
  }
  if (nu == 11) {
#pragma unroll
    for (int ct = 0; ct < 4; ++ct) {
      bf16x8 bf = ldB(bl + ((ct ^ bsw) * 32), 0, 2048);
      acc[0][ct] = MFMA(bf, af[0][10], acc[0][ct]);
      acc[1][ct] = MFMA(bf, af[1][10], acc[1][ct]);
    }
  }
  STAMP(5);
  __syncthreads();
  f32x4* red = (f32x4*)smem;
#pragma unroll
  for (int i = 0; i < 2; ++i)
#pragma unroll
    for (int ct = 0; ct < 4; ++ct) red[(((mi * 4 + kq) * 2 + i) * 4 + ct) * 64 + lane] = acc[i][ct];
  __syncthreads();
#pragma unroll
  for (int p = 0; p < 2; ++p) {
    const int ct = fc + p;
    f32x4 s = (red[(((mi * 4 + 0) * 2 + fi) * 4 + ct) * 64 + lane] + red[(((mi * 4 + 1) * 2 + fi) * 4 + ct) * 64 + lane]) +
              (red[(((mi * 4 + 2) * 2 + fi) * 4 + ct) * 64 + lane] + red[(((mi * 4 + 3) * 2 + fi) * 4 + ct) * 64 + lane]);
    bf16x4 o;
#pragma unroll
    for (int r = 0; r < 4; ++r) o[r] = (__bf16)((float)hres[p * 4 + r] + bfr(s[r]));
    *(bf16x4*)(out + (size_t)((rt0 + fi) * 16 + c) * D_ + nt * 64 + ct * 16 + g * 4) = o;
  }
  STAMP(6); STAMP_OUT(4, (const char*)hfr - (2u << 20));
}

extern "C" void kernel_launch(void* const* d_in, const int* in_sizes, int n_in, void* d_out, int out_size,
                              void* d_ws, size_t ws_size, hipStream_t stream) {
  const __bf16* x = (const __bf16*)d_in[0];
  const __bf16* wqkv = (const __bf16*)d_in[1];
  const __bf16* wo = (const __bf16*)d_in[2];
  const __bf16* wg = (const __bf16*)d_in[4];
  const __bf16* wu = (const __bf16*)d_in[5];
  const __bf16* wd = (const __bf16*)d_in[6];
  char* ws = (char*)d_ws;
  __bf16* pq = (__bf16*)(ws);
  __bf16* hfr = (__bf16*)(ws + (2u << 20));
  __bf16* hh = (__bf16*)(ws + (4u << 20));
  float* kvpart = (float*)(ws + (10u << 20));
  __bf16* kvf = (__bf16*)(ws + (13u << 20));
  float* csp = (float*)(ws + (13u << 20) + 65536);
  float* hstat = (float*)(ws + (14u << 20));
  k1_qkv<<<256, 512, 0, stream>>>(x, wqkv, pq, kvpart);
  k1b_reduce<<<77 + 118, 256, 0, stream>>>(kvpart, kvf, wo, wg, wu, wd, csp);
  k2_attn_out<<<256, 512, 0, stream>>>(x, wo, pq, kvf, hfr, hstat);
  k3_gate_up<<<256, 512, 0, stream>>>(hfr, hstat, wg, wu, csp, hh);
  k4_down<<<256, 512, 0, stream>>>(hh, wd, hfr, (__bf16*)d_out);
}
```

```cpp
#include <hip/hip_runtime.h>
#include <hip/hip_bf16.h>
#include <stdint.h>

typedef __attribute__((ext_vector_type(8))) __bf16 bf16x8;
typedef __attribute__((ext_vector_type(4))) __bf16 bf16x4;
typedef __attribute__((ext_vector_type(4))) short s16x4;
typedef __attribute__((ext_vector_type(4))) float f32x4;
typedef __attribute__((ext_vector_type(2))) float f32x2;
typedef __attribute__((ext_vector_type(4))) unsigned u32x4;
typedef __attribute__((ext_vector_type(2))) unsigned u32x2;
typedef __attribute__((address_space(3))) s16x4* lds_s16x4_ptr;

#define DI __device__ __forceinline__
#define MFMA(a, b, c) __builtin_amdgcn_mfma_f32_16x16x32_bf16((a), (b), (c), 0, 0, 0)

constexpr int S_ = 2048, D_ = 512, F_ = 1376;
constexpr int KVREC = 272;

#define STAMP_DECL
#define STAMP(i)
#define STAMP_OUT(k, wsbase)
DI float bfr(float x) { return (float)(__bf16)x; }
DI float bf2f(__bf16 x) { return (float)x; }

DI void glds16(const void* gsrc, unsigned lds_dst) {
  unsigned keep;
  asm volatile(
      "s_mov_b32 %0, m0\n\ts_mov_b32 m0, %2\n\ts_nop 0\n\tglobal_load_lds_dwordx4 %1, off\n\ts_mov_b32 m0, %0"
      : "=&s"(keep)
      : "v"(gsrc), "s"(lds_dst)
      : "memory");
}
DI void glds16s(const void* ubase, unsigned voff, unsigned lds_dst) {
  unsigned keep;
  asm volatile(
      "s_mov_b32 %0, m0\n\ts_mov_b32 m0, %3\n\ts_nop 0\n\tglobal_load_lds_dwordx4 %1, %2\n\ts_mov_b32 m0, %0"
      : "=&s"(keep)
      : "v"(voff), "s"(ubase), "s"(lds_dst)
      : "memory");
}
DI void gload16(bf16x8& dst, const void* ubase, unsigned voff) {
  asm volatile("global_load_dwordx4 %0, %1, %2" : "=v"(dst) : "v"(voff), "s"(ubase) : "memory");
}
#define TIE(x) asm volatile("" : "+v"(x))
#define WAIT_VM(n) do { __builtin_amdgcn_sched_barrier(0); asm volatile("s_waitcnt vmcnt(" #n ") lgkmcnt(0)" ::: "memory"); } while (0)
DI void wait_vm(int n) {
  switch (n) {
    case 0: WAIT_VM(0); break;   case 1: WAIT_VM(1); break;   case 2: WAIT_VM(2); break;
    case 3: WAIT_VM(3); break;   case 4: WAIT_VM(4); break;   case 5: WAIT_VM(5); break;
    case 6: WAIT_VM(6); break;   case 7: WAIT_VM(7); break;   case 8: WAIT_VM(8); break;
    case 9: WAIT_VM(9); break;   case 10: WAIT_VM(10); break; case 11: WAIT_VM(11); break;
    case 12: WAIT_VM(12); break; case 13: WAIT_VM(13); break; case 14: WAIT_VM(14); break;
    case 15: WAIT_VM(15); break; case 16: WAIT_VM(16); break; case 17: WAIT_VM(17); break;
    case 18: WAIT_VM(18); break; case 19: WAIT_VM(19); break; case 20: WAIT_VM(20); break;
    case 21: WAIT_VM(21); break; case 22: WAIT_VM(22); break; case 23: WAIT_VM(23); break;
    case 24: WAIT_VM(24); break; case 25: WAIT_VM(25); break; case 26: WAIT_VM(26); break;
    case 27: WAIT_VM(27); break; case 28: WAIT_VM(28); break; case 29: WAIT_VM(29); break;
    case 30: WAIT_VM(30); break; case 31: WAIT_VM(31); break; case 32: WAIT_VM(32); break;
    case 33: WAIT_VM(33); break; case 34: WAIT_VM(34); break; case 35: WAIT_VM(35); break;
    case 36: WAIT_VM(36); break; case 37: WAIT_VM(37); break; case 38: WAIT_VM(38); break;
    case 39: WAIT_VM(39); break; case 40: WAIT_VM(40); break; case 41: WAIT_VM(41); break;
    case 42: WAIT_VM(42); break; case 43: WAIT_VM(43); break; case 44: WAIT_VM(44); break;
    default: WAIT_VM(0); break;
  }
}
#define SB __builtin_amdgcn_sched_barrier(0)
#define XMFMA MFMA
#define XRD 1
#define XLD 1
DI void bar() { __builtin_amdgcn_s_barrier(); __builtin_amdgcn_sched_barrier(0); }

DI bf16x8 ldB(const char* smem_lane_base, int off0, int off1) {
  s16x4 lo = __builtin_amdgcn_ds_read_tr16_b64_v4i16((lds_s16x4_ptr)(smem_lane_base + off0));
  s16x4 hi = __builtin_amdgcn_ds_read_tr16_b64_v4i16((lds_s16x4_ptr)(smem_lane_base + off1));
  typedef __attribute__((ext_vector_type(8))) short s16x8;
  s16x8 v = __builtin_shufflevector(lo, hi, 0, 1, 2, 3, 4, 5, 6, 7);
  return __builtin_bit_cast(bf16x8, v);
}

DI float phi(float t) {
  float r = t > 0.f ? bfr(t + 1.0f) : bfr(__expf(t));
  return bfr(r + 1e-6f);
}

constexpr int K1_RING = 65536 + 7 * 12288;
constexpr int K1_LDS = K1_RING + 4096;
constexpr int k1_gcnt(int n, int lo) { return n > 15 ? 0 : ((lo ? 2 : 1) + ((n & 1) == 0 ? 1 : 0)); }
constexpr int k1_younger(int n, int lo) {
  return k1_gcnt(n + 1, lo) + k1_gcnt(n + 2, lo) + k1_gcnt(n + 3, lo) + k1_gcnt(n + 4, lo);
}
__global__ __launch_bounds__(512, 2) void k1_qkv(const __bf16* x, const __bf16* w, __bf16* pq, float* kvpart) {
  __shared__ __attribute__((aligned(16))) char smem[K1_LDS];
  STAMP_DECL; STAMP(0);
  const int tid = threadIdx.x, lane = tid & 63, wv = __builtin_amdgcn_readfirstlane(tid >> 6);
  const int g = lane >> 4, c = lane & 15;
  const int hw = wv & 3, rh = wv >> 2;
  const int b = blockIdx.x, xcd = b & 7, idx = b >> 3;
  const int mt = (xcd >> 1) * 8 + (idx >> 2), hq = (xcd & 1) * 4 + (idx & 3);
  const unsigned lds0 = (unsigned)(size_t)smem;

  const __bf16* xsrc = x + (size_t)(mt * 64 + wv * 8) * D_;
  const __bf16* wsrc0 = w + (size_t)((wv & 3) * 8) * 1536 + (wv >> 2) * 512 + hq * 64;
  const __bf16* wsrc1 = w + (size_t)((wv & 3) * 8) * 1536 + 1024 + hq * 64;
  unsigned xoff, woff;
  {
    const int r = lane >> 3, pos = lane & 7;
    const int row = wv * 8 + r;
    xoff = r * (D_ * 2) + (pos ^ ((row >> 1) & 7)) * 16;
    const int swz = ((r >> 1) & 1) | ((wv & 1) << 1);
    woff = r * 3072 + ((pos >> 1) ^ swz) * 32 + (pos & 1) * 16;
  }
  const int rot = 0;
  auto issue1 = [&](int n, int e) {
    const unsigned slot = lds0 + 65536 + (n % 7) * 12288;
    const int kn = (n + rot) & 15;
    if (e == 0) glds16s(wsrc0 + (size_t)kn * 32 * 1536, woff, slot + wv * 1024);
    if (e == 1) { if (wv < 4) glds16s(wsrc1 + (size_t)kn * 32 * 1536, woff, slot + 8192 + wv * 1024); }
    if (e == 2) { if ((n & 1) == 0) glds16s(xsrc + (kn >> 1) * 64, xoff, lds0 + (n >> 1) * 8192 + wv * 1024); }
  };
#pragma unroll
  for (int n = 0; n < 6; ++n) { issue1(n, 0); issue1(n, 1); issue1(n, 2); }
  STAMP(1);

  f32x4 acc[2][3];
#pragma unroll
  for (int i = 0; i < 2; ++i)
#pragma unroll
    for (int j = 0; j < 3; ++j) acc[i][j] = (f32x4){0.f, 0.f, 0.f, 0.f};
  f32x4 stt = {0.f, 0.f, 0.f, 0.f};
  f32x4 csum[3];
#pragma unroll
  for (int j = 0; j < 3; ++j) csum[j] = (f32x4){0.f, 0.f, 0.f, 0.f};
  bf16x8 ones;
#pragma unroll
  for (int j = 0; j < 8; ++j) ones[j] = (__bf16)1.0f;

  const int aoff = (rh * 32 + c) * 128 + ((g ^ (c >> 1)) * 16);
  const int boff = 65536 + g * 1024 + (c >> 2) * 128 + ((hw ^ (((c >> 3) & 1) | ((g & 1) << 1))) * 32) + (c & 3) * 8;
  bf16x8 fa[2][2], fb[2][3];
  auto rdA = [&](int n, int bu) {
    const char* sl = smem + (n >> 1) * 8192 + (aoff ^ ((n & 1) << 6));
    fa[bu][0] = *(const bf16x8*)(sl);
    fa[bu][1] = *(const bf16x8*)(sl + 2048);
  };
  auto rdB = [&](int n, int bu, int p) { fb[bu][p] = ldB(smem + boff + (n % 7) * 12288 + p * 4096, 0, 512); };
  if (wv < 4) wait_vm(k1_gcnt(5, 1) + k1_younger(0, 1)); else wait_vm(k1_gcnt(5, 0) + k1_younger(0, 0));
  bar();
  STAMP(8);
  rdA(0, 0); rdB(0, 0, 0); rdB(0, 0, 1); rdB(0, 0, 2);
#pragma unroll
  for (int n = 0; n < 16; ++n) {
    const int cu = n & 1, nx = cu ^ 1;
    if (n < 15) {
      if (wv < 4) wait_vm(k1_younger(n + 1, 1)); else wait_vm(k1_younger(n + 1, 0));
      bar();
    }
    acc[0][0] = XMFMA(fb[cu][0], fa[cu][0], acc[0][0]); SB;
    if (XRD && n < 15) rdA(n + 1, nx);
    SB;
    acc[1][0] = XMFMA(fb[cu][0], fa[cu][1], acc[1][0]); SB;
    if (XRD && n < 15) rdB(n + 1, nx, 0);
    SB;
    acc[0][1] = XMFMA(fa[cu][0], fb[cu][1], acc[0][1]); SB;
    if (XRD && n < 15) rdB(n + 1, nx, 1);
    SB;
    acc[1][1] = XMFMA(fa[cu][1], fb[cu][1], acc[1][1]); SB;
    if (XRD && n < 15) rdB(n + 1, nx, 2);
    SB;
    acc[0][2] = XMFMA(fa[cu][0], fb[cu][2], acc[0][2]); SB;
    if (XLD && n + 6 < 16) issue1(n + 6, 0);
    SB;
    acc[1][2] = XMFMA(fa[cu][1], fb[cu][2], acc[1][2]); SB;
    if (XLD && n + 6 < 16) issue1(n + 6, 1);
    SB;
    if (hw == 0) stt = XMFMA(ones, fa[cu][0], stt);
    else if (hw == 1) stt = XMFMA(fa[cu][0], fa[cu][0], stt);
    else if (hw == 2) stt = XMFMA(ones, fa[cu][1], stt);
    else stt = XMFMA(fa[cu][1], fa[cu][1], stt);
    SB;
    if (XLD && n + 6 < 16) issue1(n + 6, 2);
    SB;
    if (cu == rh) {
      csum[0] = XMFMA(ones, fb[cu][0], csum[0]);
      csum[1] = XMFMA(ones, fb[cu][1], csum[1]);
      csum[2] = XMFMA(ones, fb[cu][2], csum[2]);
    }
  }
  STAMP(6);
  float* tsum = (float*)(smem + K1_RING);
  float* tsq = tsum + 64;
  float* tcs = tsum + 128;
  float* tst = tsum + 512;
  {
    const int row = rh * 32 + (hw >> 1) * 16 + c;
    if ((hw & 1) == 0) { if (g == 0) tsum[row] = stt[0]; }
    else if (g == (c >> 2)) tsq[row] = (c & 2) ? ((c & 1) ? stt[3] : stt[2]) : ((c & 1) ? stt[1] : stt[0]);
    if (g == 0) {
#pragma unroll
      for (int p = 0; p < 3; ++p) tcs[rh * 192 + p * 64 + hw * 16 + c] = csum[p][0];
    }
  }
  __syncthreads();
  if (tid < 64) {
    const float mean = tsum[tid] * (1.0f / 512.0f);
    const float var = fmaxf(tsq[tid] * (1.0f / 512.0f) - mean * mean, 0.f);
    const float sb = bfr(sqrtf(bfr(bfr(var) + 1e-5f)));
    *(f32x2*)(tst + tid * 2) = (f32x2){bfr(mean), 1.0f / sb};
  }
  __syncthreads();
  const f32x4 csq = *(const f32x4*)(tcs + hw * 16 + 4 * g) + *(const f32x4*)(tcs + 192 + hw * 16 + 4 * g);
  const float csk = tcs[64 + hw * 16 + c] + tcs[192 + 64 + hw * 16 + c];
  const float csv = tcs[128 + hw * 16 + c] + tcs[192 + 128 + hw * 16 + c];
#pragma unroll
  for (int i = 0; i < 2; ++i) {
    const f32x2 st = *(const f32x2*)(tst + (rh * 32 + i * 16 + c) * 2);
    bf16x4 o;
#pragma unroll
    for (int r = 0; r < 4; ++r) o[r] = (__bf16)phi(bfr(st[1] * (acc[i][0][r] - st[0] * csq[r])));
    const int rt = mt * 4 + rh * 2 + i;
    *(bf16x4*)(pq + ((size_t)(rt * 16 + hq * 2 + (hw >> 1)) * 64 + lane) * 8 + (hw & 1) * 4) = o;
  }
  STAMP(7);
  {
    bf16x8 pk, vv;
#pragma unroll
    for (int i = 0; i < 2; ++i)
#pragma unroll
      for (int r = 0; r < 4; r += 2) {
        const f32x4 st = *(const f32x4*)(tst + (rh * 32 + i * 16 + 4 * g + r) * 2);
        pk[i * 4 + r] = (__bf16)phi(bfr(st[1] * (acc[i][1][r] - st[0] * csk)));
        pk[i * 4 + r + 1] = (__bf16)phi(bfr(st[3] * (acc[i][1][r + 1] - st[2] * csk)));
        vv[i * 4 + r] = (__bf16)(st[1] * (acc[i][2][r] - st[0] * csv));
        vv[i * 4 + r + 1] = (__bf16)(st[3] * (acc[i][2][r + 1] - st[2] * csv));
      }
    const f32x4 z4 = {0.f, 0.f, 0.f, 0.f};
    f32x4 kv = MFMA(pk, vv, z4);
    f32x4 zz = MFMA(pk, ones, z4);
    float* dst = kvpart + (size_t)((mt * 2 + rh) * 32 + hq * 4 + hw) * KVREC;
    *(f32x4*)(dst + lane * 4) = kv;
    if (c == 0) *(f32x4*)(dst + 256 + g * 4) = zz;
  }
  STAMP(2); STAMP_OUT(0, pq);
}

__global__ __launch_bounds__(256) void k1b_reduce(const float* kvpart, __bf16* kvf, const __bf16* wo, const __bf16* wg,
                                                  const __bf16* wu, const __bf16* wd, float* csp) {
  __shared__ __attribute__((aligned(16))) float red[32 * 64];
  STAMP_DECL; STAMP(0);
  const int tid = threadIdx.x, b = blockIdx.x, lane = tid & 63, wv = __builtin_amdgcn_readfirstlane(tid >> 6);
  if (b < 34) {
    const int q = b * 64 + lane;
    f32x4 v[16];
#pragma unroll
    for (int m = 0; m < 16; ++m) v[m] = *(const f32x4*)(kvpart + (size_t)(wv * 16 + m) * 32 * KVREC + q * 4);
    f32x4 s = v[0];
#pragma unroll
    for (int m = 1; m < 16; ++m) s += v[m];
    f32x4* r4 = (f32x4*)red;
    r4[tid] = s;
    __syncthreads();
    if (tid < 64) {
      f32x4 t = (r4[tid] + r4[64 + tid]) + (r4[128 + tid] + r4[192 + tid]);
      bf16x4 o;
#pragma unroll
      for (int j = 0; j < 4; ++j) o[j] = (__bf16)t[j];
      *(bf16x4*)(kvf + q * 4) = o;
    }
    STAMP(1); STAMP_OUT(1, (const char*)kvpart - (10u << 20));
    return;
  }
  if (b >= 77) {
    const int pb = b - 77;
    const u32x4* src = pb < 32 ? (const u32x4*)wo + (size_t)pb * 1024 : (const u32x4*)wd + (size_t)(pb - 32) * 1024;
    u32x4 v0 = src[tid], v1 = src[256 + tid], v2 = src[512 + tid], v3 = src[768 + tid];
    asm volatile("" :: "v"(v0), "v"(v1), "v"(v2), "v"(v3));
    return;
  }
  const int cb = b - 34;
  const int col0 = cb * 64 + (tid & 7) * 8;
  const __bf16* src = col0 < F_ ? (wg + col0) : (wu + (col0 - F_));
  const int kq = tid >> 3;
  bf16x8 v[16];
#pragma unroll
  for (int r = 0; r < 16; ++r) v[r] = *(const bf16x8*)(src + (size_t)(kq * 16 + r) * F_);
  float s[8];
#pragma unroll
  for (int j = 0; j < 8; ++j) s[j] = 0.f;
#pragma unroll
  for (int r = 0; r < 16; ++r)
#pragma unroll
    for (int j = 0; j < 8; ++j) s[j] += (float)v[r][j];
#pragma unroll
  for (int j = 0; j < 8; ++j) red[kq * 64 + (tid & 7) * 8 + j] = s[j];
  __syncthreads();
  if (tid < 64) {
    float t = 0.f;
#pragma unroll
    for (int q = 0; q < 32; ++q) t += red[q * 64 + tid];
    csp[cb * 64 + tid] = t;
  }
  STAMP(2); STAMP_OUT(1, (const char*)kvpart - (10u << 20));
}

constexpr int K2_LDS = 64 * 1024 + 17408;
__global__ __launch_bounds__(512, 2) void k2_attn_out(const __bf16* x, const __bf16* wo, const __bf16* pq,
                                                      const __bf16* kvf, __bf16* hfr, float* hstat) {
  __shared__ __attribute__((aligned(16))) char smem[K2_LDS];
  STAMP_DECL; STAMP(0);
  const int tid = threadIdx.x, lane = tid & 63, wv = __builtin_amdgcn_readfirstlane(tid >> 6);
  const int g = lane >> 4, c = lane & 15;
  const int rg = wv & 3, kh = wv >> 2;
  const int b = blockIdx.x, xcd = b & 7, idx = b >> 3;
  const int mt = xcd * 4 + (idx >> 3), nt = idx & 7;
  const unsigned lds0 = (unsigned)(size_t)smem;
  const int rt = mt * 4 + rg;

  bf16x4 xr[2];
#pragma unroll
  for (int p = 0; p < 2; ++p) xr[p] = *(const bf16x4*)(x + (size_t)(rt * 16 + c) * D_ + nt * 64 + (kh * 2 + p) * 16 + g * 4);
  bf16x8 pf[8];
#pragma unroll
  for (int h = 0; h < 8; ++h) pf[h] = *(const bf16x8*)(pq + ((size_t)(rt * 16 + kh * 8 + h) * 64 + lane) * 8);
#pragma unroll
  for (int i = 0; i < 3; ++i) {
    const int ins = i * 8 + wv;
    if (ins < 17) glds16s((const char*)kvf + ins * 1024, lane * 16, lds0 + 65536 + ins * 1024);
  }
  {
    const int r = lane >> 3, pos = lane & 7;
    const unsigned woff = r * (D_ * 2) + (((pos >> 1) ^ ((r >> 1) & 3)) * 32) + (pos & 1) * 16;
#pragma unroll
    for (int i = 0; i < 8; ++i) {
      const int kb = i * 8 + wv;
      glds16s(wo + (size_t)kb * 8 * D_ + nt * 64, woff, lds0 + kb * 1024);
    }
  }
  f32x4 acc[4];
#pragma unroll
  for (int j = 0; j < 4; ++j) acc[j] = (f32x4){0.f, 0.f, 0.f, 0.f};
  STAMP(1);
  WAIT_VM(0);
  bar();
  STAMP(2);
  const int brow = (g & 1) * 4 + (c >> 2);
  const char* bl = smem + kh * 32768 + (g >> 1) * 1024 + brow * 128 + (c & 3) * 8;
  const int bsw = (brow >> 1) & 3;
  const char* kvl = smem + 65536 + kh * 16 * (KVREC * 2);
  const f32x4 z4 = {0.f, 0.f, 0.f, 0.f};
#pragma unroll
  for (int h = 0; h < 8; ++h) {
    bf16x4 k0 = *(const bf16x4*)(kvl + (2 * h) * (KVREC * 2) + lane * 8);
    bf16x4 k1 = *(const bf16x4*)(kvl + (2 * h + 1) * (KVREC * 2) + lane * 8);
    bf16x4 z0 = *(const bf16x4*)(kvl + (2 * h) * (KVREC * 2) + 512 + g * 8);
    bf16x4 z1 = *(const bf16x4*)(kvl + (2 * h + 1) * (KVREC * 2) + 512 + g * 8);
    const bf16x4 zz = {(__bf16)0.f, (__bf16)0.f, (__bf16)0.f, (__bf16)0.f};
    bf16x8 a0 = __builtin_shufflevector(k0, zz, 0, 1, 2, 3, 4, 5, 6, 7);
    bf16x8 a1 = __builtin_shufflevector(zz, k1, 0, 1, 2, 3, 4, 5, 6, 7);
    bf16x8 d0 = __builtin_shufflevector(z0, zz, 0, 1, 2, 3, 4, 5, 6, 7);
    bf16x8 d1 = __builtin_shufflevector(zz, z1, 0, 1, 2, 3, 4, 5, 6, 7);
    f32x4 n0 = MFMA(a0, pf[h], z4);
    f32x4 n1 = MFMA(a1, pf[h], z4);
    f32x4 e0 = MFMA(d0, pf[h], z4);
    f32x4 e1 = MFMA(d1, pf[h], z4);
    const float r0 = __builtin_amdgcn_rcpf(bfr(bfr(e0[0]) + 1e-6f));
    const float r1 = __builtin_amdgcn_rcpf(bfr(bfr(e1[0]) + 1e-6f));
    bf16x8 of;
#pragma unroll
    for (int r = 0; r < 4; ++r) {
      of[r] = (__bf16)(bfr(n0[r]) * r0);
      of[4 + r] = (__bf16)(bfr(n1[r]) * r1);
    }
#pragma unroll
    for (int ct = 0; ct < 4; ++ct) {
      bf16x8 wf = ldB(bl + ((ct ^ bsw) * 32), h * 4096, h * 4096 + 2048);
      acc[ct] = MFMA(wf, of, acc[ct]);
    }
  }
  STAMP(3);
  __syncthreads();
  f32x4* red = (f32x4*)smem;
  red[((rg * 2 + kh) * 2 + 0) * 64 + lane] = kh ? acc[0] : acc[2];
  red[((rg * 2 + kh) * 2 + 1) * 64 + lane] = kh ? acc[1] : acc[3];
  __syncthreads();
  f32x4 f0 = (kh ? acc[2] : acc[0]), f1 = (kh ? acc[3] : acc[1]);
  {
    f32x4 o0 = red[((rg * 2 + (kh ^ 1)) * 2 + 0) * 64 + lane], o1 = red[((rg * 2 + (kh ^ 1)) * 2 + 1) * 64 + lane];
    f0 = kh ? (o0 + f0) : (f0 + o0);
    f1 = kh ? (o1 + f1) : (f1 + o1);
  }
  float hv[8];
  float s = 0.f;
#pragma unroll
  for (int r = 0; r < 4; ++r) {
    hv[r] = bfr((float)xr[0][r] + bfr(f0[r]));
    hv[4 + r] = bfr((float)xr[1][r] + bfr(f1[r]));
    s += hv[r] + hv[4 + r];
  }
  {
    bf16x8 o;
#pragma unroll
    for (int r = 0; r < 8; ++r) o[r] = (__bf16)hv[r];
    *(bf16x8*)(hfr + ((size_t)(rt * 16 + nt * 2 + kh) * 64 + lane) * 8) = o;
  }
  s += __shfl_xor(s, 16, 64);
  s += __shfl_xor(s, 32, 64);
  const float mean = s * (1.0f / 32.0f);
  float m2 = 0.f;
#pragma unroll
  for (int r = 0; r < 8; ++r) { float d = hv[r] - mean; m2 += d * d; }
  m2 += __shfl_xor(m2, 16, 64);
  m2 += __shfl_xor(m2, 32, 64);
  if (g == 0) *(f32x2*)(hstat + ((size_t)(rt * 16 + c) * 16 + nt * 2 + kh) * 2) = (f32x2){s, m2};
  STAMP(4); STAMP_OUT(2, pq);
}

constexpr int K3_RING = 7 * 20480;
constexpr int K3_LDS = K3_RING + 16384;
__global__ __launch_bounds__(512, 2) void k3_gate_up(const __bf16* hfr, const float* hstat, const __bf16* wg,
                                                     const __bf16* wu, const float* csp, __bf16* hh) {
  __shared__ __attribute__((aligned(16))) char smem[K3_LDS];
  STAMP_DECL; STAMP(0);
  const int tid = threadIdx.x, lane = tid & 63, wv = __builtin_amdgcn_readfirstlane(tid >> 6);
  const int g = lane >> 4, c = lane & 15;
  const int mw = wv & 3, nw = wv >> 2;
  const int b = blockIdx.x, xcd = b & 7, idx = b >> 3;
  const int mt = (xcd >> 2) * 8 + (idx >> 2), fgp = (xcd & 3) * 4 + (idx & 3);
  const int ft0 = 2 * ((fgp * 43) >> 4);
  const unsigned lds0 = (unsigned)(size_t)smem;
  const int rt0 = mt * 8 + mw * 2;

  const char* pA = (const char*)hfr + (size_t)(mt * 8 + wv) * 16384;
  const unsigned boffl = (lane >> 2) * (F_ * 2) + ((((lane >> 1) & 1) ^ ((lane >> 4) & 1)) * 32) + (lane & 1) * 16;
  const int q0 = wv, q1 = 8 + (wv & 3);
  const __bf16* pB0 = (q0 < 6 ? wg : wu) + (size_t)((q0 & 1) * 16) * F_ + (ft0 + ((q0 % 6) >> 1) * 2) * 16;
  const __bf16* pB1 = wu + (size_t)((q1 & 1) * 16) * F_ + (ft0 + ((q1 - 6) >> 1) * 2) * 16;
  const int rot = 0;
  auto issue1 = [&](int n, int e) {
    const unsigned slot = lds0 + (n % 7) * 20480;
    const int kn = (n + rot) & 15;
    if (e == 0) glds16s(pA + kn * 1024, lane * 16, slot + wv * 1024);
    if (e == 1) glds16s(pB0 + (size_t)kn * 32 * F_, boffl, slot + 8192 + wv * 1024);
    if (e == 2) { if (wv < 4) glds16s(pB1 + (size_t)kn * 32 * F_, boffl, slot + 16384 + wv * 1024); }
  };
  auto issue = [&](int n) { issue1(n, 0); issue1(n, 1); issue1(n, 2); };
  issue(0); issue(1);
  glds16s((const char*)hstat + (size_t)mt * 16384 + wv * 2048, lane * 16, lds0 + K3_RING + wv * 2048);
  glds16s((const char*)hstat + (size_t)mt * 16384 + wv * 2048 + 1024, lane * 16, lds0 + K3_RING + wv * 2048 + 1024);
#pragma unroll
  for (int n = 2; n < 6; ++n) issue(n);
  STAMP(1);

  f32x4 acc[2][6];
#pragma unroll
  for (int i = 0; i < 2; ++i)
#pragma unroll
    for (int j = 0; j < 6; ++j) acc[i][j] = (f32x4){0.f, 0.f, 0.f, 0.f};
  bf16x8 fa[2][2], fb[2][6];
  const int bl0 = 8192 + (4 * g + (c >> 2)) * 64 + (c & 3) * 8;
  auto rdA = [&](int n, int bu) {
    const char* sl = smem + (n % 7) * 20480;
    fa[bu][0] = *(const bf16x8*)(sl + lane * 16 + (mw * 2) * 1024);
    fa[bu][1] = *(const bf16x8*)(sl + lane * 16 + (mw * 2 + 1) * 1024);
  };
  auto rdB = [&](int n, int bu, int ct) {
    const char* sl = smem + (n % 7) * 20480;
    const int ftl = nw * 3 + (ct >> 1);
    const char* bp = sl + bl0 + ((ct & 1) * 6 + (ftl >> 1) * 2) * 1024 + (((ftl & 1) ^ (g & 1)) * 32);
    fb[bu][ct] = ldB(bp, 0, 1024);
  };
  auto rd = [&](int n, int bu) {
    rdA(n, bu);
#pragma unroll
    for (int ct = 0; ct < 6; ++ct) rdB(n, bu, ct);
  };
  if (wv < 4) WAIT_VM(17); else WAIT_VM(12);
  bar();
  STAMP(2);
  rd(0, 0);
  float mb[2], rs[2];
  f32x4 cs[6];
#pragma unroll
  for (int n = 0; n < 16; ++n) {
    if (n < 15) {
      const int y = (n + 5 < 15 ? n + 5 : 15) - (n + 1);
      if (wv < 4) wait_vm(3 * y + (n == 0 ? 2 : 0)); else wait_vm(2 * y + (n == 0 ? 2 : 0));
      bar();
    }
    if (n == 2) {
#pragma unroll
      for (int i = 0; i < 2; ++i) {
        const char* sp = smem + K3_RING + ((mw * 2 + i) * 16 + c) * 128 + g * 32;
        const f32x4 s0 = *(const f32x4*)(sp), s1 = *(const f32x4*)(sp + 16);
        float s = (s0[0] + s0[2]) + (s1[0] + s1[2]);
        s += __shfl_xor(s, 16, 64);
        s += __shfl_xor(s, 32, 64);
        const float mean = s * (1.0f / 512.0f);
        const float d0 = s0[0] * (1.0f / 32.0f) - mean, d1 = s0[2] * (1.0f / 32.0f) - mean;
        const float d2 = s1[0] * (1.0f / 32.0f) - mean, d3 = s1[2] * (1.0f / 32.0f) - mean;
        float m2 = (s0[1] + s0[3]) + (s1[1] + s1[3]) + 32.0f * ((d0 * d0 + d1 * d1) + (d2 * d2 + d3 * d3));
        m2 += __shfl_xor(m2, 16, 64);
        m2 += __shfl_xor(m2, 32, 64);
        mb[i] = bfr(mean);
        rs[i] = 1.0f / bfr(sqrtf(bfr(bfr(m2 * (1.0f / 512.0f)) + 1e-5f)));
      }
    }
    if (n == 10) {
#pragma unroll
      for (int ct = 0; ct < 6; ++ct)
        cs[ct] = *(const f32x4*)(csp + (ct & 1) * F_ + (ft0 + nw * 3 + (ct >> 1)) * 16 + g * 4);
    }
#pragma unroll
    for (int ct = 0; ct < 6; ++ct) {
      acc[0][ct] = MFMA(fb[n & 1][ct], fa[n & 1][0], acc[0][ct]);
      SB;
      if (n < 15) { if (ct == 0) rdA(n + 1, (n + 1) & 1); rdB(n + 1, (n + 1) & 1, ct); }
      SB;
      acc[1][ct] = MFMA(fb[n & 1][ct], fa[n & 1][1], acc[1][ct]);
      SB;
      if (n + 6 < 16 && ct >= 1 && ct <= 3) issue1(n + 6, ct - 1);
      SB;
    }
  }
  STAMP(6);
#pragma unroll
  for (int i = 0; i < 2; ++i) {
#pragma unroll
    for (int j = 0; j < 3; ++j) {
      const int ft = ft0 + nw * 3 + j;
      bf16x4 o;
#pragma unroll
      for (int r = 0; r < 4; ++r) {
        float gg = bfr(rs[i] * (acc[i][2 * j][r] - mb[i] * cs[2 * j][r]));
        float uu = bfr(rs[i] * (acc[i][2 * j + 1][r] - mb[i] * cs[2 * j + 1][r]));
        o[r] = (__bf16)(gg * uu);
      }
      *(bf16x4*)(hh + ((size_t)((rt0 + i) * 43 + (ft >> 1)) * 64 + lane) * 8 + (ft & 1) * 4) = o;
    }
  }
  STAMP(7); STAMP_OUT(3, (const char*)hfr - (2u << 20));
}

constexpr int K4_LDS = 160 * 1024;
__global__ __launch_bounds__(512, 2) void k4_down(const __bf16* hh, const __bf16* wd, const __bf16* hfr, __bf16* out) {
  __shared__ __attribute__((aligned(16))) char smem[K4_LDS];
  STAMP_DECL; STAMP(0);
  const int tid = threadIdx.x, lane = tid & 63, wv = __builtin_amdgcn_readfirstlane(tid >> 6);
  const int g = lane >> 4, c = lane & 15;
  const int b = blockIdx.x, xcd = b & 7, idx = b >> 3;
  const int mt = xcd * 4 + (idx >> 3), nt = idx & 7;
  const int mi = wv & 1, kq = wv >> 1;
  const unsigned lds0 = (unsigned)(size_t)smem;
  const int rt0 = mt * 4 + mi * 2;
  const int u0 = kq * 11;
  const int fi = kq >> 1, fc = (kq & 1) * 2;

  const __bf16* wsrc = wd + (size_t)(u0 * 32 + mi * 16) * D_ + nt * 64;
  const unsigned woff = (lane >> 3) * (D_ * 2) + ((((lane & 7) >> 1) ^ ((lane >> 4) & 3)) * 32) + (lane & 1) * 16;
  const unsigned ldsq = lds0 + kq * 40960 + mi * 2048;
  bf16x8 af[2][11];
  const char* asrc = (const char*)hh + (size_t)(rt0 * 43 + u0) * 1024;
  auto issue = [&](int s) {
    const int ss = (s == 10 && kq == 3) ? 9 : s;
    glds16s(wsrc + (size_t)(ss * 32) * D_, woff, ldsq + (s % 10) * 4096);
    glds16s(wsrc + (size_t)(ss * 32 + 8) * D_, woff, ldsq + (s % 10) * 4096 + 1024);
    gload16(af[0][s], asrc + ss * 1024, lane * 16);
    gload16(af[1][s], asrc + (43 + ss) * 1024, lane * 16);
  };
#pragma unroll
  for (int s = 0; s < 5; ++s) issue(s);
  f32x4 acc[2][4];
#pragma unroll
  for (int i = 0; i < 2; ++i)
#pragma unroll
    for (int j = 0; j < 4; ++j) acc[i][j] = (f32x4){0.f, 0.f, 0.f, 0.f};
  const int brow = (g & 1) * 4 + (c >> 2);
  const char* bl = smem + kq * 40960 + (g >> 1) * 1024 + brow * 128 + (c & 3) * 8;
  const int bsw = (brow >> 1) & 3;
  auto comp = [&](int s) {
    TIE(af[0][s]); TIE(af[1][s]);
#pragma unroll
    for (int ct = 0; ct < 4; ++ct) {
      bf16x8 bf = ldB(bl + ((ct ^ bsw) * 32), (s % 10) * 4096, (s % 10) * 4096 + 2048);
      acc[0][ct] = MFMA(bf, af[0][s], acc[0][ct]);
      acc[1][ct] = MFMA(bf, af[1][s], acc[1][ct]);
    }
  };
  STAMP(1);
  WAIT_VM(12);
  bar();
  STAMP(2);
  issue(5); issue(6); issue(7);
  comp(0); comp(1);
  WAIT_VM(12);
  bar();
  STAMP(3);
  issue(8); issue(9); issue(10);
  comp(2); comp(3); comp(4);
  WAIT_VM(12);
  bar();
  STAMP(4);
  bf16x8 hres;
  gload16(hres, (const char*)hfr + (size_t)((rt0 + fi) * 16 + nt * 2 + (kq & 1)) * 1024, lane * 16);
  comp(5); comp(6); comp(7);
  WAIT_VM(1);
  bar();
  comp(8); comp(9);
  if (kq < 3) comp(10);
  STAMP(5);
  WAIT_VM(0);
  TIE(hres);
  bar();
  f32x4* red = (f32x4*)smem;
#pragma unroll
  for (int i = 0; i < 2; ++i)
#pragma unroll
    for (int ct = 0; ct < 4; ++ct)
      if (i != fi || (ct >> 1) != (kq & 1)) red[(((mi * 4 + kq) * 2 + i) * 4 + ct) * 64 + lane] = acc[i][ct];
  __syncthreads();
#pragma unroll
  for (int p = 0; p < 2; ++p) {
    const int ct = fc + p;
    const f32x4 own = fi ? (fc ? acc[1][2 + p] : acc[1][p]) : (fc ? acc[0][2 + p] : acc[0][p]);
    f32x4 v[4];
#pragma unroll
    for (int q = 0; q < 4; ++q) v[q] = q == kq ? own : red[(((mi * 4 + q) * 2 + fi) * 4 + ct) * 64 + lane];
    f32x4 s = (v[0] + v[1]) + (v[2] + v[3]);
    bf16x4 o;
#pragma unroll
    for (int r = 0; r < 4; ++r) o[r] = (__bf16)((float)hres[p * 4 + r] + bfr(s[r]));
    *(bf16x4*)(out + (size_t)((rt0 + fi) * 16 + c) * D_ + nt * 64 + ct * 16 + g * 4) = o;
  }
  STAMP(6); STAMP_OUT(4, (const char*)hfr - (2u << 20));
}

extern "C" void kernel_launch(void* const* d_in, const int* in_sizes, int n_in, void* d_out, int out_size,
                              void* d_ws, size_t ws_size, hipStream_t stream) {
  const __bf16* x = (const __bf16*)d_in[0];
  const __bf16* wqkv = (const __bf16*)d_in[1];
  const __bf16* wo = (const __bf16*)d_in[2];
  const __bf16* wg = (const __bf16*)d_in[4];
  const __bf16* wu = (const __bf16*)d_in[5];
  const __bf16* wd = (const __bf16*)d_in[6];
  char* ws = (char*)d_ws;
  __bf16* pq = (__bf16*)(ws);
  __bf16* hfr = (__bf16*)(ws + (2u << 20));
  __bf16* hh = (__bf16*)(ws + (4u << 20));
  float* kvpart = (float*)(ws + (10u << 20));
  __bf16* kvf = (__bf16*)(ws + (13u << 20));
  float* csp = (float*)(ws + (13u << 20) + 65536);
  float* hstat = (float*)(ws + (14u << 20));
  k1_qkv<<<256, 512, 0, stream>>>(x, wqkv, pq, kvpart);
  k1b_reduce<<<77 + 118, 256, 0, stream>>>(kvpart, kvf, wo, wg, wu, wd, csp);
  k2_attn_out<<<256, 512, 0, stream>>>(x, wo, pq, kvf, hfr, hstat);
  k3_gate_up<<<256, 512, 0, stream>>>(hfr, hstat, wg, wu, csp, hh);
  k4_down<<<256, 512, 0, stream>>>(hh, wd, hfr, (__bf16*)d_out);
}
```

```cpp
#include <hip/hip_runtime.h>
#include <hip/hip_bf16.h>
#include <stdint.h>

typedef __attribute__((ext_vector_type(8))) __bf16 bf16x8;
typedef __attribute__((ext_vector_type(4))) __bf16 bf16x4;
typedef __attribute__((ext_vector_type(4))) short s16x4;
typedef __attribute__((ext_vector_type(4))) float f32x4;
typedef __attribute__((ext_vector_type(2))) float f32x2;
typedef __attribute__((ext_vector_type(4))) unsigned u32x4;
typedef __attribute__((ext_vector_type(2))) unsigned u32x2;
typedef __attribute__((address_space(3))) s16x4* lds_s16x4_ptr;

#define DI __device__ __forceinline__
#define MFMA(a, b, c) __builtin_amdgcn_mfma_f32_16x16x32_bf16((a), (b), (c), 0, 0, 0)

constexpr int S_ = 2048, D_ = 512, F_ = 1376;
constexpr int KVREC = 272;

#define STAMP_DECL
#define STAMP(i)
#define STAMP_OUT(k, wsbase)
DI float bfr(float x) { return (float)(__bf16)x; }
DI float bf2f(__bf16 x) { return (float)x; }

DI void glds16(const void* gsrc, unsigned lds_dst) {
  unsigned keep;
  asm volatile(
      "s_mov_b32 %0, m0\n\ts_mov_b32 m0, %2\n\ts_nop 0\n\tglobal_load_lds_dwordx4 %1, off\n\ts_mov_b32 m0, %0"
      : "=&s"(keep)
      : "v"(gsrc), "s"(lds_dst)
      : "memory");
}
DI void glds16s(const void* ubase, unsigned voff, unsigned lds_dst) {
  unsigned keep;
  asm volatile(
      "s_mov_b32 %0, m0\n\ts_mov_b32 m0, %3\n\ts_nop 0\n\tglobal_load_lds_dwordx4 %1, %2\n\ts_mov_b32 m0, %0"
      : "=&s"(keep)
      : "v"(voff), "s"(ubase), "s"(lds_dst)
      : "memory");
}
DI void gload16(bf16x8& dst, const void* ubase, unsigned voff) {
  asm volatile("global_load_dwordx4 %0, %1, %2" : "=v"(dst) : "v"(voff), "s"(ubase) : "memory");
}
DI void gload8(bf16x4& dst, const void* ubase, unsigned voff) {
  asm volatile("global_load_dwordx2 %0, %1, %2" : "=v"(dst) : "v"(voff), "s"(ubase) : "memory");
}
DI void gload16_sc1(bf16x8& dst, const void* ubase, unsigned voff) {
  asm volatile("global_load_dwordx4 %0, %1, %2 sc1" : "=v"(dst) : "v"(voff), "s"(ubase) : "memory");
}
DI void glds16s_sc1(const void* ubase, unsigned voff, unsigned lds_dst) {
  unsigned keep;
  asm volatile(
      "s_mov_b32 %0, m0\n\ts_mov_b32 m0, %3\n\ts_nop 0\n\tglobal_load_lds_dwordx4 %1, %2 sc1\n\ts_mov_b32 m0, %0"
      : "=&s"(keep)
      : "v"(voff), "s"(ubase), "s"(lds_dst)
      : "memory");
}
DI void gstore16_sc1(void* ubase, unsigned voff, bf16x8 v) {
  asm volatile("global_store_dwordx4 %0, %1, %2 sc1\n\ts_nop 1" :: "v"(voff), "v"(v), "s"(ubase) : "memory");
}
DI void gstore8_sc1(void* ubase, unsigned voff, bf16x4 v) {
  asm volatile("global_store_dwordx2 %0, %1, %2 sc1\n\ts_nop 1" :: "v"(voff), "v"(v), "s"(ubase) : "memory");
}
DI void gstore8f_sc1(void* ubase, unsigned voff, f32x2 v) {
  asm volatile("global_store_dwordx2 %0, %1, %2 sc1\n\ts_nop 1" :: "v"(voff), "v"(v), "s"(ubase) : "memory");
}
#define TIE(x) asm volatile("" : "+v"(x))
#define WAIT_VM(n) do { __builtin_amdgcn_sched_barrier(0); asm volatile("s_waitcnt vmcnt(" #n ") lgkmcnt(0)" ::: "memory"); } while (0)
DI void wait_vm(int n) {
  switch (n) {
    case 0: WAIT_VM(0); break;   case 1: WAIT_VM(1); break;   case 2: WAIT_VM(2); break;
    case 3: WAIT_VM(3); break;   case 4: WAIT_VM(4); break;   case 5: WAIT_VM(5); break;
    case 6: WAIT_VM(6); break;   case 7: WAIT_VM(7); break;   case 8: WAIT_VM(8); break;
    case 9: WAIT_VM(9); break;   case 10: WAIT_VM(10); break; case 11: WAIT_VM(11); break;
    case 12: WAIT_VM(12); break; case 13: WAIT_VM(13); break; case 14: WAIT_VM(14); break;
    case 15: WAIT_VM(15); break; case 16: WAIT_VM(16); break; case 17: WAIT_VM(17); break;
    case 18: WAIT_VM(18); break; case 19: WAIT_VM(19); break; case 20: WAIT_VM(20); break;
    case 21: WAIT_VM(21); break; case 22: WAIT_VM(22); break; case 23: WAIT_VM(23); break;
    case 24: WAIT_VM(24); break; case 25: WAIT_VM(25); break; case 26: WAIT_VM(26); break;
    case 27: WAIT_VM(27); break; case 28: WAIT_VM(28); break; case 29: WAIT_VM(29); break;
    case 30: WAIT_VM(30); break; case 31: WAIT_VM(31); break; case 32: WAIT_VM(32); break;
    case 33: WAIT_VM(33); break; case 34: WAIT_VM(34); break; case 35: WAIT_VM(35); break;
    case 36: WAIT_VM(36); break; case 37: WAIT_VM(37); break; case 38: WAIT_VM(38); break;
    case 39: WAIT_VM(39); break; case 40: WAIT_VM(40); break; case 41: WAIT_VM(41); break;
    case 42: WAIT_VM(42); break; case 43: WAIT_VM(43); break; case 44: WAIT_VM(44); break;
    default: WAIT_VM(0); break;
  }
}
#define SB __builtin_amdgcn_sched_barrier(0)
#define XMFMA MFMA
#define XRD 1
#define XLD 1
DI void bar() { __builtin_amdgcn_s_barrier(); __builtin_amdgcn_sched_barrier(0); }

DI bf16x8 ldB(const char* smem_lane_base, int off0, int off1) {
  s16x4 lo = __builtin_amdgcn_ds_read_tr16_b64_v4i16((lds_s16x4_ptr)(smem_lane_base + off0));
  s16x4 hi = __builtin_amdgcn_ds_read_tr16_b64_v4i16((lds_s16x4_ptr)(smem_lane_base + off1));
  typedef __attribute__((ext_vector_type(8))) short s16x8;
  s16x8 v = __builtin_shufflevector(lo, hi, 0, 1, 2, 3, 4, 5, 6, 7);
  return __builtin_bit_cast(bf16x8, v);
}

DI float phi(float t) {
  float r = t > 0.f ? bfr(t + 1.0f) : bfr(__expf(t));
  return bfr(r + 1e-6f);
}

constexpr int K1_RING = 65536 + 7 * 12288;
constexpr int K1_LDS = K1_RING + 4096;
constexpr int k1_gcnt(int n, int lo) { return n > 15 ? 0 : ((lo ? 2 : 1) + ((n & 1) == 0 ? 1 : 0)); }
constexpr int k1_younger(int n, int lo) {
  return k1_gcnt(n + 1, lo) + k1_gcnt(n + 2, lo) + k1_gcnt(n + 3, lo) + k1_gcnt(n + 4, lo);
}
__global__ __launch_bounds__(512, 2) void k1_qkv(const __bf16* x, const __bf16* w, __bf16* pq, float* kvpart, unsigned* cnt) {
  __shared__ __attribute__((aligned(16))) char smem[K1_LDS];
  STAMP_DECL; STAMP(0);
  const int tid = threadIdx.x, lane = tid & 63, wv = __builtin_amdgcn_readfirstlane(tid >> 6);
  const int g = lane >> 4, c = lane & 15;
  const int hw = wv & 3, rh = wv >> 2;
  const int b = blockIdx.x, xcd = b & 7, idx = b >> 3;
  const int mt = (xcd >> 1) * 8 + (idx >> 2), hq = (xcd & 1) * 4 + (idx & 3);
  const unsigned lds0 = (unsigned)(size_t)smem;

  const __bf16* xsrc = x + (size_t)(mt * 64 + wv * 8) * D_;
  const __bf16* wsrc0 = w + (size_t)((wv & 3) * 8) * 1536 + (wv >> 2) * 512 + hq * 64;
  const __bf16* wsrc1 = w + (size_t)((wv & 3) * 8) * 1536 + 1024 + hq * 64;
  unsigned xoff, woff;
  {
    const int r = lane >> 3, pos = lane & 7;
    const int row = wv * 8 + r;
    xoff = r * (D_ * 2) + (pos ^ ((row >> 1) & 7)) * 16;
    const int swz = ((r >> 1) & 1) | ((wv & 1) << 1);
    woff = r * 3072 + ((pos >> 1) ^ swz) * 32 + (pos & 1) * 16;
  }
  const int rot = 0;
  auto issue1 = [&](int n, int e) {
    const unsigned slot = lds0 + 65536 + (n % 7) * 12288;
    const int kn = (n + rot) & 15;
    if (e == 0) glds16s(wsrc0 + (size_t)kn * 32 * 1536, woff, slot + wv * 1024);
    if (e == 1) { if (wv < 4) glds16s(wsrc1 + (size_t)kn * 32 * 1536, woff, slot + 8192 + wv * 1024); }
    if (e == 2) { if ((n & 1) == 0) glds16s(xsrc + (kn >> 1) * 64, xoff, lds0 + (n >> 1) * 8192 + wv * 1024); }
  };
#pragma unroll
  for (int n = 0; n < 6; ++n) { issue1(n, 0); issue1(n, 1); issue1(n, 2); }
  STAMP(1);

  f32x4 acc[2][3];
#pragma unroll
  for (int i = 0; i < 2; ++i)
#pragma unroll
    for (int j = 0; j < 3; ++j) acc[i][j] = (f32x4){0.f, 0.f, 0.f, 0.f};
  f32x4 stt = {0.f, 0.f, 0.f, 0.f};
  f32x4 csum[3];
#pragma unroll
  for (int j = 0; j < 3; ++j) csum[j] = (f32x4){0.f, 0.f, 0.f, 0.f};
  bf16x8 ones;
#pragma unroll
  for (int j = 0; j < 8; ++j) ones[j] = (__bf16)1.0f;

  const int aoff = (rh * 32 + c) * 128 + ((g ^ (c >> 1)) * 16);
  const int boff = 65536 + g * 1024 + (c >> 2) * 128 + ((hw ^ (((c >> 3) & 1) | ((g & 1) << 1))) * 32) + (c & 3) * 8;
  bf16x8 fa[2][2], fb[2][3];
  auto rdA = [&](int n, int bu) {
    const char* sl = smem + (n >> 1) * 8192 + (aoff ^ ((n & 1) << 6));
    fa[bu][0] = *(const bf16x8*)(sl);
    fa[bu][1] = *(const bf16x8*)(sl + 2048);
  };
  auto rdB = [&](int n, int bu, int p) { fb[bu][p] = ldB(smem + boff + (n % 7) * 12288 + p * 4096, 0, 512); };
  if (wv < 4) wait_vm(k1_gcnt(5, 1) + k1_younger(0, 1)); else wait_vm(k1_gcnt(5, 0) + k1_younger(0, 0));
  bar();
  STAMP(8);
  rdA(0, 0); rdB(0, 0, 0); rdB(0, 0, 1); rdB(0, 0, 2);
#pragma unroll
  for (int n = 0; n < 16; ++n) {
    const int cu = n & 1, nx = cu ^ 1;
    if (n < 15) {
      if (wv < 4) wait_vm(k1_younger(n + 1, 1)); else wait_vm(k1_younger(n + 1, 0));
      bar();
    }
    acc[0][0] = XMFMA(fb[cu][0], fa[cu][0], acc[0][0]); SB;
    if (XRD && n < 15) rdA(n + 1, nx);
    SB;
    acc[1][0] = XMFMA(fb[cu][0], fa[cu][1], acc[1][0]); SB;
    if (XRD && n < 15) rdB(n + 1, nx, 0);
    SB;
    acc[0][1] = XMFMA(fa[cu][0], fb[cu][1], acc[0][1]); SB;
    if (XRD && n < 15) rdB(n + 1, nx, 1);
    SB;
    acc[1][1] = XMFMA(fa[cu][1], fb[cu][1], acc[1][1]); SB;
    if (XRD && n < 15) rdB(n + 1, nx, 2);
    SB;
    acc[0][2] = XMFMA(fa[cu][0], fb[cu][2], acc[0][2]); SB;
    if (XLD && n + 6 < 16) issue1(n + 6, 0);
    SB;
    acc[1][2] = XMFMA(fa[cu][1], fb[cu][2], acc[1][2]); SB;
    if (XLD && n + 6 < 16) issue1(n + 6, 1);
    SB;
    if (hw == 0) stt = XMFMA(ones, fa[cu][0], stt);
    else if (hw == 1) stt = XMFMA(fa[cu][0], fa[cu][0], stt);
    else if (hw == 2) stt = XMFMA(ones, fa[cu][1], stt);
    else stt = XMFMA(fa[cu][1], fa[cu][1], stt);
    SB;
    if (XLD && n + 6 < 16) issue1(n + 6, 2);
    SB;
    if (cu == rh) {
      csum[0] = XMFMA(ones, fb[cu][0], csum[0]);
      csum[1] = XMFMA(ones, fb[cu][1], csum[1]);
      csum[2] = XMFMA(ones, fb[cu][2], csum[2]);
    }
  }
  STAMP(6);
  float* tsum = (float*)(smem + K1_RING);
  float* tsq = tsum + 64;
  float* tcs = tsum + 128;
  float* tst = tsum + 512;
  {
    const int row = rh * 32 + (hw >> 1) * 16 + c;
    if ((hw & 1) == 0) { if (g == 0) tsum[row] = stt[0]; }
    else if (g == (c >> 2)) tsq[row] = (c & 2) ? ((c & 1) ? stt[3] : stt[2]) : ((c & 1) ? stt[1] : stt[0]);
    if (g == 0) {
#pragma unroll
      for (int p = 0; p < 3; ++p) tcs[rh * 192 + p * 64 + hw * 16 + c] = csum[p][0];
    }
  }
  __syncthreads();
  if (tid < 64) {
    const float mean = tsum[tid] * (1.0f / 512.0f);
    const float var = fmaxf(tsq[tid] * (1.0f / 512.0f) - mean * mean, 0.f);
    const float sb = bfr(sqrtf(bfr(bfr(var) + 1e-5f)));
    *(f32x2*)(tst + tid * 2) = (f32x2){bfr(mean), 1.0f / sb};
  }
  __syncthreads();
  const f32x4 csq = *(const f32x4*)(tcs + hw * 16 + 4 * g) + *(const f32x4*)(tcs + 192 + hw * 16 + 4 * g);
  const float csk = tcs[64 + hw * 16 + c] + tcs[192 + 64 + hw * 16 + c];
  const float csv = tcs[128 + hw * 16 + c] + tcs[192 + 128 + hw * 16 + c];
#pragma unroll
  for (int i = 0; i < 2; ++i) {
    const f32x2 st = *(const f32x2*)(tst + (rh * 32 + i * 16 + c) * 2);
    bf16x4 o;
#pragma unroll
    for (int r = 0; r < 4; ++r) o[r] = (__bf16)phi(bfr(st[1] * (acc[i][0][r] - st[0] * csq[r])));
    const int rt = mt * 4 + rh * 2 + i;
    *(bf16x4*)(pq + ((size_t)(rt * 16 + hq * 2 + (hw >> 1)) * 64 + lane) * 8 + (hw & 1) * 4) = o;
  }
  STAMP(7);
  {
    bf16x8 pk, vv;
#pragma unroll
    for (int i = 0; i < 2; ++i)
#pragma unroll
      for (int r = 0; r < 4; r += 2) {
        const f32x4 st = *(const f32x4*)(tst + (rh * 32 + i * 16 + 4 * g + r) * 2);
        pk[i * 4 + r] = (__bf16)phi(bfr(st[1] * (acc[i][1][r] - st[0] * csk)));
        pk[i * 4 + r + 1] = (__bf16)phi(bfr(st[3] * (acc[i][1][r + 1] - st[2] * csk)));
        vv[i * 4 + r] = (__bf16)(st[1] * (acc[i][2][r] - st[0] * csv));
        vv[i * 4 + r + 1] = (__bf16)(st[3] * (acc[i][2][r + 1] - st[2] * csv));
      }
    const f32x4 z4 = {0.f, 0.f, 0.f, 0.f};
    f32x4 kv = MFMA(pk, vv, z4);
    f32x4 zz = MFMA(pk, ones, z4);
    float* dst = kvpart + (size_t)((mt * 2 + rh) * 32 + hq * 4 + hw) * KVREC;
    *(f32x4*)(dst + lane * 4) = kv;
    if (c == 0) *(f32x4*)(dst + 256 + g * 4) = zz;
  }
  if (b == 0 && tid < 32) cnt[tid * 32] = 0u;
  STAMP(2); STAMP_OUT(0, pq);
}

__global__ __launch_bounds__(256) void k1b_reduce(const float* kvpart, __bf16* kvf, const __bf16* wo, const __bf16* wg,
                                                  const __bf16* wu, const __bf16* wd, float* csp) {
  __shared__ __attribute__((aligned(16))) float red[32 * 64];
  STAMP_DECL; STAMP(0);
  const int tid = threadIdx.x, b = blockIdx.x, lane = tid & 63, wv = __builtin_amdgcn_readfirstlane(tid >> 6);
  if (b < 34) {
    const int q = b * 64 + lane;
    f32x4 v[16];
#pragma unroll
    for (int m = 0; m < 16; ++m) v[m] = *(const f32x4*)(kvpart + (size_t)(wv * 16 + m) * 32 * KVREC + q * 4);
    f32x4 s = v[0];
#pragma unroll
    for (int m = 1; m < 16; ++m) s += v[m];
    f32x4* r4 = (f32x4*)red;
    r4[tid] = s;
    __syncthreads();
    if (tid < 64) {
      f32x4 t = (r4[tid] + r4[64 + tid]) + (r4[128 + tid] + r4[192 + tid]);
      bf16x4 o;
#pragma unroll
      for (int j = 0; j < 4; ++j) o[j] = (__bf16)t[j];
      *(bf16x4*)(kvf + q * 4) = o;
    }
    STAMP(1); STAMP_OUT(1, (const char*)kvpart - (10u << 20));
    return;
  }
  if (b >= 77) {
    const int pb = b - 77;
    const u32x4* src = pb < 32 ? (const u32x4*)wo + (size_t)pb * 1024 : (const u32x4*)wd + (size_t)(pb - 32) * 1024;
    u32x4 v0 = src[tid], v1 = src[256 + tid], v2 = src[512 + tid], v3 = src[768 + tid];
    asm volatile("" :: "v"(v0), "v"(v1), "v"(v2), "v"(v3));
    return;
  }
  const int cb = b - 34;
  const int col0 = cb * 64 + (tid & 7) * 8;
  const __bf16* src = col0 < F_ ? (wg + col0) : (wu + (col0 - F_));
  const int kq = tid >> 3;
  bf16x8 v[16];
#pragma unroll
  for (int r = 0; r < 16; ++r) v[r] = *(const bf16x8*)(src + (size_t)(kq * 16 + r) * F_);
  float s[8];
#pragma unroll
  for (int j = 0; j < 8; ++j) s[j] = 0.f;
#pragma unroll
  for (int r = 0; r < 16; ++r)
#pragma unroll
    for (int j = 0; j < 8; ++j) s[j] += (float)v[r][j];
#pragma unroll
  for (int j = 0; j < 8; ++j) red[kq * 64 + (tid & 7) * 8 + j] = s[j];
  __syncthreads();
  if (tid < 64) {
    float t = 0.f;
#pragma unroll
    for (int q = 0; q < 32; ++q) t += red[q * 64 + tid];
    csp[cb * 64 + tid] = t;
  }
  STAMP(2); STAMP_OUT(1, (const char*)kvpart - (10u << 20));
}

constexpr int B3_RING = 6 * 12288;
constexpr int R_OFF = B3_RING;
constexpr int p3_wait(int n, int lo) {
  const int gs = lo ? 3 : 2;
  int c = 0;
  if (n + 1 <= 5) {
    c = (n + 1 == 1) ? 6 : 5 - (n + 1);
    c += gs * n;
  } else {
    for (int j = n - 4; j <= n - 1; ++j) c += (j <= 9) ? gs : 0;
  }
  return c;
}
DI void group_wait(unsigned* cnt) {
  unsigned spins = 0;
  while ((unsigned)__builtin_amdgcn_readfirstlane(__hip_atomic_load(cnt, __ATOMIC_RELAXED, __HIP_MEMORY_SCOPE_AGENT)) < 16u) {
    __builtin_amdgcn_s_sleep(1);
    if (++spins > (1u << 22)) break;
  }
}
__global__ __launch_bounds__(512, 2) void k234(const __bf16* x, const __bf16* wo, const __bf16* pq, const __bf16* kvf,
                                               const __bf16* wg, const __bf16* wu, const float* csp, const __bf16* wd,
                                               __bf16* hfr, float* hstat, __bf16* hh, unsigned* cnt, __bf16* out) {
  __shared__ __attribute__((aligned(16))) char smem[163840];
  STAMP_DECL; STAMP(0);
  const int tid = threadIdx.x, lane = tid & 63, wv = __builtin_amdgcn_readfirstlane(tid >> 6);
  const int g = lane >> 4, c = lane & 15;
  const int b = blockIdx.x, xcd = b & 7, idx = b >> 3;
  const int grp = xcd * 2 + (idx >> 4), mem = idx & 15;
  const unsigned lds0 = (unsigned)(size_t)smem;
  const int mt = grp * 2 + (mem >> 3), nt = mem & 7;
  const int ft0 = 2 * ((mem * 43) >> 4);
  const unsigned woff = (lane >> 3) * (D_ * 2) + ((((lane & 7) >> 1) ^ ((lane >> 4) & 3)) * 32) + (lane & 1) * 16;
  const int brow = (g & 1) * 4 + (c >> 2);
  const int bsw = (brow >> 1) & 3;
  const int btr = (g >> 1) * 1024 + brow * 128 + (c & 3) * 8;

  const unsigned boffl = (lane >> 2) * (F_ * 2) + ((((lane >> 1) & 1) ^ ((lane >> 4) & 1)) * 32) + (lane & 1) * 16;
  const __bf16* pB0 = (wv < 6 ? wg : wu) + (size_t)((wv & 1) * 16) * F_ + (ft0 + ((wv % 6) >> 1) * 2) * 16;
  const __bf16* pB1 = wu + (size_t)((wv & 1) * 16) * F_ + (ft0 + ((2 + (wv & 3)) >> 1) * 2) * 16;
  auto issueB3 = [&](int n, int e) {
    const unsigned slot = lds0 + (n % 6) * 12288;
    if (e == 0) glds16s(pB0 + (size_t)n * 32 * F_, boffl, slot + wv * 1024);
    if (e == 1) { if (wv < 4) glds16s(pB1 + (size_t)n * 32 * F_, boffl, slot + 8192 + wv * 1024); }
  };

  {
    const int rg = wv & 3, kh = wv >> 2;
    const int rt = mt * 4 + rg;
    bf16x8 pf[8];
#pragma unroll
    for (int h = 0; h < 8; ++h) gload16(pf[h], (const char*)pq + (size_t)(rt * 16 + kh * 8 + h) * 1024, lane * 16);
#pragma unroll
    for (int i = 0; i < 3; ++i) {
      const int ins = i * 8 + wv;
      if (ins < 17) glds16s((const char*)kvf + ins * 1024, lane * 16, lds0 + R_OFF + 65536 + ins * 1024);
    }
#pragma unroll
    for (int i = 0; i < 8; ++i) {
      const int kb = i * 8 + wv;
      glds16s(wo + (size_t)kb * 8 * D_ + nt * 64, woff, lds0 + R_OFF + kb * 1024);
    }
    bf16x4 xr[2];
#pragma unroll
    for (int p = 0; p < 2; ++p) gload8(xr[p], x + (size_t)(rt * 16) * D_ + nt * 64 + (kh * 2 + p) * 16, c * (D_ * 2) + g * 8);
#pragma unroll
    for (int n = 0; n < 6; ++n) { issueB3(n, 0); issueB3(n, 1); }
    f32x4 acc[4];
#pragma unroll
    for (int j = 0; j < 4; ++j) acc[j] = (f32x4){0.f, 0.f, 0.f, 0.f};
    STAMP(1);
    if (wv < 4) WAIT_VM(14); else WAIT_VM(8);
    bar();
    STAMP(2);
    const char* bl = smem + R_OFF + kh * 32768 + btr;
    const char* kvl = smem + R_OFF + 65536 + kh * 16 * (KVREC * 2);
    const f32x4 z4 = {0.f, 0.f, 0.f, 0.f};
#pragma unroll
    for (int h = 0; h < 8; ++h) {
      TIE(pf[h]);
      bf16x4 k0 = *(const bf16x4*)(kvl + (2 * h) * (KVREC * 2) + lane * 8);
      bf16x4 k1 = *(const bf16x4*)(kvl + (2 * h + 1) * (KVREC * 2) + lane * 8);
      bf16x4 z0 = *(const bf16x4*)(kvl + (2 * h) * (KVREC * 2) + 512 + g * 8);
      bf16x4 z1 = *(const bf16x4*)(kvl + (2 * h + 1) * (KVREC * 2) + 512 + g * 8);
      const bf16x4 zz = {(__bf16)0.f, (__bf16)0.f, (__bf16)0.f, (__bf16)0.f};
      bf16x8 a0 = __builtin_shufflevector(k0, zz, 0, 1, 2, 3, 4, 5, 6, 7);
      bf16x8 a1 = __builtin_shufflevector(zz, k1, 0, 1, 2, 3, 4, 5, 6, 7);
      bf16x8 d0 = __builtin_shufflevector(z0, zz, 0, 1, 2, 3, 4, 5, 6, 7);
      bf16x8 d1 = __builtin_shufflevector(zz, z1, 0, 1, 2, 3, 4, 5, 6, 7);
      f32x4 n0 = MFMA(a0, pf[h], z4);
      f32x4 n1 = MFMA(a1, pf[h], z4);
      f32x4 e0 = MFMA(d0, pf[h], z4);
      f32x4 e1 = MFMA(d1, pf[h], z4);
      const float r0 = __builtin_amdgcn_rcpf(bfr(bfr(e0[0]) + 1e-6f));
      const float r1 = __builtin_amdgcn_rcpf(bfr(bfr(e1[0]) + 1e-6f));
      bf16x8 of;
#pragma unroll
      for (int r = 0; r < 4; ++r) {
        of[r] = (__bf16)(bfr(n0[r]) * r0);
        of[4 + r] = (__bf16)(bfr(n1[r]) * r1);
      }
#pragma unroll
      for (int ct = 0; ct < 4; ++ct) {
        bf16x8 wf = ldB(bl + ((ct ^ bsw) * 32), h * 4096, h * 4096 + 2048);
        acc[ct] = MFMA(wf, of, acc[ct]);
      }
    }
    STAMP(3);
    if (wv < 4) WAIT_VM(12); else WAIT_VM(6);
    bar();
    TIE(xr[0]); TIE(xr[1]);
    f32x4* red = (f32x4*)(smem + R_OFF);
    red[((rg * 2 + kh) * 2 + 0) * 64 + lane] = kh ? acc[0] : acc[2];
    red[((rg * 2 + kh) * 2 + 1) * 64 + lane] = kh ? acc[1] : acc[3];
    asm volatile("s_waitcnt lgkmcnt(0)" ::: "memory");
    bar();
    f32x4 f0 = (kh ? acc[2] : acc[0]), f1 = (kh ? acc[3] : acc[1]);
    {
      f32x4 o0 = red[((rg * 2 + (kh ^ 1)) * 2 + 0) * 64 + lane], o1 = red[((rg * 2 + (kh ^ 1)) * 2 + 1) * 64 + lane];
      f0 = kh ? (o0 + f0) : (f0 + o0);
      f1 = kh ? (o1 + f1) : (f1 + o1);
    }
    float hv[8];
    float s = 0.f;
#pragma unroll
    for (int r = 0; r < 4; ++r) {
      hv[r] = bfr((float)xr[0][r] + bfr(f0[r]));
      hv[4 + r] = bfr((float)xr[1][r] + bfr(f1[r]));
      s += hv[r] + hv[4 + r];
    }
    {
      bf16x8 o;
#pragma unroll
      for (int r = 0; r < 8; ++r) o[r] = (__bf16)hv[r];
      gstore16_sc1((char*)hfr + (size_t)(rt * 16 + nt * 2 + kh) * 1024, lane * 16, o);
    }
    s += __shfl_xor(s, 16, 64);
    s += __shfl_xor(s, 32, 64);
    const float mean = s * (1.0f / 32.0f);
    float m2 = 0.f;
#pragma unroll
    for (int r = 0; r < 8; ++r) { float d = hv[r] - mean; m2 += d * d; }
    m2 += __shfl_xor(m2, 16, 64);
    m2 += __shfl_xor(m2, 32, 64);
    if (g == 0) gstore8f_sc1((char*)hstat + (size_t)(rt * 16) * 128 + (nt * 2 + kh) * 8, c * 128, (f32x2){s, m2});
    WAIT_VM(0);
    bar();
    if (tid == 0) __hip_atomic_fetch_add(cnt + grp * 32, 1u, __ATOMIC_RELAXED, __HIP_MEMORY_SCOPE_AGENT);
    STAMP(4);
  }
  {
    if (wv == 0) group_wait(cnt + grp * 32);
    WAIT_VM(0);
    bar();
    STAMP(5);
    const int mw = wv & 3, nw = wv >> 2;
    const int rt0 = grp * 8 + mw * 2;
    const char* pA = (const char*)hfr + (size_t)(grp * 8 + wv) * 16384;
    auto issueA = [&](int n) { glds16s_sc1(pA + n * 1024, lane * 16, lds0 + R_OFF + (n % 6) * 8192 + wv * 1024); };
    issueA(0); issueA(1);
    glds16s_sc1((const char*)hstat + (size_t)grp * 16384 + wv * 2048, lane * 16, lds0 + R_OFF + 49152 + wv * 2048);
    glds16s_sc1((const char*)hstat + (size_t)grp * 16384 + wv * 2048 + 1024, lane * 16, lds0 + R_OFF + 49152 + wv * 2048 + 1024);
    issueA(2); issueA(3); issueA(4); issueA(5);

    f32x4 acc[2][6];
#pragma unroll
    for (int i = 0; i < 2; ++i)
#pragma unroll
      for (int j = 0; j < 6; ++j) acc[i][j] = (f32x4){0.f, 0.f, 0.f, 0.f};
    bf16x8 fa[2][2], fb[2][6];
    const int bl0 = (4 * g + (c >> 2)) * 64 + (c & 3) * 8;
    auto rdA = [&](int n, int bu) {
      const char* sl = smem + R_OFF + (n % 6) * 8192 + lane * 16;
      fa[bu][0] = *(const bf16x8*)(sl + (mw * 2) * 1024);
      fa[bu][1] = *(const bf16x8*)(sl + (mw * 2 + 1) * 1024);
    };
    auto rdB = [&](int n, int bu, int ct) {
      const int ftl = nw * 3 + (ct >> 1);
      const char* bp = smem + (n % 6) * 12288 + bl0 + ((ct & 1) * 6 + (ftl >> 1) * 2) * 1024 + (((ftl & 1) ^ (g & 1)) * 32);
      fb[bu][ct] = ldB(bp, 0, 1024);
    };
    WAIT_VM(7);
    bar();
    STAMP(6);
    rdA(0, 0);
#pragma unroll
    for (int ct = 0; ct < 6; ++ct) rdB(0, 0, ct);
    float mb[2], rs[2];
    f32x4 cs[6];
#pragma unroll
    for (int n = 0; n < 16; ++n) {
      if (n < 15) {
        if (wv < 4) wait_vm(p3_wait(n, 1)); else wait_vm(p3_wait(n, 0));
        bar();
      }
      if (n == 2) {
#pragma unroll
        for (int i = 0; i < 2; ++i) {
          const char* sp = smem + R_OFF + 49152 + ((mw * 2 + i) * 16 + c) * 128 + g * 32;
          const f32x4 s0 = *(const f32x4*)(sp), s1 = *(const f32x4*)(sp + 16);
          float s = (s0[0] + s0[2]) + (s1[0] + s1[2]);
          s += __shfl_xor(s, 16, 64);
          s += __shfl_xor(s, 32, 64);
          const float mean = s * (1.0f / 512.0f);
          const float d0 = s0[0] * (1.0f / 32.0f) - mean, d1 = s0[2] * (1.0f / 32.0f) - mean;
          const float d2 = s1[0] * (1.0f / 32.0f) - mean, d3 = s1[2] * (1.0f / 32.0f) - mean;
          float m2 = (s0[1] + s0[3]) + (s1[1] + s1[3]) + 32.0f * ((d0 * d0 + d1 * d1) + (d2 * d2 + d3 * d3));
          m2 += __shfl_xor(m2, 16, 64);
          m2 += __shfl_xor(m2, 32, 64);
          mb[i] = bfr(mean);
          rs[i] = 1.0f / bfr(sqrtf(bfr(bfr(m2 * (1.0f / 512.0f)) + 1e-5f)));
        }
      }
      if (n == 10) {
#pragma unroll
        for (int ct = 0; ct < 6; ++ct)
          cs[ct] = *(const f32x4*)(csp + (ct & 1) * F_ + (ft0 + nw * 3 + (ct >> 1)) * 16 + g * 4);
      }
#pragma unroll
      for (int ct = 0; ct < 6; ++ct) {
        acc[0][ct] = MFMA(fb[n & 1][ct], fa[n & 1][0], acc[0][ct]);
        SB;
        if (n < 15) { if (ct == 0) rdA(n + 1, (n + 1) & 1); rdB(n + 1, (n + 1) & 1, ct); }
        SB;
        acc[1][ct] = MFMA(fb[n & 1][ct], fa[n & 1][1], acc[1][ct]);
        SB;
        if (n + 6 < 16) { if (ct == 1) issueA(n + 6); if (ct == 2) issueB3(n + 6, 0); if (ct == 3) issueB3(n + 6, 1); }
        SB;
      }
    }
    STAMP(7);
#pragma unroll
    for (int i = 0; i < 2; ++i) {
#pragma unroll
      for (int j = 0; j < 3; ++j) {
        const int ft = ft0 + nw * 3 + j;
        bf16x4 o;
#pragma unroll
        for (int r = 0; r < 4; ++r) {
          float gg = bfr(rs[i] * (acc[i][2 * j][r] - mb[i] * cs[2 * j][r]));
          float uu = bfr(rs[i] * (acc[i][2 * j + 1][r] - mb[i] * cs[2 * j + 1][r]));
          o[r] = (__bf16)(gg * uu);
        }
        gstore8_sc1((char*)hh + (size_t)((rt0 + i) * 43 + (ft >> 1)) * 1024 + (ft & 1) * 8, lane * 16, o);
      }
    }
    WAIT_VM(0);
    bar();
    if (tid == 0) __hip_atomic_fetch_add(cnt + (16 + grp) * 32, 1u, __ATOMIC_RELAXED, __HIP_MEMORY_SCOPE_AGENT);
    STAMP(8);
  }
  {
    const int mi = wv & 1, kq = wv >> 1;
    const int rt0 = mt * 4 + mi * 2;
    const int u0 = kq * 11;
    const int fi = kq >> 1, fc = (kq & 1) * 2;
    const __bf16* wsrc = wd + (size_t)(u0 * 32 + mi * 16) * D_ + nt * 64;
    const unsigned ldsq = lds0 + kq * 40960 + mi * 2048;
    auto issueB = [&](int s) {
      const int ss = (s == 10 && kq == 3) ? 9 : s;
      glds16s(wsrc + (size_t)(ss * 32) * D_, woff, ldsq + (s % 10) * 4096);
      glds16s(wsrc + (size_t)(ss * 32 + 8) * D_, woff, ldsq + (s % 10) * 4096 + 1024);
    };
    if (wv == 0) { group_wait(cnt + (16 + grp) * 32); WAIT_VM(0); }
#pragma unroll
    for (int s = 0; s < 10; ++s) issueB(s);
    bar();
    STAMP(9);
    bf16x8 af[2][11];
    const char* asrc = (const char*)hh + (size_t)(rt0 * 43 + u0) * 1024;
#pragma unroll
    for (int s = 0; s < 11; ++s) {
      const int ss = (s == 10 && kq == 3) ? 9 : s;
      gload16_sc1(af[0][s], asrc + ss * 1024, lane * 16);
      gload16_sc1(af[1][s], asrc + (43 + ss) * 1024, lane * 16);
    }
    f32x4 acc[2][4];
#pragma unroll
    for (int i = 0; i < 2; ++i)
#pragma unroll
      for (int j = 0; j < 4; ++j) acc[i][j] = (f32x4){0.f, 0.f, 0.f, 0.f};
    const char* bl = smem + kq * 40960 + btr;
    auto comp = [&](int s) {
      TIE(af[0][s]); TIE(af[1][s]);
#pragma unroll
      for (int ct = 0; ct < 4; ++ct) {
        bf16x8 bf = ldB(bl + ((ct ^ bsw) * 32), (s % 10) * 4096, (s % 10) * 4096 + 2048);
        acc[0][ct] = MFMA(bf, af[0][s], acc[0][ct]);
        acc[1][ct] = MFMA(bf, af[1][s], acc[1][ct]);
      }
    };
    WAIT_VM(18);
    bar();
    STAMP(10);
    comp(0); comp(1);
    WAIT_VM(12);
    bar();
    issueB(10);
    bf16x8 hres;
    gload16_sc1(hres, (const char*)hfr + (size_t)((rt0 + fi) * 16 + nt * 2 + (kq & 1)) * 1024, lane * 16);
    comp(2); comp(3); comp(4);
    WAIT_VM(9);
    comp(5); comp(6); comp(7);
    WAIT_VM(1);
    bar();
    comp(8); comp(9);
    if (kq < 3) comp(10);
    STAMP(11);
    WAIT_VM(0);
    TIE(hres);
    bar();
    f32x4* red = (f32x4*)smem;
#pragma unroll
    for (int i = 0; i < 2; ++i)
#pragma unroll
      for (int ct = 0; ct < 4; ++ct)
        if (i != fi || (ct >> 1) != (kq & 1)) red[(((mi * 4 + kq) * 2 + i) * 4 + ct) * 64 + lane] = acc[i][ct];
    __syncthreads();
#pragma unroll
    for (int p = 0; p < 2; ++p) {
      const int ct = fc + p;
      const f32x4 own = fi ? (fc ? acc[1][2 + p] : acc[1][p]) : (fc ? acc[0][2 + p] : acc[0][p]);
      f32x4 v[4];
#pragma unroll
      for (int q = 0; q < 4; ++q) v[q] = q == kq ? own : red[(((mi * 4 + q) * 2 + fi) * 4 + ct) * 64 + lane];
      f32x4 s = (v[0] + v[1]) + (v[2] + v[3]);
      bf16x4 o;
#pragma unroll
      for (int r = 0; r < 4; ++r) o[r] = (__bf16)((float)hres[p * 4 + r] + bfr(s[r]));
      *(bf16x4*)(out + (size_t)((rt0 + fi) * 16 + c) * D_ + nt * 64 + ct * 16 + g * 4) = o;
    }
  }
  STAMP(12); STAMP_OUT(2, pq);
}

extern "C" void kernel_launch(void* const* d_in, const int* in_sizes, int n_in, void* d_out, int out_size,
                              void* d_ws, size_t ws_size, hipStream_t stream) {
  const __bf16* x = (const __bf16*)d_in[0];
  const __bf16* wqkv = (const __bf16*)d_in[1];
  const __bf16* wo = (const __bf16*)d_in[2];
  const __bf16* wg = (const __bf16*)d_in[4];
  const __bf16* wu = (const __bf16*)d_in[5];
  const __bf16* wd = (const __bf16*)d_in[6];
  char* ws = (char*)d_ws;
  __bf16* pq = (__bf16*)(ws);
  __bf16* hfr = (__bf16*)(ws + (2u << 20));
  __bf16* hh = (__bf16*)(ws + (4u << 20));
  float* kvpart = (float*)(ws + (10u << 20));
  __bf16* kvf = (__bf16*)(ws + (13u << 20));
  float* csp = (float*)(ws + (13u << 20) + 65536);
  float* hstat = (float*)(ws + (14u << 20));
  unsigned* cnt = (unsigned*)(ws + (13u << 20) + 131072);
  k1_qkv<<<256, 512, 0, stream>>>(x, wqkv, pq, kvpart, cnt);
  k1b_reduce<<<77 + 118, 256, 0, stream>>>(kvpart, kvf, wo, wg, wu, wd, csp);
  k234<<<256, 512, 0, stream>>>(x, wo, pq, kvf, wg, wu, csp, wd, hfr, hstat, hh, cnt, (__bf16*)d_out);
}
```

```cpp
#include <hip/hip_runtime.h>
#include <hip/hip_bf16.h>
#include <stdint.h>

typedef __attribute__((ext_vector_type(8))) __bf16 bf16x8;
typedef __attribute__((ext_vector_type(4))) __bf16 bf16x4;
typedef __attribute__((ext_vector_type(4))) short s16x4;
typedef __attribute__((ext_vector_type(4))) float f32x4;
typedef __attribute__((ext_vector_type(2))) float f32x2;
typedef __attribute__((ext_vector_type(4))) unsigned u32x4;
typedef __attribute__((ext_vector_type(2))) unsigned u32x2;
typedef __attribute__((address_space(3))) s16x4* lds_s16x4_ptr;

#define DI __device__ __forceinline__
#define MFMA(a, b, c) __builtin_amdgcn_mfma_f32_16x16x32_bf16((a), (b), (c), 0, 0, 0)

constexpr int S_ = 2048, D_ = 512, F_ = 1376;
constexpr int KVREC = 272;

#define STAMP_DECL
#define STAMP(i)
#define STAMP_OUT(k, wsbase)
DI float bfr(float x) { return (float)(__bf16)x; }
DI float bf2f(__bf16 x) { return (float)x; }

DI void glds16(const void* gsrc, unsigned lds_dst) {
  unsigned keep;
  asm volatile(
      "s_mov_b32 %0, m0\n\ts_mov_b32 m0, %2\n\ts_nop 0\n\tglobal_load_lds_dwordx4 %1, off\n\ts_mov_b32 m0, %0"
      : "=&s"(keep)
      : "v"(gsrc), "s"(lds_dst)
      : "memory");
}
DI void glds16s(const void* ubase, unsigned voff, unsigned lds_dst) {
  unsigned keep;
  asm volatile(
      "s_mov_b32 %0, m0\n\ts_mov_b32 m0, %3\n\ts_nop 0\n\tglobal_load_lds_dwordx4 %1, %2\n\ts_mov_b32 m0, %0"
      : "=&s"(keep)
      : "v"(voff), "s"(ubase), "s"(lds_dst)
      : "memory");
}
DI void gload16(bf16x8& dst, const void* ubase, unsigned voff) {
  asm volatile("global_load_dwordx4 %0, %1, %2" : "=v"(dst) : "v"(voff), "s"(ubase) : "memory");
}
DI void gload8(bf16x4& dst, const void* ubase, unsigned voff) {
  asm volatile("global_load_dwordx2 %0, %1, %2" : "=v"(dst) : "v"(voff), "s"(ubase) : "memory");
}
DI void gload16_sc1(bf16x8& dst, const void* ubase, unsigned voff) {
  asm volatile("global_load_dwordx4 %0, %1, %2 sc1" : "=v"(dst) : "v"(voff), "s"(ubase) : "memory");
}
DI void glds16s_sc1(const void* ubase, unsigned voff, unsigned lds_dst) {
  unsigned keep;
  asm volatile(
      "s_mov_b32 %0, m0\n\ts_mov_b32 m0, %3\n\ts_nop 0\n\tglobal_load_lds_dwordx4 %1, %2 sc1\n\ts_mov_b32 m0, %0"
      : "=&s"(keep)
      : "v"(voff), "s"(ubase), "s"(lds_dst)
      : "memory");
}
DI void gstore16_sc1(void* ubase, unsigned voff, bf16x8 v) {
  asm volatile("global_store_dwordx4 %0, %1, %2 sc1\n\ts_nop 1" :: "v"(voff), "v"(v), "s"(ubase) : "memory");
}
DI void gstore8_sc1(void* ubase, unsigned voff, bf16x4 v) {
  asm volatile("global_store_dwordx2 %0, %1, %2 sc1\n\ts_nop 1" :: "v"(voff), "v"(v), "s"(ubase) : "memory");
}
DI void gstore8f_sc1(void* ubase, unsigned voff, f32x2 v) {
  asm volatile("global_store_dwordx2 %0, %1, %2 sc1\n\ts_nop 1" :: "v"(voff), "v"(v), "s"(ubase) : "memory");
}
#define TIE(x) asm volatile("" : "+v"(x))
#define WAIT_VM(n) do { __builtin_amdgcn_sched_barrier(0); asm volatile("s_waitcnt vmcnt(" #n ") lgkmcnt(0)" ::: "memory"); } while (0)
DI void wait_vm(int n) {
  switch (n) {
    case 0: WAIT_VM(0); break;   case 1: WAIT_VM(1); break;   case 2: WAIT_VM(2); break;
    case 3: WAIT_VM(3); break;   case 4: WAIT_VM(4); break;   case 5: WAIT_VM(5); break;
    case 6: WAIT_VM(6); break;   case 7: WAIT_VM(7); break;   case 8: WAIT_VM(8); break;
    case 9: WAIT_VM(9); break;   case 10: WAIT_VM(10); break; case 11: WAIT_VM(11); break;
    case 12: WAIT_VM(12); break; case 13: WAIT_VM(13); break; case 14: WAIT_VM(14); break;
    case 15: WAIT_VM(15); break; case 16: WAIT_VM(16); break; case 17: WAIT_VM(17); break;
    case 18: WAIT_VM(18); break; case 19: WAIT_VM(19); break; case 20: WAIT_VM(20); break;
    case 21: WAIT_VM(21); break; case 22: WAIT_VM(22); break; case 23: WAIT_VM(23); break;
    case 24: WAIT_VM(24); break; case 25: WAIT_VM(25); break; case 26: WAIT_VM(26); break;
    case 27: WAIT_VM(27); break; case 28: WAIT_VM(28); break; case 29: WAIT_VM(29); break;
    case 30: WAIT_VM(30); break; case 31: WAIT_VM(31); break; case 32: WAIT_VM(32); break;
    case 33: WAIT_VM(33); break; case 34: WAIT_VM(34); break; case 35: WAIT_VM(35); break;
    case 36: WAIT_VM(36); break; case 37: WAIT_VM(37); break; case 38: WAIT_VM(38); break;
    case 39: WAIT_VM(39); break; case 40: WAIT_VM(40); break; case 41: WAIT_VM(41); break;
    case 42: WAIT_VM(42); break; case 43: WAIT_VM(43); break; case 44: WAIT_VM(44); break;
    default: WAIT_VM(0); break;
  }
}
#define SB __builtin_amdgcn_sched_barrier(0)
#define XMFMA MFMA
#define XRD 1
#define XLD 1
DI void bar() { __builtin_amdgcn_s_barrier(); __builtin_amdgcn_sched_barrier(0); }

DI bf16x8 ldB(const char* smem_lane_base, int off0, int off1) {
  s16x4 lo = __builtin_amdgcn_ds_read_tr16_b64_v4i16((lds_s16x4_ptr)(smem_lane_base + off0));
  s16x4 hi = __builtin_amdgcn_ds_read_tr16_b64_v4i16((lds_s16x4_ptr)(smem_lane_base + off1));
  typedef __attribute__((ext_vector_type(8))) short s16x8;
  s16x8 v = __builtin_shufflevector(lo, hi, 0, 1, 2, 3, 4, 5, 6, 7);
  return __builtin_bit_cast(bf16x8, v);
}

DI float phi(float t) {
  float r = t > 0.f ? bfr(t + 1.0f) : bfr(__expf(t));
  return bfr(r + 1e-6f);
}

constexpr int K1_RING = 65536 + 7 * 12288;
constexpr int K1_LDS = K1_RING + 4096;
constexpr int k1_gcnt(int n, int lo) { return n > 15 ? 0 : ((lo ? 2 : 1) + ((n & 1) == 0 ? 1 : 0)); }
constexpr int k1_younger(int n, int lo) {
  return k1_gcnt(n + 1, lo) + k1_gcnt(n + 2, lo) + k1_gcnt(n + 3, lo) + k1_gcnt(n + 4, lo);
}
__global__ __launch_bounds__(512, 2) void k1_qkv(const __bf16* x, const __bf16* w, __bf16* pq, float* kvpart, unsigned* cnt) {
  __shared__ __attribute__((aligned(16))) char smem[K1_LDS];
  STAMP_DECL; STAMP(0);
  const int tid = threadIdx.x, lane = tid & 63, wv = __builtin_amdgcn_readfirstlane(tid >> 6);
  const int g = lane >> 4, c = lane & 15;
  const int hw = wv & 3, rh = wv >> 2;
  const int b = blockIdx.x, xcd = b & 7, idx = b >> 3;
  const int mt = (xcd >> 1) * 8 + (idx >> 2), hq = (xcd & 1) * 4 + (idx & 3);
  const unsigned lds0 = (unsigned)(size_t)smem;

  const __bf16* xsrc = x + (size_t)(mt * 64 + wv * 8) * D_;
  const __bf16* wsrc0 = w + (size_t)((wv & 3) * 8) * 1536 + (wv >> 2) * 512 + hq * 64;
  const __bf16* wsrc1 = w + (size_t)((wv & 3) * 8) * 1536 + 1024 + hq * 64;
  unsigned xoff, woff;
  {
    const int r = lane >> 3, pos = lane & 7;
    const int row = wv * 8 + r;
    xoff = r * (D_ * 2) + (pos ^ ((row >> 1) & 7)) * 16;
    const int swz = ((r >> 1) & 1) | ((wv & 1) << 1);
    woff = r * 3072 + ((pos >> 1) ^ swz) * 32 + (pos & 1) * 16;
  }
  const int rot = 0;
  auto issue1 = [&](int n, int e) {
    const unsigned slot = lds0 + 65536 + (n % 7) * 12288;
    const int kn = (n + rot) & 15;
    if (e == 0) glds16s(wsrc0 + (size_t)kn * 32 * 1536, woff, slot + wv * 1024);
    if (e == 1) { if (wv < 4) glds16s(wsrc1 + (size_t)kn * 32 * 1536, woff, slot + 8192 + wv * 1024); }
    if (e == 2) { if ((n & 1) == 0) glds16s(xsrc + (kn >> 1) * 64, xoff, lds0 + (n >> 1) * 8192 + wv * 1024); }
  };
#pragma unroll
  for (int n = 0; n < 6; ++n) { issue1(n, 0); issue1(n, 1); issue1(n, 2); }
  STAMP(1);

  f32x4 acc[2][3];
#pragma unroll
  for (int i = 0; i < 2; ++i)
#pragma unroll
    for (int j = 0; j < 3; ++j) acc[i][j] = (f32x4){0.f, 0.f, 0.f, 0.f};
  f32x4 stt = {0.f, 0.f, 0.f, 0.f};
  f32x4 csum[3];
#pragma unroll
  for (int j = 0; j < 3; ++j) csum[j] = (f32x4){0.f, 0.f, 0.f, 0.f};
  bf16x8 ones;
#pragma unroll
  for (int j = 0; j < 8; ++j) ones[j] = (__bf16)1.0f;

  const int aoff = (rh * 32 + c) * 128 + ((g ^ (c >> 1)) * 16);
  const int boff = 65536 + g * 1024 + (c >> 2) * 128 + ((hw ^ (((c >> 3) & 1) | ((g & 1) << 1))) * 32) + (c & 3) * 8;
  bf16x8 fa[2][2], fb[2][3];
  auto rdA = [&](int n, int bu) {
    const char* sl = smem + (n >> 1) * 8192 + (aoff ^ ((n & 1) << 6));
    fa[bu][0] = *(const bf16x8*)(sl);
    fa[bu][1] = *(const bf16x8*)(sl + 2048);
  };
  auto rdB = [&](int n, int bu, int p) { fb[bu][p] = ldB(smem + boff + (n % 7) * 12288 + p * 4096, 0, 512); };
  if (wv < 4) wait_vm(k1_gcnt(5, 1) + k1_younger(0, 1)); else wait_vm(k1_gcnt(5, 0) + k1_younger(0, 0));
  bar();
  STAMP(8);
  rdA(0, 0); rdB(0, 0, 0); rdB(0, 0, 1); rdB(0, 0, 2);
#pragma unroll
  for (int n = 0; n < 16; ++n) {
    const int cu = n & 1, nx = cu ^ 1;
    if (n < 15) {
      if (wv < 4) wait_vm(k1_younger(n + 1, 1)); else wait_vm(k1_younger(n + 1, 0));
      bar();
    }
    acc[0][0] = XMFMA(fb[cu][0], fa[cu][0], acc[0][0]); SB;
    if (XRD && n < 15) rdA(n + 1, nx);
    SB;
    acc[1][0] = XMFMA(fb[cu][0], fa[cu][1], acc[1][0]); SB;
    if (XRD && n < 15) rdB(n + 1, nx, 0);
    SB;
    acc[0][1] = XMFMA(fa[cu][0], fb[cu][1], acc[0][1]); SB;
    if (XRD && n < 15) rdB(n + 1, nx, 1);
    SB;
    acc[1][1] = XMFMA(fa[cu][1], fb[cu][1], acc[1][1]); SB;
    if (XRD && n < 15) rdB(n + 1, nx, 2);
    SB;
    acc[0][2] = XMFMA(fa[cu][0], fb[cu][2], acc[0][2]); SB;
    if (XLD && n + 6 < 16) issue1(n + 6, 0);
    SB;
    acc[1][2] = XMFMA(fa[cu][1], fb[cu][2], acc[1][2]); SB;
    if (XLD && n + 6 < 16) issue1(n + 6, 1);
    SB;
    if (hw == 0) stt = XMFMA(ones, fa[cu][0], stt);
    else if (hw == 1) stt = XMFMA(fa[cu][0], fa[cu][0], stt);
    else if (hw == 2) stt = XMFMA(ones, fa[cu][1], stt);
    else stt = XMFMA(fa[cu][1], fa[cu][1], stt);
    SB;
    if (XLD && n + 6 < 16) issue1(n + 6, 2);
    SB;
    if (cu == rh) {
      csum[0] = XMFMA(ones, fb[cu][0], csum[0]);
      csum[1] = XMFMA(ones, fb[cu][1], csum[1]);
      csum[2] = XMFMA(ones, fb[cu][2], csum[2]);
    }
  }
  STAMP(6);
  float* tsum = (float*)(smem + K1_RING);
  float* tsq = tsum + 64;
  float* tcs = tsum + 128;
  float* tst = tsum + 512;
  {
    const int row = rh * 32 + (hw >> 1) * 16 + c;
    if ((hw & 1) == 0) { if (g == 0) tsum[row] = stt[0]; }
    else if (g == (c >> 2)) tsq[row] = (c & 2) ? ((c & 1) ? stt[3] : stt[2]) : ((c & 1) ? stt[1] : stt[0]);
    if (g == 0) {
#pragma unroll
      for (int p = 0; p < 3; ++p) tcs[rh * 192 + p * 64 + hw * 16 + c] = csum[p][0];
    }
  }
  __syncthreads();
  if (tid < 64) {
    const float mean = tsum[tid] * (1.0f / 512.0f);
    const float var = fmaxf(tsq[tid] * (1.0f / 512.0f) - mean * mean, 0.f);
    const float sb = bfr(sqrtf(bfr(bfr(var) + 1e-5f)));
    *(f32x2*)(tst + tid * 2) = (f32x2){bfr(mean), 1.0f / sb};
  }
  __syncthreads();
  const f32x4 csq = *(const f32x4*)(tcs + hw * 16 + 4 * g) + *(const f32x4*)(tcs + 192 + hw * 16 + 4 * g);
  const float csk = tcs[64 + hw * 16 + c] + tcs[192 + 64 + hw * 16 + c];
  const float csv = tcs[128 + hw * 16 + c] + tcs[192 + 128 + hw * 16 + c];
#pragma unroll
  for (int i = 0; i < 2; ++i) {
    const f32x2 st = *(const f32x2*)(tst + (rh * 32 + i * 16 + c) * 2);
    bf16x4 o;
#pragma unroll
    for (int r = 0; r < 4; ++r) o[r] = (__bf16)phi(bfr(st[1] * (acc[i][0][r] - st[0] * csq[r])));
    const int rt = mt * 4 + rh * 2 + i;
    *(bf16x4*)(pq + ((size_t)(rt * 16 + hq * 2 + (hw >> 1)) * 64 + lane) * 8 + (hw & 1) * 4) = o;
  }
  STAMP(7);
  {
    bf16x8 pk, vv;
#pragma unroll
    for (int i = 0; i < 2; ++i)
#pragma unroll
      for (int r = 0; r < 4; r += 2) {
        const f32x4 st = *(const f32x4*)(tst + (rh * 32 + i * 16 + 4 * g + r) * 2);
        pk[i * 4 + r] = (__bf16)phi(bfr(st[1] * (acc[i][1][r] - st[0] * csk)));
        pk[i * 4 + r + 1] = (__bf16)phi(bfr(st[3] * (acc[i][1][r + 1] - st[2] * csk)));
        vv[i * 4 + r] = (__bf16)(st[1] * (acc[i][2][r] - st[0] * csv));
        vv[i * 4 + r + 1] = (__bf16)(st[3] * (acc[i][2][r + 1] - st[2] * csv));
      }
    const f32x4 z4 = {0.f, 0.f, 0.f, 0.f};
    f32x4 kv = MFMA(pk, vv, z4);
    f32x4 zz = MFMA(pk, ones, z4);
    float* dst = kvpart + (size_t)((mt * 2 + rh) * 32 + hq * 4 + hw) * KVREC;
    *(f32x4*)(dst + lane * 4) = kv;
    if (c == 0) *(f32x4*)(dst + 256 + g * 4) = zz;
  }
  if (b == 0 && tid < 32) cnt[tid * 32] = 0u;
  STAMP(2); STAMP_OUT(0, pq);
}

__global__ __launch_bounds__(256) void k1b_reduce(const float* kvpart, __bf16* kvf, const __bf16* wo, const __bf16* wg,
                                                  const __bf16* wu, const __bf16* wd, float* csp) {
  __shared__ __attribute__((aligned(16))) float red[32 * 64];
  STAMP_DECL; STAMP(0);
  const int tid = threadIdx.x, b = blockIdx.x, lane = tid & 63, wv = __builtin_amdgcn_readfirstlane(tid >> 6);
  if (b < 34) {
    const int q = b * 64 + lane;
    f32x4 v[16];
#pragma unroll
    for (int m = 0; m < 16; ++m) v[m] = *(const f32x4*)(kvpart + (size_t)(wv * 16 + m) * 32 * KVREC + q * 4);
    f32x4 s = v[0];
#pragma unroll
    for (int m = 1; m < 16; ++m) s += v[m];
    f32x4* r4 = (f32x4*)red;
    r4[tid] = s;
    __syncthreads();
    if (tid < 64) {
      f32x4 t = (r4[tid] + r4[64 + tid]) + (r4[128 + tid] + r4[192 + tid]);
      bf16x4 o;
#pragma unroll
      for (int j = 0; j < 4; ++j) o[j] = (__bf16)t[j];
      *(bf16x4*)(kvf + q * 4) = o;
    }
    STAMP(1); STAMP_OUT(1, (const char*)kvpart - (10u << 20));
    return;
  }
  if (b >= 77) {
    const int pb = b - 77;
    const u32x4* src = pb < 32 ? (const u32x4*)wo + (size_t)pb * 1024 : (const u32x4*)wd + (size_t)(pb - 32) * 1024;
    u32x4 v0 = src[tid], v1 = src[256 + tid], v2 = src[512 + tid], v3 = src[768 + tid];
    asm volatile("" :: "v"(v0), "v"(v1), "v"(v2), "v"(v3));
    return;
  }
  const int cb = b - 34;
  const int col0 = cb * 64 + (tid & 7) * 8;
  const __bf16* src = col0 < F_ ? (wg + col0) : (wu + (col0 - F_));
  const int kq = tid >> 3;
  bf16x8 v[16];
#pragma unroll
  for (int r = 0; r < 16; ++r) v[r] = *(const bf16x8*)(src + (size_t)(kq * 16 + r) * F_);
  float s[8];
#pragma unroll
  for (int j = 0; j < 8; ++j) s[j] = 0.f;
#pragma unroll
  for (int r = 0; r < 16; ++r)
#pragma unroll
    for (int j = 0; j < 8; ++j) s[j] += (float)v[r][j];
#pragma unroll
  for (int j = 0; j < 8; ++j) red[kq * 64 + (tid & 7) * 8 + j] = s[j];
  __syncthreads();
  if (tid < 64) {
    float t = 0.f;
#pragma unroll
    for (int q = 0; q < 32; ++q) t += red[q * 64 + tid];
    csp[cb * 64 + tid] = t;
  }
  STAMP(2); STAMP_OUT(1, (const char*)kvpart - (10u << 20));
}

constexpr int B3_RING = 6 * 12288;
constexpr int R_OFF = B3_RING;
constexpr int p3_wait(int n, int lo) {
  const int gs = lo ? 3 : 2;
  int c = 0;
  if (n + 1 <= 5) {
    c = (n + 1 == 1) ? 6 : 5 - (n + 1);
    c += gs * n;
  } else {
    for (int j = n - 4; j <= n - 1; ++j) c += (j <= 9) ? gs : 0;
  }
  return c;
}
DI void group_wait(unsigned* cnt) {
  unsigned spins = 0;
  while ((unsigned)__builtin_amdgcn_readfirstlane(__hip_atomic_load(cnt, __ATOMIC_RELAXED, __HIP_MEMORY_SCOPE_AGENT)) < 16u) {
    __builtin_amdgcn_s_sleep(1);
    if (++spins > (1u << 22)) break;
  }
}
__global__ __launch_bounds__(512, 2) void k234(const __bf16* x, const __bf16* wo, const __bf16* pq, const __bf16* kvf,
                                               const __bf16* wg, const __bf16* wu, const float* csp, const __bf16* wd,
                                               __bf16* hfr, float* hstat, __bf16* hh, unsigned* cnt, __bf16* out) {
  __shared__ __attribute__((aligned(16))) char smem[163840];
  STAMP_DECL; STAMP(0);
  const int tid = threadIdx.x, lane = tid & 63, wv = __builtin_amdgcn_readfirstlane(tid >> 6);
  const int g = lane >> 4, c = lane & 15;
  const int b = blockIdx.x, xcd = b & 7, idx = b >> 3;
  const int grp = xcd * 2 + (idx >> 4), mem = idx & 15;
  const unsigned lds0 = (unsigned)(size_t)smem;
  const int mt = grp * 2 + (mem >> 3), nt = mem & 7;
  const int ft0 = 2 * ((mem * 43) >> 4);
  const unsigned woff = (lane >> 3) * (D_ * 2) + ((((lane & 7) >> 1) ^ ((lane >> 4) & 3)) * 32) + (lane & 1) * 16;
  const int brow = (g & 1) * 4 + (c >> 2);
  const int bsw = (brow >> 1) & 3;
  const int btr = (g >> 1) * 1024 + brow * 128 + (c & 3) * 8;

  const unsigned boffl = (lane >> 2) * (F_ * 2) + ((((lane >> 1) & 1) ^ ((lane >> 4) & 1)) * 32) + (lane & 1) * 16;
  const __bf16* pB0 = (wv < 6 ? wg : wu) + (size_t)((wv & 1) * 16) * F_ + (ft0 + ((wv % 6) >> 1) * 2) * 16;
  const __bf16* pB1 = wu + (size_t)((wv & 1) * 16) * F_ + (ft0 + ((2 + (wv & 3)) >> 1) * 2) * 16;
  auto issueB3 = [&](int n, int e) {
    const unsigned slot = lds0 + (n % 6) * 12288;
    if (e == 0) glds16s(pB0 + (size_t)n * 32 * F_, boffl, slot + wv * 1024);
    if (e == 1) { if (wv < 4) glds16s(pB1 + (size_t)n * 32 * F_, boffl, slot + 8192 + wv * 1024); }
  };

  {
    const int rg = wv & 3, kh = wv >> 2;
    const int rt = mt * 4 + rg;
    bf16x8 pf[8];
#pragma unroll
    for (int h = 0; h < 8; ++h) gload16(pf[h], (const char*)pq + (size_t)(rt * 16 + kh * 8 + h) * 1024, lane * 16);
#pragma unroll
    for (int i = 0; i < 3; ++i) {
      const int ins = i * 8 + wv;
      if (ins < 17) glds16s((const char*)kvf + ins * 1024, lane * 16, lds0 + R_OFF + 65536 + ins * 1024);
    }
#pragma unroll
    for (int i = 0; i < 8; ++i) {
      const int kb = i * 8 + wv;
      glds16s(wo + (size_t)kb * 8 * D_ + nt * 64, woff, lds0 + R_OFF + kb * 1024);
    }
    bf16x4 xr[2];
#pragma unroll
    for (int p = 0; p < 2; ++p) gload8(xr[p], x + (size_t)(rt * 16) * D_ + nt * 64 + (kh * 2 + p) * 16, c * (D_ * 2) + g * 8);
    f32x4 acc[4];
#pragma unroll
    for (int j = 0; j < 4; ++j) acc[j] = (f32x4){0.f, 0.f, 0.f, 0.f};
    STAMP(1);
    WAIT_VM(2);
    bar();
    STAMP(2);
#pragma unroll
    for (int n = 0; n < 6; ++n) { issueB3(n, 0); issueB3(n, 1); }
    const char* bl = smem + R_OFF + kh * 32768 + btr;
    const char* kvl = smem + R_OFF + 65536 + kh * 16 * (KVREC * 2);
    const f32x4 z4 = {0.f, 0.f, 0.f, 0.f};
#pragma unroll
    for (int h = 0; h < 8; ++h) {
      TIE(pf[h]);
      bf16x4 k0 = *(const bf16x4*)(kvl + (2 * h) * (KVREC * 2) + lane * 8);
      bf16x4 k1 = *(const bf16x4*)(kvl + (2 * h + 1) * (KVREC * 2) + lane * 8);
      bf16x4 z0 = *(const bf16x4*)(kvl + (2 * h) * (KVREC * 2) + 512 + g * 8);
      bf16x4 z1 = *(const bf16x4*)(kvl + (2 * h + 1) * (KVREC * 2) + 512 + g * 8);
      const bf16x4 zz = {(__bf16)0.f, (__bf16)0.f, (__bf16)0.f, (__bf16)0.f};
      bf16x8 a0 = __builtin_shufflevector(k0, zz, 0, 1, 2, 3, 4, 5, 6, 7);
      bf16x8 a1 = __builtin_shufflevector(zz, k1, 0, 1, 2, 3, 4, 5, 6, 7);
      bf16x8 d0 = __builtin_shufflevector(z0, zz, 0, 1, 2, 3, 4, 5, 6, 7);
      bf16x8 d1 = __builtin_shufflevector(zz, z1, 0, 1, 2, 3, 4, 5, 6, 7);
      f32x4 n0 = MFMA(a0, pf[h], z4);
      f32x4 n1 = MFMA(a1, pf[h], z4);
      f32x4 e0 = MFMA(d0, pf[h], z4);
      f32x4 e1 = MFMA(d1, pf[h], z4);
      const float r0 = __builtin_amdgcn_rcpf(bfr(bfr(e0[0]) + 1e-6f));
      const float r1 = __builtin_amdgcn_rcpf(bfr(bfr(e1[0]) + 1e-6f));
      bf16x8 of;
#pragma unroll
      for (int r = 0; r < 4; ++r) {
        of[r] = (__bf16)(bfr(n0[r]) * r0);
        of[4 + r] = (__bf16)(bfr(n1[r]) * r1);
      }
#pragma unroll
      for (int ct = 0; ct < 4; ++ct) {
        bf16x8 wf = ldB(bl + ((ct ^ bsw) * 32), h * 4096, h * 4096 + 2048);
        acc[ct] = MFMA(wf, of, acc[ct]);
      }
    }
    STAMP(3);
    if (wv < 4) WAIT_VM(12); else WAIT_VM(6);
    bar();
    TIE(xr[0]); TIE(xr[1]);
    f32x4* red = (f32x4*)(smem + R_OFF);
    red[((rg * 2 + kh) * 2 + 0) * 64 + lane] = kh ? acc[0] : acc[2];
    red[((rg * 2 + kh) * 2 + 1) * 64 + lane] = kh ? acc[1] : acc[3];
    asm volatile("s_waitcnt lgkmcnt(0)" ::: "memory");
    bar();
    f32x4 f0 = (kh ? acc[2] : acc[0]), f1 = (kh ? acc[3] : acc[1]);
    {
      f32x4 o0 = red[((rg * 2 + (kh ^ 1)) * 2 + 0) * 64 + lane], o1 = red[((rg * 2 + (kh ^ 1)) * 2 + 1) * 64 + lane];
      f0 = kh ? (o0 + f0) : (f0 + o0);
      f1 = kh ? (o1 + f1) : (f1 + o1);
    }
    float hv[8];
    float s = 0.f;
#pragma unroll
    for (int r = 0; r < 4; ++r) {
      hv[r] = bfr((float)xr[0][r] + bfr(f0[r]));
      hv[4 + r] = bfr((float)xr[1][r] + bfr(f1[r]));
      s += hv[r] + hv[4 + r];
    }
    {
      bf16x8 o;
#pragma unroll
      for (int r = 0; r < 8; ++r) o[r] = (__bf16)hv[r];
      gstore16_sc1((char*)hfr + (size_t)(rt * 16 + nt * 2 + kh) * 1024, lane * 16, o);
    }
    s += __shfl_xor(s, 16, 64);
    s += __shfl_xor(s, 32, 64);
    const float mean = s * (1.0f / 32.0f);
    float m2 = 0.f;
#pragma unroll
    for (int r = 0; r < 8; ++r) { float d = hv[r] - mean; m2 += d * d; }
    m2 += __shfl_xor(m2, 16, 64);
    m2 += __shfl_xor(m2, 32, 64);
    if (g == 0) gstore8f_sc1((char*)hstat + (size_t)(rt * 16 + nt * 2 + kh) * 128, c * 8, (f32x2){s, m2});
    WAIT_VM(0);
    bar();
    if (tid == 0) __hip_atomic_fetch_add(cnt + grp * 32, 1u, __ATOMIC_RELAXED, __HIP_MEMORY_SCOPE_AGENT);
    STAMP(4);
  }
  {
    if (wv == 0) group_wait(cnt + grp * 32);
    WAIT_VM(0);
    bar();
    STAMP(5);
    const int mw = wv & 3, nw = wv >> 2;
    const int rt0 = grp * 8 + mw * 2;
    const char* pA = (const char*)hfr + (size_t)(grp * 8 + wv) * 16384;
    auto issueA = [&](int n) { glds16s_sc1(pA + n * 1024, lane * 16, lds0 + R_OFF + (n % 6) * 8192 + wv * 1024); };
    issueA(0); issueA(1);
    glds16s_sc1((const char*)hstat + (size_t)grp * 16384 + wv * 2048, lane * 16, lds0 + R_OFF + 49152 + wv * 2048);
    glds16s_sc1((const char*)hstat + (size_t)grp * 16384 + wv * 2048 + 1024, lane * 16, lds0 + R_OFF + 49152 + wv * 2048 + 1024);
    issueA(2); issueA(3); issueA(4); issueA(5);

    f32x4 acc[2][6];
#pragma unroll
    for (int i = 0; i < 2; ++i)
#pragma unroll
      for (int j = 0; j < 6; ++j) acc[i][j] = (f32x4){0.f, 0.f, 0.f, 0.f};
    bf16x8 fa[2][2], fb[2][6];
    const int bl0 = (4 * g + (c >> 2)) * 64 + (c & 3) * 8;
    auto rdA = [&](int n, int bu) {
      const char* sl = smem + R_OFF + (n % 6) * 8192 + lane * 16;
      fa[bu][0] = *(const bf16x8*)(sl + (mw * 2) * 1024);
      fa[bu][1] = *(const bf16x8*)(sl + (mw * 2 + 1) * 1024);
    };
    auto rdB = [&](int n, int bu, int ct) {
      const int ftl = nw * 3 + (ct >> 1);
      const char* bp = smem + (n % 6) * 12288 + bl0 + ((ct & 1) * 6 + (ftl >> 1) * 2) * 1024 + (((ftl & 1) ^ (g & 1)) * 32);
      fb[bu][ct] = ldB(bp, 0, 1024);
    };
    WAIT_VM(7);
    bar();
    STAMP(6);
    rdA(0, 0);
#pragma unroll
    for (int ct = 0; ct < 6; ++ct) rdB(0, 0, ct);
    float mb[2], rs[2];
    f32x4 cs[6];
#pragma unroll
    for (int n = 0; n < 16; ++n) {
      if (n < 15) {
        if (wv < 4) wait_vm(p3_wait(n, 1)); else wait_vm(p3_wait(n, 0));
        bar();
      }
      if (n == 2) {
#pragma unroll
        for (int i = 0; i < 2; ++i) {
          const char* sp = smem + R_OFF + 49152 + ((mw * 2 + i) * 16 + g * 4) * 128 + c * 8;
          const f32x2 t0 = *(const f32x2*)(sp), t1 = *(const f32x2*)(sp + 128), t2 = *(const f32x2*)(sp + 256), t3 = *(const f32x2*)(sp + 384);
          const f32x4 s0 = {t0[0], t0[1], t1[0], t1[1]}, s1 = {t2[0], t2[1], t3[0], t3[1]};
          float s = (s0[0] + s0[2]) + (s1[0] + s1[2]);
          s += __shfl_xor(s, 16, 64);
          s += __shfl_xor(s, 32, 64);
          const float mean = s * (1.0f / 512.0f);
          const float d0 = s0[0] * (1.0f / 32.0f) - mean, d1 = s0[2] * (1.0f / 32.0f) - mean;
          const float d2 = s1[0] * (1.0f / 32.0f) - mean, d3 = s1[2] * (1.0f / 32.0f) - mean;
          float m2 = (s0[1] + s0[3]) + (s1[1] + s1[3]) + 32.0f * ((d0 * d0 + d1 * d1) + (d2 * d2 + d3 * d3));
          m2 += __shfl_xor(m2, 16, 64);
          m2 += __shfl_xor(m2, 32, 64);
          mb[i] = bfr(mean);
          rs[i] = 1.0f / bfr(sqrtf(bfr(bfr(m2 * (1.0f / 512.0f)) + 1e-5f)));
        }
      }
      if (n == 10) {
#pragma unroll
        for (int ct = 0; ct < 6; ++ct)
          cs[ct] = *(const f32x4*)(csp + (ct & 1) * F_ + (ft0 + nw * 3 + (ct >> 1)) * 16 + g * 4);
      }
#pragma unroll
      for (int ct = 0; ct < 6; ++ct) {
        acc[0][ct] = MFMA(fb[n & 1][ct], fa[n & 1][0], acc[0][ct]);
        SB;
        if (n < 15) { if (ct == 0) rdA(n + 1, (n + 1) & 1); rdB(n + 1, (n + 1) & 1, ct); }
        SB;
        acc[1][ct] = MFMA(fb[n & 1][ct], fa[n & 1][1], acc[1][ct]);
        SB;
        if (n + 6 < 16) { if (ct == 1) issueA(n + 6); if (ct == 2) issueB3(n + 6, 0); if (ct == 3) issueB3(n + 6, 1); }
        SB;
      }
    }
    STAMP(7);
    bar();
#pragma unroll
    for (int i = 0; i < 2; ++i) {
#pragma unroll
      for (int j = 0; j < 3; ++j) {
        const int ftl = nw * 3 + j;
        bf16x4 o;
#pragma unroll
        for (int r = 0; r < 4; ++r) {
          float gg = bfr(rs[i] * (acc[i][2 * j][r] - mb[i] * cs[2 * j][r]));
          float uu = bfr(rs[i] * (acc[i][2 * j + 1][r] - mb[i] * cs[2 * j + 1][r]));
          o[r] = (__bf16)(gg * uu);
        }
        *(bf16x4*)(smem + ((mw * 2 + i) * 3 + (ftl >> 1)) * 1024 + lane * 16 + (ftl & 1) * 8) = o;
      }
    }
    asm volatile("s_waitcnt lgkmcnt(0)" ::: "memory");
    bar();
#pragma unroll
    for (int pr = 0; pr < 3; ++pr) {
      bf16x8 v = *(const bf16x8*)(smem + (wv * 3 + pr) * 1024 + lane * 16);
      gstore16_sc1((char*)hh + (size_t)((grp * 8 + wv) * 43 + (ft0 >> 1) + pr) * 1024, lane * 16, v);
    }
    WAIT_VM(0);
    bar();
    if (tid == 0) __hip_atomic_fetch_add(cnt + (16 + grp) * 32, 1u, __ATOMIC_RELAXED, __HIP_MEMORY_SCOPE_AGENT);
    STAMP(8);
  }
  {
    const int mi = wv & 1, kq = wv >> 1;
    const int rt0 = mt * 4 + mi * 2;
    const int u0 = kq * 11;
    const int fi = kq >> 1, fc = (kq & 1) * 2;
    const __bf16* wsrc = wd + (size_t)(u0 * 32 + mi * 16) * D_ + nt * 64;
    const unsigned ldsq = lds0 + kq * 40960 + mi * 2048;
    auto issueB = [&](int s) {
      const int ss = (s == 10 && kq == 3) ? 9 : s;
      glds16s(wsrc + (size_t)(ss * 32) * D_, woff, ldsq + (s % 10) * 4096);
      glds16s(wsrc + (size_t)(ss * 32 + 8) * D_, woff, ldsq + (s % 10) * 4096 + 1024);
    };
    if (wv == 0) { group_wait(cnt + (16 + grp) * 32); WAIT_VM(0); }
#pragma unroll
    for (int s = 0; s < 10; ++s) issueB(s);
    bar();
    STAMP(9);
    bf16x8 af[2][11];
    const char* asrc = (const char*)hh + (size_t)(rt0 * 43 + u0) * 1024;
#pragma unroll
    for (int s = 0; s < 11; ++s) {
      const int ss = (s == 10 && kq == 3) ? 9 : s;
      gload16_sc1(af[0][s], asrc + ss * 1024, lane * 16);
      gload16_sc1(af[1][s], asrc + (43 + ss) * 1024, lane * 16);
    }
    f32x4 acc[2][4];
#pragma unroll
    for (int i = 0; i < 2; ++i)
#pragma unroll
      for (int j = 0; j < 4; ++j) acc[i][j] = (f32x4){0.f, 0.f, 0.f, 0.f};
    const char* bl = smem + kq * 40960 + btr;
    auto comp = [&](int s) {
      TIE(af[0][s]); TIE(af[1][s]);
#pragma unroll
      for (int ct = 0; ct < 4; ++ct) {
        bf16x8 bf = ldB(bl + ((ct ^ bsw) * 32), (s % 10) * 4096, (s % 10) * 4096 + 2048);
        acc[0][ct] = MFMA(bf, af[0][s], acc[0][ct]);
        acc[1][ct] = MFMA(bf, af[1][s], acc[1][ct]);
      }
    };
    WAIT_VM(18);
    bar();
    STAMP(10);
    comp(0); comp(1);
    WAIT_VM(12);
    bar();
    issueB(10);
    bf16x8 hres;
    gload16_sc1(hres, (const char*)hfr + (size_t)((rt0 + fi) * 16 + nt * 2 + (kq & 1)) * 1024, lane * 16);
    comp(2); comp(3); comp(4);
    WAIT_VM(9);
    comp(5); comp(6); comp(7);
    WAIT_VM(1);
    bar();
    comp(8); comp(9);
    if (kq < 3) comp(10);
    STAMP(11);
    WAIT_VM(0);
    TIE(hres);
    bar();
    f32x4* red = (f32x4*)smem;
#pragma unroll
    for (int i = 0; i < 2; ++i)
#pragma unroll
      for (int ct = 0; ct < 4; ++ct)
        if (i != fi || (ct >> 1) != (kq & 1)) red[(((mi * 4 + kq) * 2 + i) * 4 + ct) * 64 + lane] = acc[i][ct];
    __syncthreads();
#pragma unroll
    for (int p = 0; p < 2; ++p) {
      const int ct = fc + p;
      const f32x4 own = fi ? (fc ? acc[1][2 + p] : acc[1][p]) : (fc ? acc[0][2 + p] : acc[0][p]);
      f32x4 v[4];
#pragma unroll
      for (int q = 0; q < 4; ++q) v[q] = q == kq ? own : red[(((mi * 4 + q) * 2 + fi) * 4 + ct) * 64 + lane];
      f32x4 s = (v[0] + v[1]) + (v[2] + v[3]);
      bf16x4 o;
#pragma unroll
      for (int r = 0; r < 4; ++r) o[r] = (__bf16)((float)hres[p * 4 + r] + bfr(s[r]));
      *(bf16x4*)(out + (size_t)((rt0 + fi) * 16 + c) * D_ + nt * 64 + ct * 16 + g * 4) = o;
    }
  }
  STAMP(12); STAMP_OUT(2, pq);
}

extern "C" void kernel_launch(void* const* d_in, const int* in_sizes, int n_in, void* d_out, int out_size,
                              void* d_ws, size_t ws_size, hipStream_t stream) {
  const __bf16* x = (const __bf16*)d_in[0];
  const __bf16* wqkv = (const __bf16*)d_in[1];
  const __bf16* wo = (const __bf16*)d_in[2];
  const __bf16* wg = (const __bf16*)d_in[4];
  const __bf16* wu = (const __bf16*)d_in[5];
  const __bf16* wd = (const __bf16*)d_in[6];
  char* ws = (char*)d_ws;
  __bf16* pq = (__bf16*)(ws);
  __bf16* hfr = (__bf16*)(ws + (2u << 20));
  __bf16* hh = (__bf16*)(ws + (4u << 20));
  float* kvpart = (float*)(ws + (10u << 20));
  __bf16* kvf = (__bf16*)(ws + (13u << 20));
  float* csp = (float*)(ws + (13u << 20) + 65536);
  float* hstat = (float*)(ws + (14u << 20));
  unsigned* cnt = (unsigned*)(ws + (13u << 20) + 131072);
  k1_qkv<<<256, 512, 0, stream>>>(x, wqkv, pq, kvpart, cnt);
  k1b_reduce<<<77 + 118, 256, 0, stream>>>(kvpart, kvf, wo, wg, wu, wd, csp);
  k234<<<256, 512, 0, stream>>>(x, wo, pq, kvf, wg, wu, csp, wd, hfr, hstat, hh, cnt, (__bf16*)d_out);
}
```

```cpp
#include <hip/hip_runtime.h>
#include <hip/hip_bf16.h>
#include <stdint.h>

typedef __attribute__((ext_vector_type(8))) __bf16 bf16x8;
typedef __attribute__((ext_vector_type(4))) __bf16 bf16x4;
typedef __attribute__((ext_vector_type(4))) short s16x4;
typedef __attribute__((ext_vector_type(4))) float f32x4;
typedef __attribute__((ext_vector_type(2))) float f32x2;
typedef __attribute__((ext_vector_type(4))) unsigned u32x4;
typedef __attribute__((ext_vector_type(2))) unsigned u32x2;
typedef __attribute__((address_space(3))) s16x4* lds_s16x4_ptr;

#define DI __device__ __forceinline__
#define MFMA(a, b, c) __builtin_amdgcn_mfma_f32_16x16x32_bf16((a), (b), (c), 0, 0, 0)

constexpr int S_ = 2048, D_ = 512, F_ = 1376;
constexpr int KVREC = 272;

#define STAMP_DECL
#define STAMP(i)
#define STAMP_OUT(k, wsbase)
DI float bfr(float x) { return (float)(__bf16)x; }
DI float bf2f(__bf16 x) { return (float)x; }

DI void glds16(const void* gsrc, unsigned lds_dst) {
  unsigned keep;
  asm volatile(
      "s_mov_b32 %0, m0\n\ts_mov_b32 m0, %2\n\ts_nop 0\n\tglobal_load_lds_dwordx4 %1, off\n\ts_mov_b32 m0, %0"
      : "=&s"(keep)
      : "v"(gsrc), "s"(lds_dst)
      : "memory");
}
DI void glds16s(const void* ubase, unsigned voff, unsigned lds_dst) {
  unsigned keep;
  asm volatile(
      "s_mov_b32 %0, m0\n\ts_mov_b32 m0, %3\n\ts_nop 0\n\tglobal_load_lds_dwordx4 %1, %2\n\ts_mov_b32 m0, %0"
      : "=&s"(keep)
      : "v"(voff), "s"(ubase), "s"(lds_dst)
      : "memory");
}
DI void gload16(bf16x8& dst, const void* ubase, unsigned voff) {
  asm volatile("global_load_dwordx4 %0, %1, %2" : "=v"(dst) : "v"(voff), "s"(ubase) : "memory");
}
DI void gload8(bf16x4& dst, const void* ubase, unsigned voff) {
  asm volatile("global_load_dwordx2 %0, %1, %2" : "=v"(dst) : "v"(voff), "s"(ubase) : "memory");
}
DI void gload16_sc1(bf16x8& dst, const void* ubase, unsigned voff) {
  asm volatile("global_load_dwordx4 %0, %1, %2 sc1" : "=v"(dst) : "v"(voff), "s"(ubase) : "memory");
}
DI void glds16s_sc1(const void* ubase, unsigned voff, unsigned lds_dst) {
  unsigned keep;
  asm volatile(
      "s_mov_b32 %0, m0\n\ts_mov_b32 m0, %3\n\ts_nop 0\n\tglobal_load_lds_dwordx4 %1, %2 sc1\n\ts_mov_b32 m0, %0"
      : "=&s"(keep)
      : "v"(voff), "s"(ubase), "s"(lds_dst)
      : "memory");
}
DI void gstore16_sc1(void* ubase, unsigned voff, bf16x8 v) {
  asm volatile("global_store_dwordx4 %0, %1, %2 sc1\n\ts_nop 1" :: "v"(voff), "v"(v), "s"(ubase) : "memory");
}
DI void gstore8_sc1(void* ubase, unsigned voff, bf16x4 v) {
  asm volatile("global_store_dwordx2 %0, %1, %2 sc1\n\ts_nop 1" :: "v"(voff), "v"(v), "s"(ubase) : "memory");
}
DI void gstore8f_sc1(void* ubase, unsigned voff, f32x2 v) {
  asm volatile("global_store_dwordx2 %0, %1, %2 sc1\n\ts_nop 1" :: "v"(voff), "v"(v), "s"(ubase) : "memory");
}
#define TIE(x) asm volatile("" : "+v"(x))
#define WAIT_VM(n) do { __builtin_amdgcn_sched_barrier(0); asm volatile("s_waitcnt vmcnt(" #n ") lgkmcnt(0)" ::: "memory"); } while (0)
DI void wait_vm(int n) {
  switch (n) {
    case 0: WAIT_VM(0); break;   case 1: WAIT_VM(1); break;   case 2: WAIT_VM(2); break;
    case 3: WAIT_VM(3); break;   case 4: WAIT_VM(4); break;   case 5: WAIT_VM(5); break;
    case 6: WAIT_VM(6); break;   case 7: WAIT_VM(7); break;   case 8: WAIT_VM(8); break;
    case 9: WAIT_VM(9); break;   case 10: WAIT_VM(10); break; case 11: WAIT_VM(11); break;
    case 12: WAIT_VM(12); break; case 13: WAIT_VM(13); break; case 14: WAIT_VM(14); break;
    case 15: WAIT_VM(15); break; case 16: WAIT_VM(16); break; case 17: WAIT_VM(17); break;
    case 18: WAIT_VM(18); break; case 19: WAIT_VM(19); break; case 20: WAIT_VM(20); break;
    case 21: WAIT_VM(21); break; case 22: WAIT_VM(22); break; case 23: WAIT_VM(23); break;
    case 24: WAIT_VM(24); break; case 25: WAIT_VM(25); break; case 26: WAIT_VM(26); break;
    case 27: WAIT_VM(27); break; case 28: WAIT_VM(28); break; case 29: WAIT_VM(29); break;
    case 30: WAIT_VM(30); break; case 31: WAIT_VM(31); break; case 32: WAIT_VM(32); break;
    case 33: WAIT_VM(33); break; case 34: WAIT_VM(34); break; case 35: WAIT_VM(35); break;
    case 36: WAIT_VM(36); break; case 37: WAIT_VM(37); break; case 38: WAIT_VM(38); break;
    case 39: WAIT_VM(39); break; case 40: WAIT_VM(40); break; case 41: WAIT_VM(41); break;
    case 42: WAIT_VM(42); break; case 43: WAIT_VM(43); break; case 44: WAIT_VM(44); break;
    default: WAIT_VM(0); break;
  }
}
#define SB __builtin_amdgcn_sched_barrier(0)
#define XMFMA MFMA
#define XRD 1
#define XLD 1
DI void bar() { __builtin_amdgcn_s_barrier(); __builtin_amdgcn_sched_barrier(0); }

DI bf16x8 ldB(const char* smem_lane_base, int off0, int off1) {
  s16x4 lo = __builtin_amdgcn_ds_read_tr16_b64_v4i16((lds_s16x4_ptr)(smem_lane_base + off0));
  s16x4 hi = __builtin_amdgcn_ds_read_tr16_b64_v4i16((lds_s16x4_ptr)(smem_lane_base + off1));
  typedef __attribute__((ext_vector_type(8))) short s16x8;
  s16x8 v = __builtin_shufflevector(lo, hi, 0, 1, 2, 3, 4, 5, 6, 7);
  return __builtin_bit_cast(bf16x8, v);
}

DI float phi(float t) {
  float r = t > 0.f ? bfr(t + 1.0f) : bfr(__expf(t));
  return bfr(r + 1e-6f);
}

constexpr int K1_RING = 65536 + 7 * 12288;
constexpr int K1_LDS = K1_RING + 4096;
constexpr int k1_gcnt(int n, int lo) { return n > 15 ? 0 : ((lo ? 2 : 1) + ((n & 1) == 0 ? 1 : 0)); }
constexpr int k1_younger(int n, int lo) {
  return k1_gcnt(n + 1, lo) + k1_gcnt(n + 2, lo) + k1_gcnt(n + 3, lo) + k1_gcnt(n + 4, lo);
}
__global__ __launch_bounds__(512, 2) void k1_qkv(const __bf16* x, const __bf16* w, __bf16* pq, float* kvpart, unsigned* cnt) {
  __shared__ __attribute__((aligned(16))) char smem[K1_LDS];
  STAMP_DECL; STAMP(0);
  const int tid = threadIdx.x, lane = tid & 63, wv = __builtin_amdgcn_readfirstlane(tid >> 6);
  const int g = lane >> 4, c = lane & 15;
  const int hw = wv & 3, rh = wv >> 2;
  const int b = blockIdx.x, xcd = b & 7, idx = b >> 3;
  const int mt = (xcd >> 1) * 8 + (idx >> 2), hq = (xcd & 1) * 4 + (idx & 3);
  const unsigned lds0 = (unsigned)(size_t)smem;

  const __bf16* xsrc = x + (size_t)(mt * 64 + wv * 8) * D_;
  const __bf16* wsrc0 = w + (size_t)((wv & 3) * 8) * 1536 + (wv >> 2) * 512 + hq * 64;
  const __bf16* wsrc1 = w + (size_t)((wv & 3) * 8) * 1536 + 1024 + hq * 64;
  unsigned xoff, woff;
  {
    const int r = lane >> 3, pos = lane & 7;
    const int row = wv * 8 + r;
    xoff = r * (D_ * 2) + (pos ^ ((row >> 1) & 7)) * 16;
    const int swz = ((r >> 1) & 1) | ((wv & 1) << 1);
    woff = r * 3072 + ((pos >> 1) ^ swz) * 32 + (pos & 1) * 16;
  }
  const int rot = 0;
  auto issue1 = [&](int n, int e) {
    const unsigned slot = lds0 + 65536 + (n % 7) * 12288;
    const int kn = (n + rot) & 15;
    if (e == 0) glds16s(wsrc0 + (size_t)kn * 32 * 1536, woff, slot + wv * 1024);
    if (e == 1) { if (wv < 4) glds16s(wsrc1 + (size_t)kn * 32 * 1536, woff, slot + 8192 + wv * 1024); }
    if (e == 2) { if ((n & 1) == 0) glds16s(xsrc + (kn >> 1) * 64, xoff, lds0 + (n >> 1) * 8192 + wv * 1024); }
  };
#pragma unroll
  for (int n = 0; n < 6; ++n) { issue1(n, 0); issue1(n, 1); issue1(n, 2); }
  STAMP(1);

  f32x4 acc[2][3];
#pragma unroll
  for (int i = 0; i < 2; ++i)
#pragma unroll
    for (int j = 0; j < 3; ++j) acc[i][j] = (f32x4){0.f, 0.f, 0.f, 0.f};
  f32x4 stt = {0.f, 0.f, 0.f, 0.f};
  f32x4 csum[3];
#pragma unroll
  for (int j = 0; j < 3; ++j) csum[j] = (f32x4){0.f, 0.f, 0.f, 0.f};
  bf16x8 ones;
#pragma unroll
  for (int j = 0; j < 8; ++j) ones[j] = (__bf16)1.0f;

  const int aoff = (rh * 32 + c) * 128 + ((g ^ (c >> 1)) * 16);
  const int boff = 65536 + g * 1024 + (c >> 2) * 128 + ((hw ^ (((c >> 3) & 1) | ((g & 1) << 1))) * 32) + (c & 3) * 8;
  bf16x8 fa[2][2], fb[2][3];
  auto rdA = [&](int n, int bu) {
    const char* sl = smem + (n >> 1) * 8192 + (aoff ^ ((n & 1) << 6));
    fa[bu][0] = *(const bf16x8*)(sl);
    fa[bu][1] = *(const bf16x8*)(sl + 2048);
  };
  auto rdB = [&](int n, int bu, int p) { fb[bu][p] = ldB(smem + boff + (n % 7) * 12288 + p * 4096, 0, 512); };
  if (wv < 4) wait_vm(k1_gcnt(5, 1) + k1_younger(0, 1)); else wait_vm(k1_gcnt(5, 0) + k1_younger(0, 0));
  bar();
  STAMP(8);
  rdA(0, 0); rdB(0, 0, 0); rdB(0, 0, 1); rdB(0, 0, 2);
#pragma unroll
  for (int n = 0; n < 16; ++n) {
    const int cu = n & 1, nx = cu ^ 1;
    if (n < 15) {
      if (wv < 4) wait_vm(k1_younger(n + 1, 1)); else wait_vm(k1_younger(n + 1, 0));
      bar();
    }
    acc[0][0] = XMFMA(fb[cu][0], fa[cu][0], acc[0][0]); SB;
    if (XRD && n < 15) rdA(n + 1, nx);
    SB;
    acc[1][0] = XMFMA(fb[cu][0], fa[cu][1], acc[1][0]); SB;
    if (XRD && n < 15) rdB(n + 1, nx, 0);
    SB;
    acc[0][1] = XMFMA(fa[cu][0], fb[cu][1], acc[0][1]); SB;
    if (XRD && n < 15) rdB(n + 1, nx, 1);
    SB;
    acc[1][1] = XMFMA(fa[cu][1], fb[cu][1], acc[1][1]); SB;
    if (XRD && n < 15) rdB(n + 1, nx, 2);
    SB;
    acc[0][2] = XMFMA(fa[cu][0], fb[cu][2], acc[0][2]); SB;
    if (XLD && n + 6 < 16) issue1(n + 6, 0);
    SB;
    acc[1][2] = XMFMA(fa[cu][1], fb[cu][2], acc[1][2]); SB;
    if (XLD && n + 6 < 16) issue1(n + 6, 1);
    SB;
    if (hw == 0) stt = XMFMA(ones, fa[cu][0], stt);
    else if (hw == 1) stt = XMFMA(fa[cu][0], fa[cu][0], stt);
    else if (hw == 2) stt = XMFMA(ones, fa[cu][1], stt);
    else stt = XMFMA(fa[cu][1], fa[cu][1], stt);
    SB;
    if (XLD && n + 6 < 16) issue1(n + 6, 2);
    SB;
    if (cu == rh) {
      csum[0] = XMFMA(ones, fb[cu][0], csum[0]);
      csum[1] = XMFMA(ones, fb[cu][1], csum[1]);
      csum[2] = XMFMA(ones, fb[cu][2], csum[2]);
    }
  }
  STAMP(6);
  float* tsum = (float*)(smem + K1_RING);
  float* tsq = tsum + 64;
  float* tcs = tsum + 128;
  float* tst = tsum + 512;
  {
    const int row = rh * 32 + (hw >> 1) * 16 + c;
    if ((hw & 1) == 0) { if (g == 0) tsum[row] = stt[0]; }
    else if (g == (c >> 2)) tsq[row] = (c & 2) ? ((c & 1) ? stt[3] : stt[2]) : ((c & 1) ? stt[1] : stt[0]);
    if (g == 0) {
#pragma unroll
      for (int p = 0; p < 3; ++p) tcs[rh * 192 + p * 64 + hw * 16 + c] = csum[p][0];
    }
  }
  __syncthreads();
  if (tid < 64) {
    const float mean = tsum[tid] * (1.0f / 512.0f);
    const float var = fmaxf(tsq[tid] * (1.0f / 512.0f) - mean * mean, 0.f);
    const float sb = bfr(sqrtf(bfr(bfr(var) + 1e-5f)));
    *(f32x2*)(tst + tid * 2) = (f32x2){bfr(mean), 1.0f / sb};
  }
  __syncthreads();
  const f32x4 csq = *(const f32x4*)(tcs + hw * 16 + 4 * g) + *(const f32x4*)(tcs + 192 + hw * 16 + 4 * g);
  const float csk = tcs[64 + hw * 16 + c] + tcs[192 + 64 + hw * 16 + c];
  const float csv = tcs[128 + hw * 16 + c] + tcs[192 + 128 + hw * 16 + c];
#pragma unroll
  for (int i = 0; i < 2; ++i) {
    const f32x2 st = *(const f32x2*)(tst + (rh * 32 + i * 16 + c) * 2);
    bf16x4 o;
#pragma unroll
    for (int r = 0; r < 4; ++r) o[r] = (__bf16)phi(bfr(st[1] * (acc[i][0][r] - st[0] * csq[r])));
    const int rt = mt * 4 + rh * 2 + i;
    *(bf16x4*)(pq + ((size_t)(rt * 16 + hq * 2 + (hw >> 1)) * 64 + lane) * 8 + (hw & 1) * 4) = o;
  }
  STAMP(7);
  {
    bf16x8 pk, vv;
#pragma unroll
    for (int i = 0; i < 2; ++i)
#pragma unroll
      for (int r = 0; r < 4; r += 2) {
        const f32x4 st = *(const f32x4*)(tst + (rh * 32 + i * 16 + 4 * g + r) * 2);
        pk[i * 4 + r] = (__bf16)phi(bfr(st[1] * (acc[i][1][r] - st[0] * csk)));
        pk[i * 4 + r + 1] = (__bf16)phi(bfr(st[3] * (acc[i][1][r + 1] - st[2] * csk)));
        vv[i * 4 + r] = (__bf16)(st[1] * (acc[i][2][r] - st[0] * csv));
        vv[i * 4 + r + 1] = (__bf16)(st[3] * (acc[i][2][r + 1] - st[2] * csv));
      }
    const f32x4 z4 = {0.f, 0.f, 0.f, 0.f};
    f32x4 kv = MFMA(pk, vv, z4);
    f32x4 zz = MFMA(pk, ones, z4);
    float* dst = kvpart + (size_t)((mt * 2 + rh) * 32 + hq * 4 + hw) * KVREC;
    *(f32x4*)(dst + lane * 4) = kv;
    if (c == 0) *(f32x4*)(dst + 256 + g * 4) = zz;
  }
  if (b == 0 && tid < 32) cnt[tid * 32] = 0u;
  STAMP(2); STAMP_OUT(0, pq);
}

__global__ __launch_bounds__(256) void k1b_reduce(const float* kvpart, __bf16* kvf, const __bf16* wo, const __bf16* wg,
                                                  const __bf16* wu, const __bf16* wd, float* csp) {
  __shared__ __attribute__((aligned(16))) float red[32 * 64];
  STAMP_DECL; STAMP(0);
  const int tid = threadIdx.x, b = blockIdx.x, lane = tid & 63, wv = __builtin_amdgcn_readfirstlane(tid >> 6);
  if (b < 34) {
    const int q = b * 64 + lane;
    f32x4 v[16];
#pragma unroll
    for (int m = 0; m < 16; ++m) v[m] = *(const f32x4*)(kvpart + (size_t)(wv * 16 + m) * 32 * KVREC + q * 4);
    f32x4 s = v[0];
#pragma unroll
    for (int m = 1; m < 16; ++m) s += v[m];
    f32x4* r4 = (f32x4*)red;
    r4[tid] = s;
    __syncthreads();
    if (tid < 64) {
      f32x4 t = (r4[tid] + r4[64 + tid]) + (r4[128 + tid] + r4[192 + tid]);
      bf16x4 o;
#pragma unroll
      for (int j = 0; j < 4; ++j) o[j] = (__bf16)t[j];
      *(bf16x4*)(kvf + q * 4) = o;
    }
    STAMP(1); STAMP_OUT(1, (const char*)kvpart - (10u << 20));
    return;
  }
  if (b >= 77) {
    const int pb = b - 77;
    const u32x4* src = pb < 32 ? (const u32x4*)wo + (size_t)pb * 1024 : (const u32x4*)wd + (size_t)(pb - 32) * 1024;
    u32x4 v0 = src[tid], v1 = src[256 + tid], v2 = src[512 + tid], v3 = src[768 + tid];
    asm volatile("" :: "v"(v0), "v"(v1), "v"(v2), "v"(v3));
    return;
  }
  const int cb = b - 34;
  const int col0 = cb * 64 + (tid & 7) * 8;
  const __bf16* src = col0 < F_ ? (wg + col0) : (wu + (col0 - F_));
  const int kq = tid >> 3;
  bf16x8 v[16];
#pragma unroll
  for (int r = 0; r < 16; ++r) v[r] = *(const bf16x8*)(src + (size_t)(kq * 16 + r) * F_);
  float s[8];
#pragma unroll
  for (int j = 0; j < 8; ++j) s[j] = 0.f;
#pragma unroll
  for (int r = 0; r < 16; ++r)
#pragma unroll
    for (int j = 0; j < 8; ++j) s[j] += (float)v[r][j];
#pragma unroll
  for (int j = 0; j < 8; ++j) red[kq * 64 + (tid & 7) * 8 + j] = s[j];
  __syncthreads();
  if (tid < 64) {
    float t = 0.f;
#pragma unroll
    for (int q = 0; q < 32; ++q) t += red[q * 64 + tid];
    csp[cb * 64 + tid] = t;
  }
  STAMP(2); STAMP_OUT(1, (const char*)kvpart - (10u << 20));
}

constexpr int B3_RING = 6 * 12288;
constexpr int R_OFF = B3_RING;
constexpr int p3_wait(int n, int lo) {
  const int gs = lo ? 3 : 2;
  int c = 0;
  if (n + 1 <= 5) {
    c = (n + 1 == 1) ? 6 : 5 - (n + 1);
    c += gs * n;
  } else {
    for (int j = n - 4; j <= n - 1; ++j) c += (j <= 9) ? gs : 0;
  }
  return c;
}
DI void group_wait(unsigned* cnt) {
  unsigned spins = 0;
  while ((unsigned)__builtin_amdgcn_readfirstlane(__hip_atomic_load(cnt, __ATOMIC_RELAXED, __HIP_MEMORY_SCOPE_AGENT)) < 16u) {
    __builtin_amdgcn_s_sleep(1);
    if (++spins > (1u << 22)) break;
  }
}
__global__ __launch_bounds__(512, 2) void k234(const __bf16* x, const __bf16* wo, const __bf16* pq, const __bf16* kvf,
                                               const __bf16* wg, const __bf16* wu, const float* csp, const __bf16* wd,
                                               __bf16* hfr, float* hstat, __bf16* hh, unsigned* cnt, __bf16* out) {
  __shared__ __attribute__((aligned(16))) char smem[163840];
  STAMP_DECL; STAMP(0);
  const int tid = threadIdx.x, lane = tid & 63, wv = __builtin_amdgcn_readfirstlane(tid >> 6);
  const int g = lane >> 4, c = lane & 15;
  const int b = blockIdx.x, xcd = b & 7, idx = b >> 3;
  const int grp = xcd * 2 + (idx >> 4), mem = idx & 15;
  const unsigned lds0 = (unsigned)(size_t)smem;
  const int mt = grp * 2 + (mem >> 3), nt = mem & 7;
  const int ft0 = 2 * ((mem * 43) >> 4);
  const unsigned woff = (lane >> 3) * (D_ * 2) + ((((lane & 7) >> 1) ^ ((lane >> 4) & 3)) * 32) + (lane & 1) * 16;
  const int brow = (g & 1) * 4 + (c >> 2);
  const int bsw = (brow >> 1) & 3;
  const int btr = (g >> 1) * 1024 + brow * 128 + (c & 3) * 8;

  const unsigned boffl = (lane >> 2) * (F_ * 2) + ((((lane >> 1) & 1) ^ ((lane >> 4) & 1)) * 32) + (lane & 1) * 16;
  const __bf16* pB0 = (wv < 6 ? wg : wu) + (size_t)((wv & 1) * 16) * F_ + (ft0 + ((wv % 6) >> 1) * 2) * 16;
  const __bf16* pB1 = wu + (size_t)((wv & 1) * 16) * F_ + (ft0 + ((2 + (wv & 3)) >> 1) * 2) * 16;
  auto issueB3 = [&](int n, int e) {
    const unsigned slot = lds0 + (n % 6) * 12288;
    if (e == 0) glds16s(pB0 + (size_t)n * 32 * F_, boffl, slot + wv * 1024);
    if (e == 1) { if (wv < 4) glds16s(pB1 + (size_t)n * 32 * F_, boffl, slot + 8192 + wv * 1024); }
  };

  {
    const int rg = wv & 3, kh = wv >> 2;
    const int rt = mt * 4 + rg;
    bf16x8 pf[8];
#pragma unroll
    for (int h = 0; h < 8; ++h) gload16(pf[h], (const char*)pq + (size_t)(rt * 16 + kh * 8 + h) * 1024, lane * 16);
#pragma unroll
    for (int i = 0; i < 3; ++i) {
      const int ins = i * 8 + wv;
      if (ins < 17) glds16s((const char*)kvf + ins * 1024, lane * 16, lds0 + R_OFF + 65536 + ins * 1024);
    }
#pragma unroll
    for (int i = 0; i < 8; ++i) {
      const int kb = i * 8 + wv;
      glds16s(wo + (size_t)kb * 8 * D_ + nt * 64, woff, lds0 + R_OFF + kb * 1024);
    }
    bf16x4 xr[2];
#pragma unroll
    for (int p = 0; p < 2; ++p) gload8(xr[p], x + (size_t)(rt * 16) * D_ + nt * 64 + (kh * 2 + p) * 16, c * (D_ * 2) + g * 8);
    f32x4 acc[4];
#pragma unroll
    for (int j = 0; j < 4; ++j) acc[j] = (f32x4){0.f, 0.f, 0.f, 0.f};
    STAMP(1);
    WAIT_VM(2);
    bar();
    STAMP(2);
    const char* bl = smem + R_OFF + kh * 32768 + btr;
    const char* kvl = smem + R_OFF + 65536 + kh * 16 * (KVREC * 2);
    const f32x4 z4 = {0.f, 0.f, 0.f, 0.f};
#pragma unroll
    for (int h = 0; h < 8; ++h) {
      TIE(pf[h]);
      bf16x4 k0 = *(const bf16x4*)(kvl + (2 * h) * (KVREC * 2) + lane * 8);
      bf16x4 k1 = *(const bf16x4*)(kvl + (2 * h + 1) * (KVREC * 2) + lane * 8);
      bf16x4 z0 = *(const bf16x4*)(kvl + (2 * h) * (KVREC * 2) + 512 + g * 8);
      bf16x4 z1 = *(const bf16x4*)(kvl + (2 * h + 1) * (KVREC * 2) + 512 + g * 8);
      const bf16x4 zz = {(__bf16)0.f, (__bf16)0.f, (__bf16)0.f, (__bf16)0.f};
      bf16x8 a0 = __builtin_shufflevector(k0, zz, 0, 1, 2, 3, 4, 5, 6, 7);
      bf16x8 a1 = __builtin_shufflevector(zz, k1, 0, 1, 2, 3, 4, 5, 6, 7);
      bf16x8 d0 = __builtin_shufflevector(z0, zz, 0, 1, 2, 3, 4, 5, 6, 7);
      bf16x8 d1 = __builtin_shufflevector(zz, z1, 0, 1, 2, 3, 4, 5, 6, 7);
      f32x4 n0 = MFMA(a0, pf[h], z4);
      f32x4 n1 = MFMA(a1, pf[h], z4);
      f32x4 e0 = MFMA(d0, pf[h], z4);
      f32x4 e1 = MFMA(d1, pf[h], z4);
      const float r0 = __builtin_amdgcn_rcpf(bfr(bfr(e0[0]) + 1e-6f));
      const float r1 = __builtin_amdgcn_rcpf(bfr(bfr(e1[0]) + 1e-6f));
      bf16x8 of;
#pragma unroll
      for (int r = 0; r < 4; ++r) {
        of[r] = (__bf16)(bfr(n0[r]) * r0);
        of[4 + r] = (__bf16)(bfr(n1[r]) * r1);
      }
#pragma unroll
      for (int ct = 0; ct < 4; ++ct) {
        bf16x8 wf = ldB(bl + ((ct ^ bsw) * 32), h * 4096, h * 4096 + 2048);
        acc[ct] = MFMA(wf, of, acc[ct]);
      }
    }
    STAMP(3);
    WAIT_VM(0);
    bar();
    TIE(xr[0]); TIE(xr[1]);
    f32x4* red = (f32x4*)(smem + R_OFF);
    red[((rg * 2 + kh) * 2 + 0) * 64 + lane] = kh ? acc[0] : acc[2];
    red[((rg * 2 + kh) * 2 + 1) * 64 + lane] = kh ? acc[1] : acc[3];
    asm volatile("s_waitcnt lgkmcnt(0)" ::: "memory");
    bar();
    f32x4 f0 = (kh ? acc[2] : acc[0]), f1 = (kh ? acc[3] : acc[1]);
    {
      f32x4 o0 = red[((rg * 2 + (kh ^ 1)) * 2 + 0) * 64 + lane], o1 = red[((rg * 2 + (kh ^ 1)) * 2 + 1) * 64 + lane];
      f0 = kh ? (o0 + f0) : (f0 + o0);
      f1 = kh ? (o1 + f1) : (f1 + o1);
    }
    float hv[8];
    float s = 0.f;
#pragma unroll
    for (int r = 0; r < 4; ++r) {
      hv[r] = bfr((float)xr[0][r] + bfr(f0[r]));
      hv[4 + r] = bfr((float)xr[1][r] + bfr(f1[r]));
      s += hv[r] + hv[4 + r];
    }
    {
      bf16x8 o;
#pragma unroll
      for (int r = 0; r < 8; ++r) o[r] = (__bf16)hv[r];
      gstore16_sc1((char*)hfr + (size_t)(rt * 16 + nt * 2 + kh) * 1024, lane * 16, o);
    }
    s += __shfl_xor(s, 16, 64);
    s += __shfl_xor(s, 32, 64);
    const float mean = s * (1.0f / 32.0f);
    float m2 = 0.f;
#pragma unroll
    for (int r = 0; r < 8; ++r) { float d = hv[r] - mean; m2 += d * d; }
    m2 += __shfl_xor(m2, 16, 64);
    m2 += __shfl_xor(m2, 32, 64);
    if (g == 0) gstore8f_sc1((char*)hstat + (size_t)(rt * 16 + nt * 2 + kh) * 128, c * 8, (f32x2){s, m2});
#pragma unroll
    for (int n = 0; n < 6; ++n) { issueB3(n, 0); issueB3(n, 1); }
    if (wv < 4) WAIT_VM(12); else WAIT_VM(6);
    bar();
    if (tid == 0) __hip_atomic_fetch_add(cnt + grp * 32, 1u, __ATOMIC_RELAXED, __HIP_MEMORY_SCOPE_AGENT);
    STAMP(4);
  }
  {
    if (wv == 7) group_wait(cnt + grp * 32);
    WAIT_VM(0);
    bar();
    STAMP(5);
    const int mw = wv & 3, nw = wv >> 2;
    const int rt0 = grp * 8 + mw * 2;
    const char* pA = (const char*)hfr + (size_t)(grp * 8 + wv) * 16384;
    auto issueA = [&](int n) { glds16s_sc1(pA + n * 1024, lane * 16, lds0 + R_OFF + (n % 6) * 8192 + wv * 1024); };
    issueA(0); issueA(1);
    glds16s_sc1((const char*)hstat + (size_t)grp * 16384 + wv * 2048, lane * 16, lds0 + R_OFF + 49152 + wv * 2048);
    glds16s_sc1((const char*)hstat + (size_t)grp * 16384 + wv * 2048 + 1024, lane * 16, lds0 + R_OFF + 49152 + wv * 2048 + 1024);
    issueA(2); issueA(3); issueA(4); issueA(5);

    f32x4 acc[2][6];
#pragma unroll
    for (int i = 0; i < 2; ++i)
#pragma unroll
      for (int j = 0; j < 6; ++j) acc[i][j] = (f32x4){0.f, 0.f, 0.f, 0.f};
    bf16x8 fa[2][2], fb[2][6];
    const int bl0 = (4 * g + (c >> 2)) * 64 + (c & 3) * 8;
    auto rdA = [&](int n, int bu) {
      const char* sl = smem + R_OFF + (n % 6) * 8192 + lane * 16;
      fa[bu][0] = *(const bf16x8*)(sl + (mw * 2) * 1024);
      fa[bu][1] = *(const bf16x8*)(sl + (mw * 2 + 1) * 1024);
    };
    auto rdB = [&](int n, int bu, int ct) {
      const int ftl = nw * 3 + (ct >> 1);
      const char* bp = smem + (n % 6) * 12288 + bl0 + ((ct & 1) * 6 + (ftl >> 1) * 2) * 1024 + (((ftl & 1) ^ (g & 1)) * 32);
      fb[bu][ct] = ldB(bp, 0, 1024);
    };
    WAIT_VM(7);
    bar();
    STAMP(6);
    rdA(0, 0);
#pragma unroll
    for (int ct = 0; ct < 6; ++ct) rdB(0, 0, ct);
    float mb[2], rs[2];
    f32x4 cs[6];
#pragma unroll
    for (int n = 0; n < 16; ++n) {
      if (n < 15) {
        if (wv < 4) wait_vm(p3_wait(n, 1)); else wait_vm(p3_wait(n, 0));
        bar();
      }
      if (n == 2) {
#pragma unroll
        for (int i = 0; i < 2; ++i) {
          const char* sp = smem + R_OFF + 49152 + ((mw * 2 + i) * 16 + g * 4) * 128 + c * 8;
          const f32x2 t0 = *(const f32x2*)(sp), t1 = *(const f32x2*)(sp + 128), t2 = *(const f32x2*)(sp + 256), t3 = *(const f32x2*)(sp + 384);
          const f32x4 s0 = {t0[0], t0[1], t1[0], t1[1]}, s1 = {t2[0], t2[1], t3[0], t3[1]};
          float s = (s0[0] + s0[2]) + (s1[0] + s1[2]);
          s += __shfl_xor(s, 16, 64);
          s += __shfl_xor(s, 32, 64);
          const float mean = s * (1.0f / 512.0f);
          const float d0 = s0[0] * (1.0f / 32.0f) - mean, d1 = s0[2] * (1.0f / 32.0f) - mean;
          const float d2 = s1[0] * (1.0f / 32.0f) - mean, d3 = s1[2] * (1.0f / 32.0f) - mean;
          float m2 = (s0[1] + s0[3]) + (s1[1] + s1[3]) + 32.0f * ((d0 * d0 + d1 * d1) + (d2 * d2 + d3 * d3));
          m2 += __shfl_xor(m2, 16, 64);
          m2 += __shfl_xor(m2, 32, 64);
          mb[i] = bfr(mean);
          rs[i] = 1.0f / bfr(sqrtf(bfr(bfr(m2 * (1.0f / 512.0f)) + 1e-5f)));
        }
      }
      if (n == 10) {
#pragma unroll
        for (int ct = 0; ct < 6; ++ct)
          cs[ct] = *(const f32x4*)(csp + (ct & 1) * F_ + (ft0 + nw * 3 + (ct >> 1)) * 16 + g * 4);
      }
#pragma unroll
      for (int ct = 0; ct < 6; ++ct) {
        acc[0][ct] = MFMA(fb[n & 1][ct], fa[n & 1][0], acc[0][ct]);
        SB;
        if (n < 15) { if (ct == 0) rdA(n + 1, (n + 1) & 1); rdB(n + 1, (n + 1) & 1, ct); }
        SB;
        acc[1][ct] = MFMA(fb[n & 1][ct], fa[n & 1][1], acc[1][ct]);
        SB;
        if (n + 6 < 16) { if (ct == 1) issueA(n + 6); if (ct == 2) issueB3(n + 6, 0); if (ct == 3) issueB3(n + 6, 1); }
        SB;
      }
    }
    STAMP(7);
    bar();
#pragma unroll
    for (int i = 0; i < 2; ++i) {
#pragma unroll
      for (int j = 0; j < 3; ++j) {
        const int ftl = nw * 3 + j;
        bf16x4 o;
#pragma unroll
        for (int r = 0; r < 4; ++r) {
          float gg = bfr(rs[i] * (acc[i][2 * j][r] - mb[i] * cs[2 * j][r]));
          float uu = bfr(rs[i] * (acc[i][2 * j + 1][r] - mb[i] * cs[2 * j + 1][r]));
          o[r] = (__bf16)(gg * uu);
        }
        *(bf16x4*)(smem + ((mw * 2 + i) * 3 + (ftl >> 1)) * 1024 + lane * 16 + (ftl & 1) * 8) = o;
      }
    }
    asm volatile("s_waitcnt lgkmcnt(0)" ::: "memory");
    bar();
#pragma unroll
    for (int pr = 0; pr < 3; ++pr) {
      bf16x8 v = *(const bf16x8*)(smem + (wv * 3 + pr) * 1024 + lane * 16);
      gstore16_sc1((char*)hh + (size_t)((grp * 8 + wv) * 43 + (ft0 >> 1) + pr) * 1024, lane * 16, v);
    }
    asm volatile("s_waitcnt lgkmcnt(0)" ::: "memory");
    bar();
  }
  {
    const int mi = wv & 1, kq = wv >> 1;
    const int rt0 = mt * 4 + mi * 2;
    const int u0 = kq * 11;
    const int fi = kq >> 1, fc = (kq & 1) * 2;
    const __bf16* wsrc = wd + (size_t)(u0 * 32 + mi * 16) * D_ + nt * 64;
    const unsigned ldsq = lds0 + kq * 40960 + mi * 2048;
    auto issueB = [&](int s) {
      const int ss = (s == 10 && kq == 3) ? 9 : s;
      glds16s(wsrc + (size_t)(ss * 32) * D_, woff, ldsq + (s % 10) * 4096);
      glds16s(wsrc + (size_t)(ss * 32 + 8) * D_, woff, ldsq + (s % 10) * 4096 + 1024);
    };
    if (wv != 7) {
#pragma unroll
      for (int s = 0; s < 10; ++s) issueB(s);
      WAIT_VM(20);
    } else {
      WAIT_VM(0);
    }
    bar();
    if (tid == 0) __hip_atomic_fetch_add(cnt + (16 + grp) * 32, 1u, __ATOMIC_RELAXED, __HIP_MEMORY_SCOPE_AGENT);
    STAMP(8);
    if (wv == 7) {
      group_wait(cnt + (16 + grp) * 32);
      WAIT_VM(0);
#pragma unroll
      for (int s = 0; s < 10; ++s) issueB(s);
    }
    bar();
    STAMP(9);
    bf16x8 af[2][11];
    const char* asrc = (const char*)hh + (size_t)(rt0 * 43 + u0) * 1024;
#pragma unroll
    for (int s = 0; s < 11; ++s) {
      const int ss = (s == 10 && kq == 3) ? 9 : s;
      gload16_sc1(af[0][s], asrc + ss * 1024, lane * 16);
      gload16_sc1(af[1][s], asrc + (43 + ss) * 1024, lane * 16);
    }
    f32x4 acc[2][4];
#pragma unroll
    for (int i = 0; i < 2; ++i)
#pragma unroll
      for (int j = 0; j < 4; ++j) acc[i][j] = (f32x4){0.f, 0.f, 0.f, 0.f};
    const char* bl = smem + kq * 40960 + btr;
    auto comp = [&](int s) {
      TIE(af[0][s]); TIE(af[1][s]);
#pragma unroll
      for (int ct = 0; ct < 4; ++ct) {
        bf16x8 bf = ldB(bl + ((ct ^ bsw) * 32), (s % 10) * 4096, (s % 10) * 4096 + 2048);
        acc[0][ct] = MFMA(bf, af[0][s], acc[0][ct]);
        acc[1][ct] = MFMA(bf, af[1][s], acc[1][ct]);
      }
    };
    WAIT_VM(18);
    bar();
    STAMP(10);
    comp(0); comp(1);
    WAIT_VM(12);
    bar();
    issueB(10);
    bf16x8 hres;
    gload16_sc1(hres, (const char*)hfr + (size_t)((rt0 + fi) * 16 + nt * 2 + (kq & 1)) * 1024, lane * 16);
    comp(2); comp(3); comp(4);
    WAIT_VM(9);
    comp(5); comp(6); comp(7);
    WAIT_VM(1);
    bar();
    comp(8); comp(9);
    if (kq < 3) comp(10);
    STAMP(11);
    WAIT_VM(0);
    TIE(hres);
    bar();
    f32x4* red = (f32x4*)smem;
#pragma unroll
    for (int i = 0; i < 2; ++i)
#pragma unroll
      for (int ct = 0; ct < 4; ++ct)
        if (i != fi || (ct >> 1) != (kq & 1)) red[(((mi * 4 + kq) * 2 + i) * 4 + ct) * 64 + lane] = acc[i][ct];
    __syncthreads();
#pragma unroll
    for (int p = 0; p < 2; ++p) {
      const int ct = fc + p;
      const f32x4 own = fi ? (fc ? acc[1][2 + p] : acc[1][p]) : (fc ? acc[0][2 + p] : acc[0][p]);
      f32x4 v[4];
#pragma unroll
      for (int q = 0; q < 4; ++q) v[q] = q == kq ? own : red[(((mi * 4 + q) * 2 + fi) * 4 + ct) * 64 + lane];
      f32x4 s = (v[0] + v[1]) + (v[2] + v[3]);
      bf16x4 o;
#pragma unroll
      for (int r = 0; r < 4; ++r) o[r] = (__bf16)((float)hres[p * 4 + r] + bfr(s[r]));
      *(bf16x4*)(out + (size_t)((rt0 + fi) * 16 + c) * D_ + nt * 64 + ct * 16 + g * 4) = o;
    }
  }
  STAMP(12); STAMP_OUT(2, pq);
}

extern "C" void kernel_launch(void* const* d_in, const int* in_sizes, int n_in, void* d_out, int out_size,
                              void* d_ws, size_t ws_size, hipStream_t stream) {
  const __bf16* x = (const __bf16*)d_in[0];
  const __bf16* wqkv = (const __bf16*)d_in[1];
  const __bf16* wo = (const __bf16*)d_in[2];
  const __bf16* wg = (const __bf16*)d_in[4];
  const __bf16* wu = (const __bf16*)d_in[5];
  const __bf16* wd = (const __bf16*)d_in[6];
  char* ws = (char*)d_ws;
  __bf16* pq = (__bf16*)(ws);
  __bf16* hfr = (__bf16*)(ws + (2u << 20));
  __bf16* hh = (__bf16*)(ws + (4u << 20));
  float* kvpart = (float*)(ws + (10u << 20));
  __bf16* kvf = (__bf16*)(ws + (13u << 20));
  float* csp = (float*)(ws + (13u << 20) + 65536);
  float* hstat = (float*)(ws + (14u << 20));
  unsigned* cnt = (unsigned*)(ws + (13u << 20) + 131072);
  k1_qkv<<<256, 512, 0, stream>>>(x, wqkv, pq, kvpart, cnt);
  k1b_reduce<<<77 + 118, 256, 0, stream>>>(kvpart, kvf, wo, wg, wu, wd, csp);
  k234<<<256, 512, 0, stream>>>(x, wo, pq, kvf, wg, wu, csp, wd, hfr, hstat, hh, cnt, (__bf16*)d_out);
}
```

```cpp
#include <hip/hip_runtime.h>
#include <hip/hip_bf16.h>
#include <stdint.h>

typedef __attribute__((ext_vector_type(8))) __bf16 bf16x8;
typedef __attribute__((ext_vector_type(4))) __bf16 bf16x4;
typedef __attribute__((ext_vector_type(4))) short s16x4;
typedef __attribute__((ext_vector_type(4))) float f32x4;
typedef __attribute__((ext_vector_type(2))) float f32x2;
typedef __attribute__((ext_vector_type(4))) unsigned u32x4;
typedef __attribute__((ext_vector_type(2))) unsigned u32x2;
typedef __attribute__((address_space(3))) s16x4* lds_s16x4_ptr;

#define DI __device__ __forceinline__
#define MFMA(a, b, c) __builtin_amdgcn_mfma_f32_16x16x32_bf16((a), (b), (c), 0, 0, 0)

constexpr int S_ = 2048, D_ = 512, F_ = 1376;
constexpr int KVREC = 272;

#define STAMP_DECL
#define STAMP(i)
#define STAMP_OUT(k, wsbase)
DI float bfr(float x) { return (float)(__bf16)x; }
DI float bf2f(__bf16 x) { return (float)x; }

DI void glds16(const void* gsrc, unsigned lds_dst) {
  unsigned keep;
  asm volatile(
      "s_mov_b32 %0, m0\n\ts_mov_b32 m0, %2\n\ts_nop 0\n\tglobal_load_lds_dwordx4 %1, off\n\ts_mov_b32 m0, %0"
      : "=&s"(keep)
      : "v"(gsrc), "s"(lds_dst)
      : "memory");
}
DI void glds16s(const void* ubase, unsigned voff, unsigned lds_dst) {
  unsigned keep;
  asm volatile(
      "s_mov_b32 %0, m0\n\ts_mov_b32 m0, %3\n\ts_nop 0\n\tglobal_load_lds_dwordx4 %1, %2\n\ts_mov_b32 m0, %0"
      : "=&s"(keep)
      : "v"(voff), "s"(ubase), "s"(lds_dst)
      : "memory");
}
DI void gload16(bf16x8& dst, const void* ubase, unsigned voff) {
  asm volatile("global_load_dwordx4 %0, %1, %2" : "=v"(dst) : "v"(voff), "s"(ubase) : "memory");
}
DI void gload8(bf16x4& dst, const void* ubase, unsigned voff) {
  asm volatile("global_load_dwordx2 %0, %1, %2" : "=v"(dst) : "v"(voff), "s"(ubase) : "memory");
}
DI void gload16_sc1(bf16x8& dst, const void* ubase, unsigned voff) {
  asm volatile("global_load_dwordx4 %0, %1, %2 sc1" : "=v"(dst) : "v"(voff), "s"(ubase) : "memory");
}
DI void glds16s_sc1(const void* ubase, unsigned voff, unsigned lds_dst) {
  unsigned keep;
  asm volatile(
      "s_mov_b32 %0, m0\n\ts_mov_b32 m0, %3\n\ts_nop 0\n\tglobal_load_lds_dwordx4 %1, %2 sc1\n\ts_mov_b32 m0, %0"
      : "=&s"(keep)
      : "v"(voff), "s"(ubase), "s"(lds_dst)
      : "memory");
}
DI void gstore16_sc1(void* ubase, unsigned voff, bf16x8 v) {
  asm volatile("global_store_dwordx4 %0, %1, %2 sc1\n\ts_nop 1" :: "v"(voff), "v"(v), "s"(ubase) : "memory");
}
DI void gstore8_sc1(void* ubase, unsigned voff, bf16x4 v) {
  asm volatile("global_store_dwordx2 %0, %1, %2 sc1\n\ts_nop 1" :: "v"(voff), "v"(v), "s"(ubase) : "memory");
}
DI void gstore8f_sc1(void* ubase, unsigned voff, f32x2 v) {
  asm volatile("global_store_dwordx2 %0, %1, %2 sc1\n\ts_nop 1" :: "v"(voff), "v"(v), "s"(ubase) : "memory");
}
#define TIE(x) asm volatile("" : "+v"(x))
#define WAIT_VM(n) do { __builtin_amdgcn_sched_barrier(0); asm volatile("s_waitcnt vmcnt(" #n ") lgkmcnt(0)" ::: "memory"); } while (0)
DI void wait_vm(int n) {
  switch (n) {
    case 0: WAIT_VM(0); break;   case 1: WAIT_VM(1); break;   case 2: WAIT_VM(2); break;
    case 3: WAIT_VM(3); break;   case 4: WAIT_VM(4); break;   case 5: WAIT_VM(5); break;
    case 6: WAIT_VM(6); break;   case 7: WAIT_VM(7); break;   case 8: WAIT_VM(8); break;
    case 9: WAIT_VM(9); break;   case 10: WAIT_VM(10); break; case 11: WAIT_VM(11); break;
    case 12: WAIT_VM(12); break; case 13: WAIT_VM(13); break; case 14: WAIT_VM(14); break;
    case 15: WAIT_VM(15); break; case 16: WAIT_VM(16); break; case 17: WAIT_VM(17); break;
    case 18: WAIT_VM(18); break; case 19: WAIT_VM(19); break; case 20: WAIT_VM(20); break;
    case 21: WAIT_VM(21); break; case 22: WAIT_VM(22); break; case 23: WAIT_VM(23); break;
    case 24: WAIT_VM(24); break; case 25: WAIT_VM(25); break; case 26: WAIT_VM(26); break;
    case 27: WAIT_VM(27); break; case 28: WAIT_VM(28); break; case 29: WAIT_VM(29); break;
    case 30: WAIT_VM(30); break; case 31: WAIT_VM(31); break; case 32: WAIT_VM(32); break;
    case 33: WAIT_VM(33); break; case 34: WAIT_VM(34); break; case 35: WAIT_VM(35); break;
    case 36: WAIT_VM(36); break; case 37: WAIT_VM(37); break; case 38: WAIT_VM(38); break;
    case 39: WAIT_VM(39); break; case 40: WAIT_VM(40); break; case 41: WAIT_VM(41); break;
    case 42: WAIT_VM(42); break; case 43: WAIT_VM(43); break; case 44: WAIT_VM(44); break;
    default: WAIT_VM(0); break;
  }
}
#define SB __builtin_amdgcn_sched_barrier(0)
#define XMFMA MFMA
#define XRD 1
#define XLD 1
DI void bar() { __builtin_amdgcn_s_barrier(); __builtin_amdgcn_sched_barrier(0); }

DI bf16x8 ldB(const char* smem_lane_base, int off0, int off1) {
  s16x4 lo = __builtin_amdgcn_ds_read_tr16_b64_v4i16((lds_s16x4_ptr)(smem_lane_base + off0));
  s16x4 hi = __builtin_amdgcn_ds_read_tr16_b64_v4i16((lds_s16x4_ptr)(smem_lane_base + off1));
  typedef __attribute__((ext_vector_type(8))) short s16x8;
  s16x8 v = __builtin_shufflevector(lo, hi, 0, 1, 2, 3, 4, 5, 6, 7);
  return __builtin_bit_cast(bf16x8, v);
}

DI float phi(float t) {
  float r = t > 0.f ? bfr(t + 1.0f) : bfr(__expf(t));
  return bfr(r + 1e-6f);
}

constexpr int K1_RING = 65536 + 7 * 12288;
constexpr int K1_LDS = K1_RING + 4096;
constexpr int k1_gcnt(int n, int lo) { return n > 15 ? 0 : ((lo ? 2 : 1) + ((n & 1) == 0 ? 1 : 0)); }
constexpr int k1_younger(int n, int lo) {
  return k1_gcnt(n + 1, lo) + k1_gcnt(n + 2, lo) + k1_gcnt(n + 3, lo) + k1_gcnt(n + 4, lo);
}
__global__ __launch_bounds__(512, 2) void k1_qkv(const __bf16* x, const __bf16* w, __bf16* pq, float* kvpart, unsigned* cnt,
                                                 const __bf16* wo, const __bf16* wg, const __bf16* wu, const __bf16* wd) {
  __shared__ __attribute__((aligned(16))) char smem[K1_LDS];
  STAMP_DECL; STAMP(0);
  const int tid = threadIdx.x, lane = tid & 63, wv = __builtin_amdgcn_readfirstlane(tid >> 6);
  const int g = lane >> 4, c = lane & 15;
  const int hw = wv & 3, rh = wv >> 2;
  const int b = blockIdx.x, xcd = b & 7, idx = b >> 3;
  const int mt = (xcd >> 1) * 8 + (idx >> 2), hq = (xcd & 1) * 4 + (idx & 3);
  const unsigned lds0 = (unsigned)(size_t)smem;

  const __bf16* xsrc = x + (size_t)(mt * 64 + wv * 8) * D_;
  const __bf16* wsrc0 = w + (size_t)((wv & 3) * 8) * 1536 + (wv >> 2) * 512 + hq * 64;
  const __bf16* wsrc1 = w + (size_t)((wv & 3) * 8) * 1536 + 1024 + hq * 64;
  unsigned xoff, woff;
  {
    const int r = lane >> 3, pos = lane & 7;
    const int row = wv * 8 + r;
    xoff = r * (D_ * 2) + (pos ^ ((row >> 1) & 7)) * 16;
    const int swz = ((r >> 1) & 1) | ((wv & 1) << 1);
    woff = r * 3072 + ((pos >> 1) ^ swz) * 32 + (pos & 1) * 16;
  }
  const int rot = 0;
  auto issue1 = [&](int n, int e) {
    const unsigned slot = lds0 + 65536 + (n % 7) * 12288;
    const int kn = (n + rot) & 15;
    if (e == 0) glds16s(wsrc0 + (size_t)kn * 32 * 1536, woff, slot + wv * 1024);
    if (e == 1) { if (wv < 4) glds16s(wsrc1 + (size_t)kn * 32 * 1536, woff, slot + 8192 + wv * 1024); }
    if (e == 2) { if ((n & 1) == 0) glds16s(xsrc + (kn >> 1) * 64, xoff, lds0 + (n >> 1) * 8192 + wv * 1024); }
  };
#pragma unroll
  for (int n = 0; n < 6; ++n) { issue1(n, 0); issue1(n, 1); issue1(n, 2); }
  STAMP(1);

  f32x4 acc[2][3];
#pragma unroll
  for (int i = 0; i < 2; ++i)
#pragma unroll
    for (int j = 0; j < 3; ++j) acc[i][j] = (f32x4){0.f, 0.f, 0.f, 0.f};
  f32x4 stt = {0.f, 0.f, 0.f, 0.f};
  f32x4 csum[3];
#pragma unroll
  for (int j = 0; j < 3; ++j) csum[j] = (f32x4){0.f, 0.f, 0.f, 0.f};
  bf16x8 ones;
#pragma unroll
  for (int j = 0; j < 8; ++j) ones[j] = (__bf16)1.0f;

  const int aoff = (rh * 32 + c) * 128 + ((g ^ (c >> 1)) * 16);
  const int boff = 65536 + g * 1024 + (c >> 2) * 128 + ((hw ^ (((c >> 3) & 1) | ((g & 1) << 1))) * 32) + (c & 3) * 8;
  bf16x8 fa[2][2], fb[2][3], pfx[3];
  auto rdA = [&](int n, int bu) {
    const char* sl = smem + (n >> 1) * 8192 + (aoff ^ ((n & 1) << 6));
    fa[bu][0] = *(const bf16x8*)(sl);
    fa[bu][1] = *(const bf16x8*)(sl + 2048);
  };
  auto rdB = [&](int n, int bu, int p) { fb[bu][p] = ldB(smem + boff + (n % 7) * 12288 + p * 4096, 0, 512); };
  if (wv < 4) wait_vm(k1_gcnt(5, 1) + k1_younger(0, 1)); else wait_vm(k1_gcnt(5, 0) + k1_younger(0, 0));
  bar();
  STAMP(8);
  rdA(0, 0); rdB(0, 0, 0); rdB(0, 0, 1); rdB(0, 0, 2);
#pragma unroll
  for (int n = 0; n < 16; ++n) {
    const int cu = n & 1, nx = cu ^ 1;
    if (n < 15) {
      if (wv < 4) wait_vm(k1_younger(n + 1, 1) + (n > 10 ? 3 : 0)); else wait_vm(k1_younger(n + 1, 0) + (n > 10 ? 3 : 0));
      bar();
    }
    if (n == 10) {
#pragma unroll
      for (int j = 0; j < 3; ++j) {
        int pc = b * 8 + wv + 2048 * j;
        pc = pc < 4640 ? pc : pc - 2048;
        const char* src = pc < 512 ? (const char*)wo + (size_t)pc * 1024
                        : pc < 1888 ? (const char*)wg + (size_t)(pc - 512) * 1024
                        : pc < 3264 ? (const char*)wu + (size_t)(pc - 1888) * 1024 : (const char*)wd + (size_t)(pc - 3264) * 1024;
        gload16(pfx[j], src, lane * 16);
      }
    }
    acc[0][0] = XMFMA(fb[cu][0], fa[cu][0], acc[0][0]); SB;
    if (XRD && n < 15) rdA(n + 1, nx);
    SB;
    acc[1][0] = XMFMA(fb[cu][0], fa[cu][1], acc[1][0]); SB;
    if (XRD && n < 15) rdB(n + 1, nx, 0);
    SB;
    acc[0][1] = XMFMA(fa[cu][0], fb[cu][1], acc[0][1]); SB;
    if (XRD && n < 15) rdB(n + 1, nx, 1);
    SB;
    acc[1][1] = XMFMA(fa[cu][1], fb[cu][1], acc[1][1]); SB;
    if (XRD && n < 15) rdB(n + 1, nx, 2);
    SB;
    acc[0][2] = XMFMA(fa[cu][0], fb[cu][2], acc[0][2]); SB;
    if (XLD && n + 6 < 16) issue1(n + 6, 0);
    SB;
    acc[1][2] = XMFMA(fa[cu][1], fb[cu][2], acc[1][2]); SB;
    if (XLD && n + 6 < 16) issue1(n + 6, 1);
    SB;
    if (hw == 0) stt = XMFMA(ones, fa[cu][0], stt);
    else if (hw == 1) stt = XMFMA(fa[cu][0], fa[cu][0], stt);
    else if (hw == 2) stt = XMFMA(ones, fa[cu][1], stt);
    else stt = XMFMA(fa[cu][1], fa[cu][1], stt);
    SB;
    if (XLD && n + 6 < 16) issue1(n + 6, 2);
    SB;
    if (cu == rh) {
      csum[0] = XMFMA(ones, fb[cu][0], csum[0]);
      csum[1] = XMFMA(ones, fb[cu][1], csum[1]);
      csum[2] = XMFMA(ones, fb[cu][2], csum[2]);
    }
  }
  STAMP(6);
  float* tsum = (float*)(smem + K1_RING);
  float* tsq = tsum + 64;
  float* tcs = tsum + 128;
  float* tst = tsum + 512;
  {
    const int row = rh * 32 + (hw >> 1) * 16 + c;
    if ((hw & 1) == 0) { if (g == 0) tsum[row] = stt[0]; }
    else if (g == (c >> 2)) tsq[row] = (c & 2) ? ((c & 1) ? stt[3] : stt[2]) : ((c & 1) ? stt[1] : stt[0]);
    if (g == 0) {
#pragma unroll
      for (int p = 0; p < 3; ++p) tcs[rh * 192 + p * 64 + hw * 16 + c] = csum[p][0];
    }
  }
  __syncthreads();
  if (tid < 64) {
    const float mean = tsum[tid] * (1.0f / 512.0f);
    const float var = fmaxf(tsq[tid] * (1.0f / 512.0f) - mean * mean, 0.f);
    const float sb = bfr(sqrtf(bfr(bfr(var) + 1e-5f)));
    *(f32x2*)(tst + tid * 2) = (f32x2){bfr(mean), 1.0f / sb};
  }
  __syncthreads();
  const f32x4 csq = *(const f32x4*)(tcs + hw * 16 + 4 * g) + *(const f32x4*)(tcs + 192 + hw * 16 + 4 * g);
  const float csk = tcs[64 + hw * 16 + c] + tcs[192 + 64 + hw * 16 + c];
  const float csv = tcs[128 + hw * 16 + c] + tcs[192 + 128 + hw * 16 + c];
#pragma unroll
  for (int i = 0; i < 2; ++i) {
    const f32x2 st = *(const f32x2*)(tst + (rh * 32 + i * 16 + c) * 2);
    bf16x4 o;
#pragma unroll
    for (int r = 0; r < 4; ++r) o[r] = (__bf16)phi(bfr(st[1] * (acc[i][0][r] - st[0] * csq[r])));
    const int rt = mt * 4 + rh * 2 + i;
    *(bf16x4*)(pq + ((size_t)(rt * 16 + hq * 2 + (hw >> 1)) * 64 + lane) * 8 + (hw & 1) * 4) = o;
  }
  STAMP(7);
  {
    bf16x8 pk, vv;
#pragma unroll
    for (int i = 0; i < 2; ++i)
#pragma unroll
      for (int r = 0; r < 4; r += 2) {
        const f32x4 st = *(const f32x4*)(tst + (rh * 32 + i * 16 + 4 * g + r) * 2);
        pk[i * 4 + r] = (__bf16)phi(bfr(st[1] * (acc[i][1][r] - st[0] * csk)));
        pk[i * 4 + r + 1] = (__bf16)phi(bfr(st[3] * (acc[i][1][r + 1] - st[2] * csk)));
        vv[i * 4 + r] = (__bf16)(st[1] * (acc[i][2][r] - st[0] * csv));
        vv[i * 4 + r + 1] = (__bf16)(st[3] * (acc[i][2][r + 1] - st[2] * csv));
      }
    const f32x4 z4 = {0.f, 0.f, 0.f, 0.f};
    f32x4 kv = MFMA(pk, vv, z4);
    f32x4 zz = MFMA(pk, ones, z4);
    float* dst = kvpart + (size_t)((mt * 2 + rh) * 32 + hq * 4 + hw) * KVREC;
    *(f32x4*)(dst + lane * 4) = kv;
    if (c == 0) *(f32x4*)(dst + 256 + g * 4) = zz;
  }
  if (b == 0 && tid < 32) cnt[tid * 32] = 0u;
  WAIT_VM(0);
  TIE(pfx[0]); TIE(pfx[1]); TIE(pfx[2]);
  STAMP(2); STAMP_OUT(0, pq);
}

__global__ __launch_bounds__(256) void k1b_reduce(const float* kvpart, __bf16* kvf, const __bf16* wo, const __bf16* wg,
                                                  const __bf16* wu, const __bf16* wd, float* csp) {
  __shared__ __attribute__((aligned(16))) float red[32 * 64];
  STAMP_DECL; STAMP(0);
  const int tid = threadIdx.x, b = blockIdx.x, lane = tid & 63, wv = __builtin_amdgcn_readfirstlane(tid >> 6);
  if (b < 34) {
    const int q = b * 64 + lane;
    f32x4 v[16];
#pragma unroll
    for (int m = 0; m < 16; ++m) v[m] = *(const f32x4*)(kvpart + (size_t)(wv * 16 + m) * 32 * KVREC + q * 4);
    f32x4 s = v[0];
#pragma unroll
    for (int m = 1; m < 16; ++m) s += v[m];
    f32x4* r4 = (f32x4*)red;
    r4[tid] = s;
    __syncthreads();
    if (tid < 64) {
      f32x4 t = (r4[tid] + r4[64 + tid]) + (r4[128 + tid] + r4[192 + tid]);
      bf16x4 o;
#pragma unroll
      for (int j = 0; j < 4; ++j) o[j] = (__bf16)t[j];
      *(bf16x4*)(kvf + q * 4) = o;
    }
    STAMP(1); STAMP_OUT(1, (const char*)kvpart - (10u << 20));
    return;
  }
  if (b >= 77) {
    const int pb = b - 77;
    const u32x4* src = pb < 32 ? (const u32x4*)wo + (size_t)pb * 1024 : (const u32x4*)wd + (size_t)(pb - 32) * 1024;
    u32x4 v0 = src[tid], v1 = src[256 + tid], v2 = src[512 + tid], v3 = src[768 + tid];
    asm volatile("" :: "v"(v0), "v"(v1), "v"(v2), "v"(v3));
    return;
  }
  const int cb = b - 34;
  const int col0 = cb * 64 + (tid & 7) * 8;
  const __bf16* src = col0 < F_ ? (wg + col0) : (wu + (col0 - F_));
  const int kq = tid >> 3;
  bf16x8 v[16];
#pragma unroll
  for (int r = 0; r < 16; ++r) v[r] = *(const bf16x8*)(src + (size_t)(kq * 16 + r) * F_);
  float s[8];
#pragma unroll
  for (int j = 0; j < 8; ++j) s[j] = 0.f;
#pragma unroll
  for (int r = 0; r < 16; ++r)
#pragma unroll
    for (int j = 0; j < 8; ++j) s[j] += (float)v[r][j];
#pragma unroll
  for (int j = 0; j < 8; ++j) red[kq * 64 + (tid & 7) * 8 + j] = s[j];
  __syncthreads();
  if (tid < 64) {
    float t = 0.f;
#pragma unroll
    for (int q = 0; q < 32; ++q) t += red[q * 64 + tid];
    csp[cb * 64 + tid] = t;
  }
  STAMP(2); STAMP_OUT(1, (const char*)kvpart - (10u << 20));
}

constexpr int B3_RING = 6 * 12288;
constexpr int R_OFF = B3_RING;
constexpr int p3_wait(int n, int lo) {
  const int gs = lo ? 3 : 2;
  int c = 0;
  if (n + 1 <= 5) {
    c = (n + 1 == 1) ? 6 : 5 - (n + 1);
    c += gs * n;
  } else {
    for (int j = n - 4; j <= n - 1; ++j) c += (j <= 9) ? gs : 0;
  }
  return c;
}
DI void group_wait(unsigned* cnt) {
  unsigned spins = 0;
  while ((unsigned)__builtin_amdgcn_readfirstlane(__hip_atomic_load(cnt, __ATOMIC_RELAXED, __HIP_MEMORY_SCOPE_AGENT)) < 16u) {
    __builtin_amdgcn_s_sleep(1);
    if (++spins > (1u << 22)) break;
  }
}
__global__ __launch_bounds__(512, 2) void k234(const __bf16* x, const __bf16* wo, const __bf16* pq, const __bf16* kvf,
                                               const __bf16* wg, const __bf16* wu, const float* csp, const __bf16* wd,
                                               __bf16* hfr, float* hstat, __bf16* hh, unsigned* cnt, __bf16* out) {
  __shared__ __attribute__((aligned(16))) char smem[163840];
  STAMP_DECL; STAMP(0);
  const int tid = threadIdx.x, lane = tid & 63, wv = __builtin_amdgcn_readfirstlane(tid >> 6);
  const int g = lane >> 4, c = lane & 15;
  const int b = blockIdx.x, xcd = b & 7, idx = b >> 3;
  const int grp = xcd * 2 + (idx >> 4), mem = idx & 15;
  const unsigned lds0 = (unsigned)(size_t)smem;
  const int mt = grp * 2 + (mem >> 3), nt = mem & 7;
  const int ft0 = 2 * ((mem * 43) >> 4);
  const unsigned woff = (lane >> 3) * (D_ * 2) + ((((lane & 7) >> 1) ^ ((lane >> 4) & 3)) * 32) + (lane & 1) * 16;
  const int brow = (g & 1) * 4 + (c >> 2);
  const int bsw = (brow >> 1) & 3;
  const int btr = (g >> 1) * 1024 + brow * 128 + (c & 3) * 8;

  const unsigned boffl = (lane >> 2) * (F_ * 2) + ((((lane >> 1) & 1) ^ ((lane >> 4) & 1)) * 32) + (lane & 1) * 16;
  const __bf16* pB0 = (wv < 6 ? wg : wu) + (size_t)((wv & 1) * 16) * F_ + (ft0 + ((wv % 6) >> 1) * 2) * 16;
  const __bf16* pB1 = wu + (size_t)((wv & 1) * 16) * F_ + (ft0 + ((2 + (wv & 3)) >> 1) * 2) * 16;
  auto issueB3 = [&](int n, int e) {
    const unsigned slot = lds0 + (n % 6) * 12288;
    if (e == 0) glds16s(pB0 + (size_t)n * 32 * F_, boffl, slot + wv * 1024);
    if (e == 1) { if (wv < 4) glds16s(pB1 + (size_t)n * 32 * F_, boffl, slot + 8192 + wv * 1024); }
  };

  {
    const int rg = wv & 3, kh = wv >> 2;
    const int rt = mt * 4 + rg;
    bf16x8 pf[8];
#pragma unroll
    for (int h = 0; h < 8; ++h) gload16(pf[h], (const char*)pq + (size_t)(rt * 16 + kh * 8 + h) * 1024, lane * 16);
#pragma unroll
    for (int i = 0; i < 3; ++i) {
      const int ins = i * 8 + wv;
      if (ins < 17) glds16s((const char*)kvf + ins * 1024, lane * 16, lds0 + R_OFF + 65536 + ins * 1024);
    }
#pragma unroll
    for (int i = 0; i < 8; ++i) {
      const int kb = i * 8 + wv;
      glds16s(wo + (size_t)kb * 8 * D_ + nt * 64, woff, lds0 + R_OFF + kb * 1024);
    }
    bf16x4 xr[2];
#pragma unroll
    for (int p = 0; p < 2; ++p) gload8(xr[p], x + (size_t)(rt * 16) * D_ + nt * 64 + (kh * 2 + p) * 16, c * (D_ * 2) + g * 8);
    f32x4 acc[4];
#pragma unroll
    for (int j = 0; j < 4; ++j) acc[j] = (f32x4){0.f, 0.f, 0.f, 0.f};
    STAMP(1);
    WAIT_VM(2);
    bar();
    STAMP(2);
    const char* bl = smem + R_OFF + kh * 32768 + btr;
    const char* kvl = smem + R_OFF + 65536 + kh * 16 * (KVREC * 2);
    const f32x4 z4 = {0.f, 0.f, 0.f, 0.f};
#pragma unroll
    for (int h = 0; h < 8; ++h) {
      TIE(pf[h]);
      bf16x4 k0 = *(const bf16x4*)(kvl + (2 * h) * (KVREC * 2) + lane * 8);
      bf16x4 k1 = *(const bf16x4*)(kvl + (2 * h + 1) * (KVREC * 2) + lane * 8);
      bf16x4 z0 = *(const bf16x4*)(kvl + (2 * h) * (KVREC * 2) + 512 + g * 8);
      bf16x4 z1 = *(const bf16x4*)(kvl + (2 * h + 1) * (KVREC * 2) + 512 + g * 8);
      const bf16x4 zz = {(__bf16)0.f, (__bf16)0.f, (__bf16)0.f, (__bf16)0.f};
      bf16x8 a0 = __builtin_shufflevector(k0, zz, 0, 1, 2, 3, 4, 5, 6, 7);
      bf16x8 a1 = __builtin_shufflevector(zz, k1, 0, 1, 2, 3, 4, 5, 6, 7);
      bf16x8 d0 = __builtin_shufflevector(z0, zz, 0, 1, 2, 3, 4, 5, 6, 7);
      bf16x8 d1 = __builtin_shufflevector(zz, z1, 0, 1, 2, 3, 4, 5, 6, 7);
      f32x4 n0 = MFMA(a0, pf[h], z4);
      f32x4 n1 = MFMA(a1, pf[h], z4);
      f32x4 e0 = MFMA(d0, pf[h], z4);
      f32x4 e1 = MFMA(d1, pf[h], z4);
      if (h < 6) { issueB3(h, 0); issueB3(h, 1); }
      const float r0 = __builtin_amdgcn_rcpf(bfr(bfr(e0[0]) + 1e-6f));
      const float r1 = __builtin_amdgcn_rcpf(bfr(bfr(e1[0]) + 1e-6f));
      bf16x8 of;
#pragma unroll
      for (int r = 0; r < 4; ++r) {
        of[r] = (__bf16)(bfr(n0[r]) * r0);
        of[4 + r] = (__bf16)(bfr(n1[r]) * r1);
      }
#pragma unroll
      for (int ct = 0; ct < 4; ++ct) {
        bf16x8 wf = ldB(bl + ((ct ^ bsw) * 32), h * 4096, h * 4096 + 2048);
        acc[ct] = MFMA(wf, of, acc[ct]);
      }
    }
    STAMP(3);
    if (wv < 4) WAIT_VM(12); else WAIT_VM(6);
    bar();
    TIE(xr[0]); TIE(xr[1]);
    f32x4* red = (f32x4*)(smem + R_OFF);
    red[((rg * 2 + kh) * 2 + 0) * 64 + lane] = kh ? acc[0] : acc[2];
    red[((rg * 2 + kh) * 2 + 1) * 64 + lane] = kh ? acc[1] : acc[3];
    asm volatile("s_waitcnt lgkmcnt(0)" ::: "memory");
    bar();
    f32x4 f0 = (kh ? acc[2] : acc[0]), f1 = (kh ? acc[3] : acc[1]);
    {
      f32x4 o0 = red[((rg * 2 + (kh ^ 1)) * 2 + 0) * 64 + lane], o1 = red[((rg * 2 + (kh ^ 1)) * 2 + 1) * 64 + lane];
      f0 = kh ? (o0 + f0) : (f0 + o0);
      f1 = kh ? (o1 + f1) : (f1 + o1);
    }
    float hv[8];
    float s = 0.f;
#pragma unroll
    for (int r = 0; r < 4; ++r) {
      hv[r] = bfr((float)xr[0][r] + bfr(f0[r]));
      hv[4 + r] = bfr((float)xr[1][r] + bfr(f1[r]));
      s += hv[r] + hv[4 + r];
    }
    {
      bf16x8 o;
#pragma unroll
      for (int r = 0; r < 8; ++r) o[r] = (__bf16)hv[r];
      gstore16_sc1((char*)hfr + (size_t)(rt * 16 + nt * 2 + kh) * 1024, lane * 16, o);
    }
    s += __shfl_xor(s, 16, 64);
    s += __shfl_xor(s, 32, 64);
    const float mean = s * (1.0f / 32.0f);
    float m2 = 0.f;
#pragma unroll
    for (int r = 0; r < 8; ++r) { float d = hv[r] - mean; m2 += d * d; }
    m2 += __shfl_xor(m2, 16, 64);
    m2 += __shfl_xor(m2, 32, 64);
    if (g == 0) gstore8f_sc1((char*)hstat + (size_t)(rt * 16 + nt * 2 + kh) * 128, c * 8, (f32x2){s, m2});
    WAIT_VM(0);
    bar();
    if (tid == 0) __hip_atomic_fetch_add(cnt + grp * 32, 1u, __ATOMIC_RELAXED, __HIP_MEMORY_SCOPE_AGENT);
    STAMP(4);
  }
  {
    if (wv == 0) group_wait(cnt + grp * 32);
    WAIT_VM(0);
    bar();
    STAMP(5);
    const int mw = wv & 3, nw = wv >> 2;
    const int rt0 = grp * 8 + mw * 2;
    const char* pA = (const char*)hfr + (size_t)(grp * 8 + wv) * 16384;
    auto issueA = [&](int n) { glds16s_sc1(pA + n * 1024, lane * 16, lds0 + R_OFF + (n % 6) * 8192 + wv * 1024); };
    issueA(0); issueA(1);
    glds16s_sc1((const char*)hstat + (size_t)grp * 16384 + wv * 2048, lane * 16, lds0 + R_OFF + 49152 + wv * 2048);
    glds16s_sc1((const char*)hstat + (size_t)grp * 16384 + wv * 2048 + 1024, lane * 16, lds0 + R_OFF + 49152 + wv * 2048 + 1024);
    issueA(2); issueA(3); issueA(4); issueA(5);

    f32x4 acc[2][6];
#pragma unroll
    for (int i = 0; i < 2; ++i)
#pragma unroll
      for (int j = 0; j < 6; ++j) acc[i][j] = (f32x4){0.f, 0.f, 0.f, 0.f};
    bf16x8 fa[2][2], fb[2][6];
    const int bl0 = (4 * g + (c >> 2)) * 64 + (c & 3) * 8;
    auto rdA = [&](int n, int bu) {
      const char* sl = smem + R_OFF + (n % 6) * 8192 + lane * 16;
      fa[bu][0] = *(const bf16x8*)(sl + (mw * 2) * 1024);
      fa[bu][1] = *(const bf16x8*)(sl + (mw * 2 + 1) * 1024);
    };
    auto rdB = [&](int n, int bu, int ct) {
      const int ftl = nw * 3 + (ct >> 1);
      const char* bp = smem + (n % 6) * 12288 + bl0 + ((ct & 1) * 6 + (ftl >> 1) * 2) * 1024 + (((ftl & 1) ^ (g & 1)) * 32);
      fb[bu][ct] = ldB(bp, 0, 1024);
    };
    WAIT_VM(7);
    bar();
    STAMP(6);
    rdA(0, 0);
#pragma unroll
    for (int ct = 0; ct < 6; ++ct) rdB(0, 0, ct);
    float mb[2], rs[2];
    f32x4 cs[6];
#pragma unroll
    for (int n = 0; n < 16; ++n) {
      if (n < 15) {
        if (wv < 4) wait_vm(p3_wait(n, 1)); else wait_vm(p3_wait(n, 0));
        bar();
      }
      if (n == 2) {
#pragma unroll
        for (int i = 0; i < 2; ++i) {
          const char* sp = smem + R_OFF + 49152 + ((mw * 2 + i) * 16 + g * 4) * 128 + c * 8;
          const f32x2 t0 = *(const f32x2*)(sp), t1 = *(const f32x2*)(sp + 128), t2 = *(const f32x2*)(sp + 256), t3 = *(const f32x2*)(sp + 384);
          const f32x4 s0 = {t0[0], t0[1], t1[0], t1[1]}, s1 = {t2[0], t2[1], t3[0], t3[1]};
          float s = (s0[0] + s0[2]) + (s1[0] + s1[2]);
          s += __shfl_xor(s, 16, 64);
          s += __shfl_xor(s, 32, 64);
          const float mean = s * (1.0f / 512.0f);
          const float d0 = s0[0] * (1.0f / 32.0f) - mean, d1 = s0[2] * (1.0f / 32.0f) - mean;
          const float d2 = s1[0] * (1.0f / 32.0f) - mean, d3 = s1[2] * (1.0f / 32.0f) - mean;
          float m2 = (s0[1] + s0[3]) + (s1[1] + s1[3]) + 32.0f * ((d0 * d0 + d1 * d1) + (d2 * d2 + d3 * d3));
          m2 += __shfl_xor(m2, 16, 64);
          m2 += __shfl_xor(m2, 32, 64);
          mb[i] = bfr(mean);
          rs[i] = 1.0f / bfr(sqrtf(bfr(bfr(m2 * (1.0f / 512.0f)) + 1e-5f)));
        }
      }
      if (n == 10) {
#pragma unroll
        for (int ct = 0; ct < 6; ++ct)
          cs[ct] = *(const f32x4*)(csp + (ct & 1) * F_ + (ft0 + nw * 3 + (ct >> 1)) * 16 + g * 4);
      }
#pragma unroll
      for (int ct = 0; ct < 6; ++ct) {
        acc[0][ct] = MFMA(fb[n & 1][ct], fa[n & 1][0], acc[0][ct]);
        SB;
        if (n < 15) { if (ct == 0) rdA(n + 1, (n + 1) & 1); rdB(n + 1, (n + 1) & 1, ct); }
        SB;
        acc[1][ct] = MFMA(fb[n & 1][ct], fa[n & 1][1], acc[1][ct]);
        SB;
        if (n + 6 < 16) { if (ct == 1) issueA(n + 6); if (ct == 2) issueB3(n + 6, 0); if (ct == 3) issueB3(n + 6, 1); }
        SB;
      }
    }
    STAMP(7);
    bar();
#pragma unroll
    for (int i = 0; i < 2; ++i) {
#pragma unroll
      for (int j = 0; j < 3; ++j) {
        const int ftl = nw * 3 + j;
        bf16x4 o;
#pragma unroll
        for (int r = 0; r < 4; ++r) {
          float gg = bfr(rs[i] * (acc[i][2 * j][r] - mb[i] * cs[2 * j][r]));
          float uu = bfr(rs[i] * (acc[i][2 * j + 1][r] - mb[i] * cs[2 * j + 1][r]));
          o[r] = (__bf16)(gg * uu);
        }
        *(bf16x4*)(smem + ((mw * 2 + i) * 3 + (ftl >> 1)) * 1024 + lane * 16 + (ftl & 1) * 8) = o;
      }
    }
    asm volatile("s_waitcnt lgkmcnt(0)" ::: "memory");
    bar();
#pragma unroll
    for (int pr = 0; pr < 3; ++pr) {
      bf16x8 v = *(const bf16x8*)(smem + (wv * 3 + pr) * 1024 + lane * 16);
      gstore16_sc1((char*)hh + (size_t)((grp * 8 + wv) * 43 + (ft0 >> 1) + pr) * 1024, lane * 16, v);
    }
    WAIT_VM(0);
    bar();
    if (tid == 0) __hip_atomic_fetch_add(cnt + (16 + grp) * 32, 1u, __ATOMIC_RELAXED, __HIP_MEMORY_SCOPE_AGENT);
    STAMP(8);
  }
  {
    const int mi = wv & 1, kq = wv >> 1;
    const int rt0 = mt * 4 + mi * 2;
    const int u0 = kq * 11;
    const int fi = kq >> 1, fc = (kq & 1) * 2;
    const __bf16* wsrc = wd + (size_t)(u0 * 32 + mi * 16) * D_ + nt * 64;
    const unsigned ldsq = lds0 + kq * 40960 + mi * 2048;
    auto issueB = [&](int s) {
      const int ss = (s == 10 && kq == 3) ? 9 : s;
      glds16s(wsrc + (size_t)(ss * 32) * D_, woff, ldsq + (s % 10) * 4096);
      glds16s(wsrc + (size_t)(ss * 32 + 8) * D_, woff, ldsq + (s % 10) * 4096 + 1024);
    };
    if (wv == 0) { group_wait(cnt + (16 + grp) * 32); WAIT_VM(0); }
#pragma unroll
    for (int s = 0; s < 10; ++s) issueB(s);
    bar();
    STAMP(9);
    bf16x8 af[2][11];
    const char* asrc = (const char*)hh + (size_t)(rt0 * 43 + u0) * 1024;
#pragma unroll
    for (int s = 0; s < 11; ++s) {
      const int ss = (s == 10 && kq == 3) ? 9 : s;
      gload16_sc1(af[0][s], asrc + ss * 1024, lane * 16);
      gload16_sc1(af[1][s], asrc + (43 + ss) * 1024, lane * 16);
    }
    f32x4 acc[2][4];
#pragma unroll
    for (int i = 0; i < 2; ++i)
#pragma unroll
      for (int j = 0; j < 4; ++j) acc[i][j] = (f32x4){0.f, 0.f, 0.f, 0.f};
    const char* bl = smem + kq * 40960 + btr;
    auto comp = [&](int s) {
      TIE(af[0][s]); TIE(af[1][s]);
#pragma unroll
      for (int ct = 0; ct < 4; ++ct) {
        bf16x8 bf = ldB(bl + ((ct ^ bsw) * 32), (s % 10) * 4096, (s % 10) * 4096 + 2048);
        acc[0][ct] = MFMA(bf, af[0][s], acc[0][ct]);
        acc[1][ct] = MFMA(bf, af[1][s], acc[1][ct]);
      }
    };
    WAIT_VM(18);
    bar();
    STAMP(10);
    comp(0); comp(1);
    WAIT_VM(12);
    bar();
    issueB(10);
    bf16x8 hres;
    gload16_sc1(hres, (const char*)hfr + (size_t)((rt0 + fi) * 16 + nt * 2 + (kq & 1)) * 1024, lane * 16);
    comp(2); comp(3); comp(4);
    WAIT_VM(9);
    comp(5); comp(6); comp(7);
    WAIT_VM(1);
    bar();
    comp(8); comp(9);
    if (kq < 3) comp(10);
    STAMP(11);
    WAIT_VM(0);
    TIE(hres);
    bar();
    f32x4* red = (f32x4*)smem;
#pragma unroll
    for (int i = 0; i < 2; ++i)
#pragma unroll
      for (int ct = 0; ct < 4; ++ct)
        if (i != fi || (ct >> 1) != (kq & 1)) red[(((mi * 4 + kq) * 2 + i) * 4 + ct) * 64 + lane] = acc[i][ct];
    __syncthreads();
#pragma unroll
    for (int p = 0; p < 2; ++p) {
      const int ct = fc + p;
      const f32x4 own = fi ? (fc ? acc[1][2 + p] : acc[1][p]) : (fc ? acc[0][2 + p] : acc[0][p]);
      f32x4 v[4];
#pragma unroll
      for (int q = 0; q < 4; ++q) v[q] = q == kq ? own : red[(((mi * 4 + q) * 2 + fi) * 4 + ct) * 64 + lane];
      f32x4 s = (v[0] + v[1]) + (v[2] + v[3]);
      bf16x4 o;
#pragma unroll
      for (int r = 0; r < 4; ++r) o[r] = (__bf16)((float)hres[p * 4 + r] + bfr(s[r]));
      *(bf16x4*)(out + (size_t)((rt0 + fi) * 16 + c) * D_ + nt * 64 + ct * 16 + g * 4) = o;
    }
  }
  STAMP(12); STAMP_OUT(2, pq);
}

extern "C" void kernel_launch(void* const* d_in, const int* in_sizes, int n_in, void* d_out, int out_size,
                              void* d_ws, size_t ws_size, hipStream_t stream) {
  const __bf16* x = (const __bf16*)d_in[0];
  const __bf16* wqkv = (const __bf16*)d_in[1];
  const __bf16* wo = (const __bf16*)d_in[2];
  const __bf16* wg = (const __bf16*)d_in[4];
  const __bf16* wu = (const __bf16*)d_in[5];
  const __bf16* wd = (const __bf16*)d_in[6];
  char* ws = (char*)d_ws;
  __bf16* pq = (__bf16*)(ws);
  __bf16* hfr = (__bf16*)(ws + (2u << 20));
  __bf16* hh = (__bf16*)(ws + (4u << 20));
  float* kvpart = (float*)(ws + (10u << 20));
  __bf16* kvf = (__bf16*)(ws + (13u << 20));
  float* csp = (float*)(ws + (13u << 20) + 65536);
  float* hstat = (float*)(ws + (14u << 20));
  unsigned* cnt = (unsigned*)(ws + (13u << 20) + 131072);
  k1_qkv<<<256, 512, 0, stream>>>(x, wqkv, pq, kvpart, cnt, wo, wg, wu, wd);
  k1b_reduce<<<77, 256, 0, stream>>>(kvpart, kvf, wo, wg, wu, wd, csp);
  k234<<<256, 512, 0, stream>>>(x, wo, pq, kvf, wg, wu, csp, wd, hfr, hstat, hh, cnt, (__bf16*)d_out);
}
```

```cpp
#include <hip/hip_runtime.h>
#include <hip/hip_bf16.h>
#include <stdint.h>

typedef __attribute__((ext_vector_type(8))) __bf16 bf16x8;
typedef __attribute__((ext_vector_type(4))) __bf16 bf16x4;
typedef __attribute__((ext_vector_type(4))) short s16x4;
typedef __attribute__((ext_vector_type(4))) float f32x4;
typedef __attribute__((ext_vector_type(2))) float f32x2;
typedef __attribute__((ext_vector_type(4))) unsigned u32x4;
typedef __attribute__((ext_vector_type(2))) unsigned u32x2;
typedef __attribute__((address_space(3))) s16x4* lds_s16x4_ptr;

#define DI __device__ __forceinline__
#define MFMA(a, b, c) __builtin_amdgcn_mfma_f32_16x16x32_bf16((a), (b), (c), 0, 0, 0)

constexpr int S_ = 2048, D_ = 512, F_ = 1376;
constexpr int KVREC = 272;

#define STAMP_DECL
#define STAMP(i)
#define STAMP_OUT(k, wsbase)
DI float bfr(float x) { return (float)(__bf16)x; }
DI float bf2f(__bf16 x) { return (float)x; }

DI void glds16(const void* gsrc, unsigned lds_dst) {
  unsigned keep;
  asm volatile(
      "s_mov_b32 %0, m0\n\ts_mov_b32 m0, %2\n\ts_nop 0\n\tglobal_load_lds_dwordx4 %1, off\n\ts_mov_b32 m0, %0"
      : "=&s"(keep)
      : "v"(gsrc), "s"(lds_dst)
      : "memory");
}
DI void glds16s(const void* ubase, unsigned voff, unsigned lds_dst) {
  unsigned keep;
  asm volatile(
      "s_mov_b32 %0, m0\n\ts_mov_b32 m0, %3\n\ts_nop 0\n\tglobal_load_lds_dwordx4 %1, %2\n\ts_mov_b32 m0, %0"
      : "=&s"(keep)
      : "v"(voff), "s"(ubase), "s"(lds_dst)
      : "memory");
}
DI void gload16(bf16x8& dst, const void* ubase, unsigned voff) {
  asm volatile("global_load_dwordx4 %0, %1, %2" : "=v"(dst) : "v"(voff), "s"(ubase) : "memory");
}
DI void gload8(bf16x4& dst, const void* ubase, unsigned voff) {
  asm volatile("global_load_dwordx2 %0, %1, %2" : "=v"(dst) : "v"(voff), "s"(ubase) : "memory");
}
DI void gload16_sc1(bf16x8& dst, const void* ubase, unsigned voff) {
  asm volatile("global_load_dwordx4 %0, %1, %2 sc1" : "=v"(dst) : "v"(voff), "s"(ubase) : "memory");
}
DI void glds16s_sc1(const void* ubase, unsigned voff, unsigned lds_dst) {
  unsigned keep;
  asm volatile(
      "s_mov_b32 %0, m0\n\ts_mov_b32 m0, %3\n\ts_nop 0\n\tglobal_load_lds_dwordx4 %1, %2 sc1\n\ts_mov_b32 m0, %0"
      : "=&s"(keep)
      : "v"(voff), "s"(ubase), "s"(lds_dst)
      : "memory");
}
DI void gstore16_sc1(void* ubase, unsigned voff, bf16x8 v) {
  asm volatile("global_store_dwordx4 %0, %1, %2 sc1\n\ts_nop 1" :: "v"(voff), "v"(v), "s"(ubase) : "memory");
}
DI void gstore8_sc1(void* ubase, unsigned voff, bf16x4 v) {
  asm volatile("global_store_dwordx2 %0, %1, %2 sc1\n\ts_nop 1" :: "v"(voff), "v"(v), "s"(ubase) : "memory");
}
DI void gstore8f_sc1(void* ubase, unsigned voff, f32x2 v) {
  asm volatile("global_store_dwordx2 %0, %1, %2 sc1\n\ts_nop 1" :: "v"(voff), "v"(v), "s"(ubase) : "memory");
}
#define TIE(x) asm volatile("" : "+v"(x))
#define WAIT_VM(n) do { __builtin_amdgcn_sched_barrier(0); asm volatile("s_waitcnt vmcnt(" #n ") lgkmcnt(0)" ::: "memory"); } while (0)
DI void wait_vm(int n) {
  switch (n) {
    case 0: WAIT_VM(0); break;   case 1: WAIT_VM(1); break;   case 2: WAIT_VM(2); break;
    case 3: WAIT_VM(3); break;   case 4: WAIT_VM(4); break;   case 5: WAIT_VM(5); break;
    case 6: WAIT_VM(6); break;   case 7: WAIT_VM(7); break;   case 8: WAIT_VM(8); break;
    case 9: WAIT_VM(9); break;   case 10: WAIT_VM(10); break; case 11: WAIT_VM(11); break;
    case 12: WAIT_VM(12); break; case 13: WAIT_VM(13); break; case 14: WAIT_VM(14); break;
    case 15: WAIT_VM(15); break; case 16: WAIT_VM(16); break; case 17: WAIT_VM(17); break;
    case 18: WAIT_VM(18); break; case 19: WAIT_VM(19); break; case 20: WAIT_VM(20); break;
    case 21: WAIT_VM(21); break; case 22: WAIT_VM(22); break; case 23: WAIT_VM(23); break;
    case 24: WAIT_VM(24); break; case 25: WAIT_VM(25); break; case 26: WAIT_VM(26); break;
    case 27: WAIT_VM(27); break; case 28: WAIT_VM(28); break; case 29: WAIT_VM(29); break;
    case 30: WAIT_VM(30); break; case 31: WAIT_VM(31); break; case 32: WAIT_VM(32); break;
    case 33: WAIT_VM(33); break; case 34: WAIT_VM(34); break; case 35: WAIT_VM(35); break;
    case 36: WAIT_VM(36); break; case 37: WAIT_VM(37); break; case 38: WAIT_VM(38); break;
    case 39: WAIT_VM(39); break; case 40: WAIT_VM(40); break; case 41: WAIT_VM(41); break;
    case 42: WAIT_VM(42); break; case 43: WAIT_VM(43); break; case 44: WAIT_VM(44); break;
    default: WAIT_VM(0); break;
  }
}
#define SB __builtin_amdgcn_sched_barrier(0)
#define XMFMA MFMA
#define XRD 1
#define XLD 1
#define YMFMA MFMA
#define YRD 1
#define YLD 1
DI void bar() { __builtin_amdgcn_s_barrier(); __builtin_amdgcn_sched_barrier(0); }

DI bf16x8 ldB(const char* smem_lane_base, int off0, int off1) {
  s16x4 lo = __builtin_amdgcn_ds_read_tr16_b64_v4i16((lds_s16x4_ptr)(smem_lane_base + off0));
  s16x4 hi = __builtin_amdgcn_ds_read_tr16_b64_v4i16((lds_s16x4_ptr)(smem_lane_base + off1));
  typedef __attribute__((ext_vector_type(8))) short s16x8;
  s16x8 v = __builtin_shufflevector(lo, hi, 0, 1, 2, 3, 4, 5, 6, 7);
  return __builtin_bit_cast(bf16x8, v);
}

DI float phi(float t) {
  float r = t > 0.f ? bfr(t + 1.0f) : bfr(__expf(t));
  return bfr(r + 1e-6f);
}

constexpr int K1_RING = 65536 + 7 * 12288;
constexpr int K1_LDS = K1_RING + 4096;
constexpr int k1_gcnt(int n, int lo) { return n > 15 ? 0 : ((lo ? 2 : 1) + ((n & 1) == 0 ? 1 : 0)); }
constexpr int k1_younger(int n, int lo) {
  return k1_gcnt(n + 1, lo) + k1_gcnt(n + 2, lo) + k1_gcnt(n + 3, lo) + k1_gcnt(n + 4, lo);
}
__global__ __launch_bounds__(512, 2) void k1_qkv(const __bf16* x, const __bf16* w, __bf16* pq, float* kvpart, unsigned* cnt,
                                                 const __bf16* wo, const __bf16* wg, const __bf16* wu, const __bf16* wd) {
  __shared__ __attribute__((aligned(16))) char smem[K1_LDS];
  STAMP_DECL; STAMP(0);
  const int tid = threadIdx.x, lane = tid & 63, wv = __builtin_amdgcn_readfirstlane(tid >> 6);
  const int g = lane >> 4, c = lane & 15;
  const int hw = wv & 3, rh = wv >> 2;
  const int b = blockIdx.x, xcd = b & 7, idx = b >> 3;
  const int mt = (xcd >> 1) * 8 + (idx >> 2), hq = (xcd & 1) * 4 + (idx & 3);
  const unsigned lds0 = (unsigned)(size_t)smem;

  const __bf16* xsrc = x + (size_t)(mt * 64 + wv * 8) * D_;
  const __bf16* wsrc0 = w + (size_t)((wv & 3) * 8) * 1536 + (wv >> 2) * 512 + hq * 64;
  const __bf16* wsrc1 = w + (size_t)((wv & 3) * 8) * 1536 + 1024 + hq * 64;
  unsigned xoff, woff;
  {
    const int r = lane >> 3, pos = lane & 7;
    const int row = wv * 8 + r;
    xoff = r * (D_ * 2) + (pos ^ ((row >> 1) & 7)) * 16;
    const int swz = ((r >> 1) & 1) | ((wv & 1) << 1);
    woff = r * 3072 + ((pos >> 1) ^ swz) * 32 + (pos & 1) * 16;
  }
  const int rot = 0;
  auto issue1 = [&](int n, int e) {
    const unsigned slot = lds0 + 65536 + (n % 7) * 12288;
    const int kn = (n + rot) & 15;
    if (e == 0) glds16s(wsrc0 + (size_t)kn * 32 * 1536, woff, slot + wv * 1024);
    if (e == 1) { if (wv < 4) glds16s(wsrc1 + (size_t)kn * 32 * 1536, woff, slot + 8192 + wv * 1024); }
    if (e == 2) { if ((n & 1) == 0) glds16s(xsrc + (kn >> 1) * 64, xoff, lds0 + (n >> 1) * 8192 + wv * 1024); }
  };
#pragma unroll
  for (int n = 0; n < 6; ++n) { issue1(n, 0); issue1(n, 1); issue1(n, 2); }
  STAMP(1);

  f32x4 acc[2][3];
#pragma unroll
  for (int i = 0; i < 2; ++i)
#pragma unroll
    for (int j = 0; j < 3; ++j) acc[i][j] = (f32x4){0.f, 0.f, 0.f, 0.f};
  f32x4 stt = {0.f, 0.f, 0.f, 0.f};
  f32x4 csum[3];
#pragma unroll
  for (int j = 0; j < 3; ++j) csum[j] = (f32x4){0.f, 0.f, 0.f, 0.f};
  bf16x8 ones;
#pragma unroll
  for (int j = 0; j < 8; ++j) ones[j] = (__bf16)1.0f;

  const int aoff = (rh * 32 + c) * 128 + ((g ^ (c >> 1)) * 16);
  const int boff = 65536 + g * 1024 + (c >> 2) * 128 + ((hw ^ (((c >> 3) & 1) | ((g & 1) << 1))) * 32) + (c & 3) * 8;
  bf16x8 fa[2][2], fb[2][3], pfx[3];
  auto rdA = [&](int n, int bu) {
    const char* sl = smem + (n >> 1) * 8192 + (aoff ^ ((n & 1) << 6));
    fa[bu][0] = *(const bf16x8*)(sl);
    fa[bu][1] = *(const bf16x8*)(sl + 2048);
  };
  auto rdB = [&](int n, int bu, int p) { fb[bu][p] = ldB(smem + boff + (n % 7) * 12288 + p * 4096, 0, 512); };
  if (wv < 4) wait_vm(k1_gcnt(5, 1) + k1_younger(0, 1)); else wait_vm(k1_gcnt(5, 0) + k1_younger(0, 0));
  bar();
  STAMP(8);
  rdA(0, 0); rdB(0, 0, 0); rdB(0, 0, 1); rdB(0, 0, 2);
#pragma unroll
  for (int n = 0; n < 16; ++n) {
    const int cu = n & 1, nx = cu ^ 1;
    if (n < 15) {
      if (wv < 4) wait_vm(k1_younger(n + 1, 1) + (n > 10 ? 3 : 0)); else wait_vm(k1_younger(n + 1, 0) + (n > 10 ? 3 : 0));
      bar();
    }
    if (n == 10) {
#pragma unroll
      for (int j = 0; j < 3; ++j) {
        int pc = b * 8 + wv + 2048 * j;
        pc = pc < 4640 ? pc : pc - 2048;
        const char* src = pc < 512 ? (const char*)wo + (size_t)pc * 1024
                        : pc < 1888 ? (const char*)wg + (size_t)(pc - 512) * 1024
                        : pc < 3264 ? (const char*)wu + (size_t)(pc - 1888) * 1024 : (const char*)wd + (size_t)(pc - 3264) * 1024;
        gload16(pfx[j], src, lane * 16);
      }
    }
    acc[0][0] = XMFMA(fb[cu][0], fa[cu][0], acc[0][0]); SB;
    if (XRD && n < 15) rdA(n + 1, nx);
    SB;
    acc[1][0] = XMFMA(fb[cu][0], fa[cu][1], acc[1][0]); SB;
    if (XRD && n < 15) rdB(n + 1, nx, 0);
    SB;
    acc[0][1] = XMFMA(fa[cu][0], fb[cu][1], acc[0][1]); SB;
    if (XRD && n < 15) rdB(n + 1, nx, 1);
    SB;
    acc[1][1] = XMFMA(fa[cu][1], fb[cu][1], acc[1][1]); SB;
    if (XRD && n < 15) rdB(n + 1, nx, 2);
    SB;
    acc[0][2] = XMFMA(fa[cu][0], fb[cu][2], acc[0][2]); SB;
    if (XLD && n + 6 < 16) issue1(n + 6, 0);
    SB;
    acc[1][2] = XMFMA(fa[cu][1], fb[cu][2], acc[1][2]); SB;
    if (XLD && n + 6 < 16) issue1(n + 6, 1);
    SB;
    if (hw == 0) stt = XMFMA(ones, fa[cu][0], stt);
    else if (hw == 1) stt = XMFMA(fa[cu][0], fa[cu][0], stt);
    else if (hw == 2) stt = XMFMA(ones, fa[cu][1], stt);
    else stt = XMFMA(fa[cu][1], fa[cu][1], stt);
    SB;
    if (XLD && n + 6 < 16) issue1(n + 6, 2);
    SB;
    if (cu == rh) {
      csum[0] = XMFMA(ones, fb[cu][0], csum[0]);
      csum[1] = XMFMA(ones, fb[cu][1], csum[1]);
      csum[2] = XMFMA(ones, fb[cu][2], csum[2]);
    }
  }
  STAMP(6);
  float* tsum = (float*)(smem + K1_RING);
  float* tsq = tsum + 64;
  float* tcs = tsum + 128;
  float* tst = tsum + 512;
  {
    const int row = rh * 32 + (hw >> 1) * 16 + c;
    if ((hw & 1) == 0) { if (g == 0) tsum[row] = stt[0]; }
    else if (g == (c >> 2)) tsq[row] = (c & 2) ? ((c & 1) ? stt[3] : stt[2]) : ((c & 1) ? stt[1] : stt[0]);
    if (g == 0) {
#pragma unroll
      for (int p = 0; p < 3; ++p) tcs[rh * 192 + p * 64 + hw * 16 + c] = csum[p][0];
    }
  }
  __syncthreads();
  if (tid < 64) {
    const float mean = tsum[tid] * (1.0f / 512.0f);
    const float var = fmaxf(tsq[tid] * (1.0f / 512.0f) - mean * mean, 0.f);
    const float sb = bfr(sqrtf(bfr(bfr(var) + 1e-5f)));
    *(f32x2*)(tst + tid * 2) = (f32x2){bfr(mean), 1.0f / sb};
  }
  __syncthreads();
  const f32x4 csq = *(const f32x4*)(tcs + hw * 16 + 4 * g) + *(const f32x4*)(tcs + 192 + hw * 16 + 4 * g);
  const float csk = tcs[64 + hw * 16 + c] + tcs[192 + 64 + hw * 16 + c];
  const float csv = tcs[128 + hw * 16 + c] + tcs[192 + 128 + hw * 16 + c];
#pragma unroll
  for (int i = 0; i < 2; ++i) {
    const f32x2 st = *(const f32x2*)(tst + (rh * 32 + i * 16 + c) * 2);
    bf16x4 o;
#pragma unroll
    for (int r = 0; r < 4; ++r) o[r] = (__bf16)phi(bfr(st[1] * (acc[i][0][r] - st[0] * csq[r])));
    const int rt = mt * 4 + rh * 2 + i;
    *(bf16x4*)(pq + ((size_t)(rt * 16 + hq * 2 + (hw >> 1)) * 64 + lane) * 8 + (hw & 1) * 4) = o;
  }
  STAMP(7);
  {
    bf16x8 pk, vv;
#pragma unroll
    for (int i = 0; i < 2; ++i)
#pragma unroll
      for (int r = 0; r < 4; r += 2) {
        const f32x4 st = *(const f32x4*)(tst + (rh * 32 + i * 16 + 4 * g + r) * 2);
        pk[i * 4 + r] = (__bf16)phi(bfr(st[1] * (acc[i][1][r] - st[0] * csk)));
        pk[i * 4 + r + 1] = (__bf16)phi(bfr(st[3] * (acc[i][1][r + 1] - st[2] * csk)));
        vv[i * 4 + r] = (__bf16)(st[1] * (acc[i][2][r] - st[0] * csv));
        vv[i * 4 + r + 1] = (__bf16)(st[3] * (acc[i][2][r + 1] - st[2] * csv));
      }
    const f32x4 z4 = {0.f, 0.f, 0.f, 0.f};
    f32x4 kv = MFMA(pk, vv, z4);
    f32x4 zz = MFMA(pk, ones, z4);
    float* dst = kvpart + (size_t)((mt * 2 + rh) * 32 + hq * 4 + hw) * KVREC;
    *(f32x4*)(dst + lane * 4) = kv;
    if (c == 0) *(f32x4*)(dst + 256 + g * 4) = zz;
  }
  if (b == 0 && tid < 32) cnt[tid * 32] = 0u;
  WAIT_VM(0);
  TIE(pfx[0]); TIE(pfx[1]); TIE(pfx[2]);
  STAMP(2); STAMP_OUT(0, pq);
}

__global__ __launch_bounds__(256) void k1b_reduce(const float* kvpart, __bf16* kvf, const __bf16* wo, const __bf16* wg,
                                                  const __bf16* wu, const __bf16* wd, float* csp) {
  __shared__ __attribute__((aligned(16))) float red[32 * 64];
  STAMP_DECL; STAMP(0);
  const int tid = threadIdx.x, b = blockIdx.x, lane = tid & 63, wv = __builtin_amdgcn_readfirstlane(tid >> 6);
  if (b < 34) {
    const int q = b * 64 + lane;
    f32x4 v[16];
#pragma unroll
    for (int m = 0; m < 16; ++m) v[m] = *(const f32x4*)(kvpart + (size_t)(wv * 16 + m) * 32 * KVREC + q * 4);
    f32x4 s = v[0];
#pragma unroll
    for (int m = 1; m < 16; ++m) s += v[m];
    f32x4* r4 = (f32x4*)red;
    r4[tid] = s;
    __syncthreads();
    if (tid < 64) {
      f32x4 t = (r4[tid] + r4[64 + tid]) + (r4[128 + tid] + r4[192 + tid]);
      bf16x4 o;
#pragma unroll
      for (int j = 0; j < 4; ++j) o[j] = (__bf16)t[j];
      *(bf16x4*)(kvf + q * 4) = o;
    }
    STAMP(1); STAMP_OUT(1, (const char*)kvpart - (10u << 20));
    return;
  }
  if (b >= 77) {
    const int pb = b - 77;
    const u32x4* src = pb < 32 ? (const u32x4*)wo + (size_t)pb * 1024 : (const u32x4*)wd + (size_t)(pb - 32) * 1024;
    u32x4 v0 = src[tid], v1 = src[256 + tid], v2 = src[512 + tid], v3 = src[768 + tid];
    asm volatile("" :: "v"(v0), "v"(v1), "v"(v2), "v"(v3));
    return;
  }
  const int cb = b - 34;
  const int col0 = cb * 64 + (tid & 7) * 8;
  const __bf16* src = col0 < F_ ? (wg + col0) : (wu + (col0 - F_));
  const int kq = tid >> 3;
  bf16x8 v[16];
#pragma unroll
  for (int r = 0; r < 16; ++r) v[r] = *(const bf16x8*)(src + (size_t)(kq * 16 + r) * F_);
  float s[8];
#pragma unroll
  for (int j = 0; j < 8; ++j) s[j] = 0.f;
#pragma unroll
  for (int r = 0; r < 16; ++r)
#pragma unroll
    for (int j = 0; j < 8; ++j) s[j] += (float)v[r][j];
#pragma unroll
  for (int j = 0; j < 8; ++j) red[kq * 64 + (tid & 7) * 8 + j] = s[j];
  __syncthreads();
  if (tid < 64) {
    float t = 0.f;
#pragma unroll
    for (int q = 0; q < 32; ++q) t += red[q * 64 + tid];
    csp[cb * 64 + tid] = t;
  }
  STAMP(2); STAMP_OUT(1, (const char*)kvpart - (10u << 20));
}

constexpr int B3_RING = 6 * 12288;
constexpr int R_OFF = B3_RING;
constexpr int p3_wait(int n, int lo) {
  const int gs = lo ? 3 : 2;
  int c = 0;
  if (n + 1 <= 5) {
    c = (n + 1 == 1) ? 6 : 5 - (n + 1);
    c += gs * n;
  } else {
    for (int j = n - 4; j <= n - 1; ++j) c += gs;
  }
  return c;
}
DI int p4_slot(int q) {
  if (q < 25) {
    const int i = q / 5, j = q - i * 5, sl = (10 + i) % 6;
    return j < 3 ? sl * 12288 + j * 4096 : R_OFF + sl * 8192 + (j - 3) * 4096;
  }
  const int r = q - 25;
  return r < 3 ? 3 * 12288 + r * 4096 : (r < 5 ? R_OFF + 3 * 8192 + (r - 3) * 4096 : R_OFF + 49152 + (r - 5) * 4096);
}
DI void group_wait(unsigned* cnt) {
  unsigned spins = 0;
  while ((unsigned)__builtin_amdgcn_readfirstlane(__hip_atomic_load(cnt, __ATOMIC_RELAXED, __HIP_MEMORY_SCOPE_AGENT)) < 16u) {
    __builtin_amdgcn_s_sleep(1);
    if (++spins > (1u << 22)) break;
  }
}
__global__ __launch_bounds__(512, 2) void k234(const __bf16* x, const __bf16* wo, const __bf16* pq, const __bf16* kvf,
                                               const __bf16* wg, const __bf16* wu, const float* csp, const __bf16* wd,
                                               __bf16* hfr, float* hstat, __bf16* hh, unsigned* cnt, __bf16* out) {
  __shared__ __attribute__((aligned(16))) char smem[163840];
  STAMP_DECL; STAMP(0);
  const int tid = threadIdx.x, lane = tid & 63, wv = __builtin_amdgcn_readfirstlane(tid >> 6);
  const int g = lane >> 4, c = lane & 15;
  const int b = blockIdx.x, xcd = b & 7, idx = b >> 3;
  const int grp = xcd * 2 + (idx >> 4), mem = idx & 15;
  const unsigned lds0 = (unsigned)(size_t)smem;
  const int mt = grp * 2 + (mem >> 3), nt = mem & 7;
  const int ft0 = 2 * ((mem * 43) >> 4);
  const unsigned woff = (lane >> 3) * (D_ * 2) + ((((lane & 7) >> 1) ^ ((lane >> 4) & 3)) * 32) + (lane & 1) * 16;
  const int brow = (g & 1) * 4 + (c >> 2);
  const int bsw = (brow >> 1) & 3;
  const int btr = (g >> 1) * 1024 + brow * 128 + (c & 3) * 8;

  const unsigned boffl = (lane >> 2) * (F_ * 2) + ((((lane >> 1) & 1) ^ ((lane >> 4) & 1)) * 32) + (lane & 1) * 16;
  const __bf16* pB0 = (wv < 6 ? wg : wu) + (size_t)((wv & 1) * 16) * F_ + (ft0 + ((wv % 6) >> 1) * 2) * 16;
  const __bf16* pB1 = wu + (size_t)((wv & 1) * 16) * F_ + (ft0 + ((2 + (wv & 3)) >> 1) * 2) * 16;
  auto issueB3 = [&](int n, int e) {
    const unsigned slot = lds0 + (n % 6) * 12288;
    if (e == 0) glds16s(pB0 + (size_t)n * 32 * F_, boffl, slot + wv * 1024);
    if (e == 1) { if (wv < 4) glds16s(pB1 + (size_t)n * 32 * F_, boffl, slot + 8192 + wv * 1024); }
  };

  auto issueB4 = [&](int q, int pc) {
    const int kq = q & 3, st = q >> 2;
    glds16s(wd + (size_t)((kq * 11 + st) * 32 + pc * 8) * D_ + nt * 64, woff, lds0 + p4_slot(q) + pc * 1024);
  };

  {
    const int rg = wv & 3, kh = wv >> 2;
    const int rt = mt * 4 + rg;
    bf16x8 pf[8];
#pragma unroll
    for (int h = 0; h < 8; ++h) gload16(pf[h], (const char*)pq + (size_t)(rt * 16 + kh * 8 + h) * 1024, lane * 16);
#pragma unroll
    for (int i = 0; i < 3; ++i) {
      const int ins = i * 8 + wv;
      if (ins < 17) glds16s((const char*)kvf + ins * 1024, lane * 16, lds0 + R_OFF + 65536 + ins * 1024);
    }
#pragma unroll
    for (int i = 0; i < 8; ++i) {
      const int kb = i * 8 + wv;
      glds16s(wo + (size_t)kb * 8 * D_ + nt * 64, woff, lds0 + R_OFF + kb * 1024);
    }
    bf16x4 xr[2];
#pragma unroll
    for (int p = 0; p < 2; ++p) gload8(xr[p], x + (size_t)(rt * 16) * D_ + nt * 64 + (kh * 2 + p) * 16, c * (D_ * 2) + g * 8);
    f32x4 acc[4];
#pragma unroll
    for (int j = 0; j < 4; ++j) acc[j] = (f32x4){0.f, 0.f, 0.f, 0.f};
    STAMP(1);
    WAIT_VM(2);
    bar();
    STAMP(2);
    const char* bl = smem + R_OFF + kh * 32768 + btr;
    const char* kvl = smem + R_OFF + 65536 + kh * 16 * (KVREC * 2);
    const f32x4 z4 = {0.f, 0.f, 0.f, 0.f};
    f32x4 n0[8], n1[8];
    float e0[8], e1[8];
#pragma unroll
    for (int h = 0; h < 8; ++h) {
      TIE(pf[h]);
      bf16x4 k0 = *(const bf16x4*)(kvl + (2 * h) * (KVREC * 2) + lane * 8);
      bf16x4 k1 = *(const bf16x4*)(kvl + (2 * h + 1) * (KVREC * 2) + lane * 8);
      bf16x4 z0 = *(const bf16x4*)(kvl + (2 * h) * (KVREC * 2) + 512 + g * 8);
      bf16x4 z1 = *(const bf16x4*)(kvl + (2 * h + 1) * (KVREC * 2) + 512 + g * 8);
      const bf16x4 zz = {(__bf16)0.f, (__bf16)0.f, (__bf16)0.f, (__bf16)0.f};
      bf16x8 a0 = __builtin_shufflevector(k0, zz, 0, 1, 2, 3, 4, 5, 6, 7);
      bf16x8 a1 = __builtin_shufflevector(zz, k1, 0, 1, 2, 3, 4, 5, 6, 7);
      bf16x8 d0 = __builtin_shufflevector(z0, zz, 0, 1, 2, 3, 4, 5, 6, 7);
      bf16x8 d1 = __builtin_shufflevector(zz, z1, 0, 1, 2, 3, 4, 5, 6, 7);
      n0[h] = MFMA(a0, pf[h], z4);
      n1[h] = MFMA(a1, pf[h], z4);
      e0[h] = MFMA(d0, pf[h], z4)[0];
      e1[h] = MFMA(d1, pf[h], z4)[0];
    }
#pragma unroll
    for (int n = 0; n < 6; ++n) { issueB3(n, 0); issueB3(n, 1); }
#pragma unroll
    for (int h = 0; h < 8; ++h) {
      const float r0 = __builtin_amdgcn_rcpf(bfr(bfr(e0[h]) + 1e-6f));
      const float r1 = __builtin_amdgcn_rcpf(bfr(bfr(e1[h]) + 1e-6f));
      bf16x8 nb;
#pragma unroll
      for (int r = 0; r < 4; ++r) { nb[r] = (__bf16)n0[h][r]; nb[4 + r] = (__bf16)n1[h][r]; }
      const u32x4 nu = __builtin_bit_cast(u32x4, nb);
      bf16x8 of;
#pragma unroll
      for (int q = 0; q < 4; ++q) {
        const float rr = q < 2 ? r0 : r1;
        of[2 * q] = (__bf16)(__builtin_bit_cast(float, nu[q] << 16) * rr);
        of[2 * q + 1] = (__bf16)(__builtin_bit_cast(float, nu[q] & 0xffff0000u) * rr);
      }
#pragma unroll
      for (int ct = 0; ct < 4; ++ct) {
        bf16x8 wf = ldB(bl + ((ct ^ bsw) * 32), h * 4096, h * 4096 + 2048);
        acc[ct] = MFMA(wf, of, acc[ct]);
      }
    }
    STAMP(3);
    if (wv < 4) WAIT_VM(12); else WAIT_VM(6);
    bar();
    TIE(xr[0]); TIE(xr[1]);
    f32x4* red = (f32x4*)(smem + R_OFF);
    red[((rg * 2 + kh) * 2 + 0) * 64 + lane] = kh ? acc[0] : acc[2];
    red[((rg * 2 + kh) * 2 + 1) * 64 + lane] = kh ? acc[1] : acc[3];
    asm volatile("s_waitcnt lgkmcnt(0)" ::: "memory");
    bar();
    f32x4 f0 = (kh ? acc[2] : acc[0]), f1 = (kh ? acc[3] : acc[1]);
    {
      f32x4 o0 = red[((rg * 2 + (kh ^ 1)) * 2 + 0) * 64 + lane], o1 = red[((rg * 2 + (kh ^ 1)) * 2 + 1) * 64 + lane];
      f0 = kh ? (o0 + f0) : (f0 + o0);
      f1 = kh ? (o1 + f1) : (f1 + o1);
    }
    float hv[8];
    float s = 0.f;
#pragma unroll
    for (int r = 0; r < 4; ++r) {
      hv[r] = bfr((float)xr[0][r] + bfr(f0[r]));
      hv[4 + r] = bfr((float)xr[1][r] + bfr(f1[r]));
      s += hv[r] + hv[4 + r];
    }
    {
      bf16x8 o;
#pragma unroll
      for (int r = 0; r < 8; ++r) o[r] = (__bf16)hv[r];
      gstore16_sc1((char*)hfr + (size_t)(rt * 16 + nt * 2 + kh) * 1024, lane * 16, o);
    }
    s += __shfl_xor(s, 16, 64);
    s += __shfl_xor(s, 32, 64);
    const float mean = s * (1.0f / 32.0f);
    float m2 = 0.f;
#pragma unroll
    for (int r = 0; r < 8; ++r) { float d = hv[r] - mean; m2 += d * d; }
    m2 += __shfl_xor(m2, 16, 64);
    m2 += __shfl_xor(m2, 32, 64);
    if (g == 0) gstore8f_sc1((char*)hstat + (size_t)(rt * 16 + nt * 2 + kh) * 128, c * 8, (f32x2){s, m2});
    WAIT_VM(0);
    bar();
    if (tid == 0) __hip_atomic_fetch_add(cnt + grp * 32, 1u, __ATOMIC_RELAXED, __HIP_MEMORY_SCOPE_AGENT);
    STAMP(4);
  }
  {
    if (wv == 0) group_wait(cnt + grp * 32);
    WAIT_VM(0);
    bar();
    STAMP(5);
    const int mw = wv & 3, nw = wv >> 2;
    const int rt0 = grp * 8 + mw * 2;
    const char* pA = (const char*)hfr + (size_t)(grp * 8 + wv) * 16384;
    auto issueA = [&](int n) { glds16s_sc1(pA + n * 1024, lane * 16, lds0 + R_OFF + (n % 6) * 8192 + wv * 1024); };
    bf16x8 csr[6];
#pragma unroll
    for (int ct = 0; ct < 6; ++ct) gload16(csr[ct], csp + (ct & 1) * F_ + (ft0 + nw * 3 + (ct >> 1)) * 16, g * 16);
    issueA(0); issueA(1);
    glds16s_sc1((const char*)hstat + (size_t)grp * 16384 + wv * 2048, lane * 16, lds0 + R_OFF + 49152 + wv * 2048);
    glds16s_sc1((const char*)hstat + (size_t)grp * 16384 + wv * 2048 + 1024, lane * 16, lds0 + R_OFF + 49152 + wv * 2048 + 1024);
    issueA(2); issueA(3); issueA(4); issueA(5);

    f32x4 acc[2][6];
#pragma unroll
    for (int i = 0; i < 2; ++i)
#pragma unroll
      for (int j = 0; j < 6; ++j) acc[i][j] = (f32x4){0.f, 0.f, 0.f, 0.f};
    bf16x8 fa[2][2], fb[2][6];
    const int bl0 = (4 * g + (c >> 2)) * 64 + (c & 3) * 8;
    auto rdA = [&](int n, int bu) {
      const char* sl = smem + R_OFF + (n % 6) * 8192 + lane * 16;
      fa[bu][0] = *(const bf16x8*)(sl + (mw * 2) * 1024);
      fa[bu][1] = *(const bf16x8*)(sl + (mw * 2 + 1) * 1024);
    };
    auto rdB = [&](int n, int bu, int ct) {
      const int ftl = nw * 3 + (ct >> 1);
      const char* bp = smem + (n % 6) * 12288 + bl0 + ((ct & 1) * 6 + (ftl >> 1) * 2) * 1024 + (((ftl & 1) ^ (g & 1)) * 32);
      fb[bu][ct] = ldB(bp, 0, 1024);
    };
    WAIT_VM(7);
    bar();
    STAMP(6);
    rdA(0, 0);
#pragma unroll
    for (int ct = 0; ct < 6; ++ct) rdB(0, 0, ct);
    float mb[2], rs[2];
    f32x4 cs[6];
#pragma unroll
    for (int n = 0; n < 16; ++n) {
      if (n < 15) {
        if (wv < 4) wait_vm(p3_wait(n, 1)); else wait_vm(p3_wait(n, 0));
        bar();
      }
      if (n == 2) {
#pragma unroll
        for (int i = 0; i < 2; ++i) {
          const char* sp = smem + R_OFF + 49152 + ((mw * 2 + i) * 16 + g * 4) * 128 + c * 8;
          const f32x2 t0 = *(const f32x2*)(sp), t1 = *(const f32x2*)(sp + 128), t2 = *(const f32x2*)(sp + 256), t3 = *(const f32x2*)(sp + 384);
          const f32x4 s0 = {t0[0], t0[1], t1[0], t1[1]}, s1 = {t2[0], t2[1], t3[0], t3[1]};
          float s = (s0[0] + s0[2]) + (s1[0] + s1[2]);
          s += __shfl_xor(s, 16, 64);
          s += __shfl_xor(s, 32, 64);
          const float mean = s * (1.0f / 512.0f);
          const float d0 = s0[0] * (1.0f / 32.0f) - mean, d1 = s0[2] * (1.0f / 32.0f) - mean;
          const float d2 = s1[0] * (1.0f / 32.0f) - mean, d3 = s1[2] * (1.0f / 32.0f) - mean;
          float m2 = (s0[1] + s0[3]) + (s1[1] + s1[3]) + 32.0f * ((d0 * d0 + d1 * d1) + (d2 * d2 + d3 * d3));
          m2 += __shfl_xor(m2, 16, 64);
          m2 += __shfl_xor(m2, 32, 64);
          mb[i] = bfr(mean);
          rs[i] = 1.0f / bfr(sqrtf(bfr(bfr(m2 * (1.0f / 512.0f)) + 1e-5f)));
        }
      }
#pragma unroll
      for (int ct = 0; ct < 6; ++ct) {
        acc[0][ct] = YMFMA(fb[n & 1][ct], fa[n & 1][0], acc[0][ct]);
        SB;
        if (YRD && n < 15) { if (ct == 0) rdA(n + 1, (n + 1) & 1); rdB(n + 1, (n + 1) & 1, ct); }
        SB;
        acc[1][ct] = YMFMA(fb[n & 1][ct], fa[n & 1][1], acc[1][ct]);
        SB;
        if (YLD && n + 6 < 16) { if (ct == 1) issueA(n + 6); if (ct == 2) issueB3(n + 6, 0); if (ct == 3) issueB3(n + 6, 1); }
        if (n >= 10 && n < 15) {
          if (ct == 1) issueB4((n - 10) * 5 + (wv >> 2), wv & 3);
          if (ct == 2) issueB4((n - 10) * 5 + 2 + (wv >> 2), wv & 3);
          if (ct == 3) { if (wv < 4) issueB4((n - 10) * 5 + 4, wv & 3); }
        }
        SB;
      }
    }
#pragma unroll
    for (int ct = 0; ct < 6; ++ct) { TIE(csr[ct]); cs[ct] = __builtin_bit_cast(f32x4, csr[ct]); }
    STAMP(7);
#pragma unroll
    for (int i = 0; i < 2; ++i) {
#pragma unroll
      for (int j = 0; j < 3; ++j) {
        const int ftl = nw * 3 + j;
        bf16x4 o;
#pragma unroll
        for (int r = 0; r < 4; ++r) {
          float gg = bfr(rs[i] * (acc[i][2 * j][r] - mb[i] * cs[2 * j][r]));
          float uu = bfr(rs[i] * (acc[i][2 * j + 1][r] - mb[i] * cs[2 * j + 1][r]));
          o[r] = (__bf16)(gg * uu);
        }
        *(bf16x4*)(smem + 139264 + ((mw * 2 + i) * 3 + (ftl >> 1)) * 1024 + lane * 16 + (ftl & 1) * 8) = o;
      }
    }
    asm volatile("s_waitcnt lgkmcnt(0)" ::: "memory");
    bar();
#pragma unroll
    for (int pr = 0; pr < 3; ++pr) {
      bf16x8 v = *(const bf16x8*)(smem + 139264 + (wv * 3 + pr) * 1024 + lane * 16);
      gstore16_sc1((char*)hh + (size_t)((grp * 8 + wv) * 43 + (ft0 >> 1) + pr) * 1024, lane * 16, v);
    }
    WAIT_VM(0);
    bar();
    if (tid == 0) __hip_atomic_fetch_add(cnt + (16 + grp) * 32, 1u, __ATOMIC_RELAXED, __HIP_MEMORY_SCOPE_AGENT);
    STAMP(8);
  }
  {
    const int mi = wv & 1, kq = wv >> 1;
    const int rt0 = mt * 4 + mi * 2;
    const int u0 = kq * 11;
    const int fi = kq >> 1, fc = (kq & 1) * 2;
    if (wv == 0) { group_wait(cnt + (16 + grp) * 32); WAIT_VM(0); }
#pragma unroll
    for (int e = 0; e < 8; ++e) {
      const int q = 25 + e * 2 + (wv >> 2);
      if (q < 40) issueB4(q, wv & 3);
    }
    bar();
    STAMP(9);
    bf16x8 af[2][11];
    const char* asrc = (const char*)hh + (size_t)(rt0 * 43 + u0) * 1024;
#pragma unroll
    for (int s = 0; s < 11; ++s) {
      const int ss = (s == 10 && kq == 3) ? 9 : s;
      gload16_sc1(af[0][s], asrc + ss * 1024, lane * 16);
      gload16_sc1(af[1][s], asrc + (43 + ss) * 1024, lane * 16);
    }
    f32x4 acc[2][4];
#pragma unroll
    for (int i = 0; i < 2; ++i)
#pragma unroll
      for (int j = 0; j < 4; ++j) acc[i][j] = (f32x4){0.f, 0.f, 0.f, 0.f};
    auto comp = [&](int s) {
      TIE(af[0][s]); TIE(af[1][s]);
      const char* bl = smem + p4_slot((s % 10) * 4 + kq) + btr;
#pragma unroll
      for (int ct = 0; ct < 4; ++ct) {
        bf16x8 bf = ldB(bl + ((ct ^ bsw) * 32), 0, 2048);
        acc[0][ct] = MFMA(bf, af[0][s], acc[0][ct]);
        acc[1][ct] = MFMA(bf, af[1][s], acc[1][ct]);
      }
    };
    WAIT_VM(18);
    bar();
    STAMP(10);
    comp(0); comp(1);
    WAIT_VM(12);
    bar();
    {
      const int ss = kq == 3 ? 9 : 10;
      glds16s(wd + (size_t)((u0 + ss) * 32 + mi * 16) * D_ + nt * 64, woff, lds0 + p4_slot(kq) + mi * 2048);
      glds16s(wd + (size_t)((u0 + ss) * 32 + mi * 16 + 8) * D_ + nt * 64, woff, lds0 + p4_slot(kq) + mi * 2048 + 1024);
    }
    bf16x8 hres;
    gload16_sc1(hres, (const char*)hfr + (size_t)((rt0 + fi) * 16 + nt * 2 + (kq & 1)) * 1024, lane * 16);
    comp(2); comp(3); comp(4);
    WAIT_VM(9);
    comp(5); comp(6); comp(7);
    WAIT_VM(1);
    bar();
    comp(8); comp(9);
    if (kq < 3) comp(10);
    STAMP(11);
    WAIT_VM(0);
    TIE(hres);
    bar();
    f32x4* red = (f32x4*)smem;
#pragma unroll
    for (int i = 0; i < 2; ++i)
#pragma unroll
      for (int ct = 0; ct < 4; ++ct)
        if (i != fi || (ct >> 1) != (kq & 1)) red[(((mi * 4 + kq) * 2 + i) * 4 + ct) * 64 + lane] = acc[i][ct];
    __syncthreads();
#pragma unroll
    for (int p = 0; p < 2; ++p) {
      const int ct = fc + p;
      const f32x4 own = fi ? (fc ? acc[1][2 + p] : acc[1][p]) : (fc ? acc[0][2 + p] : acc[0][p]);
      f32x4 v[4];
#pragma unroll
      for (int q = 0; q < 4; ++q) v[q] = q == kq ? own : red[(((mi * 4 + q) * 2 + fi) * 4 + ct) * 64 + lane];
      f32x4 s = (v[0] + v[1]) + (v[2] + v[3]);
      bf16x4 o;
#pragma unroll
      for (int r = 0; r < 4; ++r) o[r] = (__bf16)((float)hres[p * 4 + r] + bfr(s[r]));
      *(bf16x4*)(out + (size_t)((rt0 + fi) * 16 + c) * D_ + nt * 64 + ct * 16 + g * 4) = o;
    }
  }
  STAMP(12); STAMP_OUT(2, pq);
}

extern "C" void kernel_launch(void* const* d_in, const int* in_sizes, int n_in, void* d_out, int out_size,
                              void* d_ws, size_t ws_size, hipStream_t stream) {
  const __bf16* x = (const __bf16*)d_in[0];
  const __bf16* wqkv = (const __bf16*)d_in[1];
  const __bf16* wo = (const __bf16*)d_in[2];
  const __bf16* wg = (const __bf16*)d_in[4];
  const __bf16* wu = (const __bf16*)d_in[5];
  const __bf16* wd = (const __bf16*)d_in[6];
  char* ws = (char*)d_ws;
  __bf16* pq = (__bf16*)(ws);
  __bf16* hfr = (__bf16*)(ws + (2u << 20));
  __bf16* hh = (__bf16*)(ws + (4u << 20));
  float* kvpart = (float*)(ws + (10u << 20));
  __bf16* kvf = (__bf16*)(ws + (13u << 20));
  float* csp = (float*)(ws + (13u << 20) + 65536);
  float* hstat = (float*)(ws + (14u << 20));
  unsigned* cnt = (unsigned*)(ws + (13u << 20) + 131072);
  k1_qkv<<<256, 512, 0, stream>>>(x, wqkv, pq, kvpart, cnt, wo, wg, wu, wd);
  k1b_reduce<<<77, 256, 0, stream>>>(kvpart, kvf, wo, wg, wu, wd, csp);
  k234<<<256, 512, 0, stream>>>(x, wo, pq, kvf, wg, wu, csp, wd, hfr, hstat, hh, cnt, (__bf16*)d_out);
}
```

```cpp
#include <hip/hip_runtime.h>
#include <hip/hip_bf16.h>
#include <stdint.h>

typedef __attribute__((ext_vector_type(8))) __bf16 bf16x8;
typedef __attribute__((ext_vector_type(4))) __bf16 bf16x4;
typedef __attribute__((ext_vector_type(4))) short s16x4;
typedef __attribute__((ext_vector_type(4))) float f32x4;
typedef __attribute__((ext_vector_type(2))) float f32x2;
typedef __attribute__((ext_vector_type(4))) unsigned u32x4;
typedef __attribute__((ext_vector_type(2))) unsigned u32x2;
typedef __attribute__((address_space(3))) s16x4* lds_s16x4_ptr;

#define DI __device__ __forceinline__
#define MFMA(a, b, c) __builtin_amdgcn_mfma_f32_16x16x32_bf16((a), (b), (c), 0, 0, 0)

constexpr int S_ = 2048, D_ = 512, F_ = 1376;
constexpr int KVREC = 272;

#define STAMP_DECL
#define STAMP(i)
#define STAMP_OUT(k, wsbase)
DI float bfr(float x) { return (float)(__bf16)x; }
DI float bf2f(__bf16 x) { return (float)x; }

DI void glds16(const void* gsrc, unsigned lds_dst) {
  unsigned keep;
  asm volatile(
      "s_mov_b32 %0, m0\n\ts_mov_b32 m0, %2\n\ts_nop 0\n\tglobal_load_lds_dwordx4 %1, off\n\ts_mov_b32 m0, %0"
      : "=&s"(keep)
      : "v"(gsrc), "s"(lds_dst)
      : "memory");
}
DI void glds16s(const void* ubase, unsigned voff, unsigned lds_dst) {
  unsigned keep;
  asm volatile(
      "s_mov_b32 %0, m0\n\ts_mov_b32 m0, %3\n\ts_nop 0\n\tglobal_load_lds_dwordx4 %1, %2\n\ts_mov_b32 m0, %0"
      : "=&s"(keep)
      : "v"(voff), "s"(ubase), "s"(lds_dst)
      : "memory");
}
DI void gload16(bf16x8& dst, const void* ubase, unsigned voff) {
  asm volatile("global_load_dwordx4 %0, %1, %2" : "=v"(dst) : "v"(voff), "s"(ubase) : "memory");
}
DI void gload8(bf16x4& dst, const void* ubase, unsigned voff) {
  asm volatile("global_load_dwordx2 %0, %1, %2" : "=v"(dst) : "v"(voff), "s"(ubase) : "memory");
}
DI void gload16_sc1(bf16x8& dst, const void* ubase, unsigned voff) {
  asm volatile("global_load_dwordx4 %0, %1, %2 sc1" : "=v"(dst) : "v"(voff), "s"(ubase) : "memory");
}
DI void glds16s_sc1(const void* ubase, unsigned voff, unsigned lds_dst) {
  unsigned keep;
  asm volatile(
      "s_mov_b32 %0, m0\n\ts_mov_b32 m0, %3\n\ts_nop 0\n\tglobal_load_lds_dwordx4 %1, %2 sc1\n\ts_mov_b32 m0, %0"
      : "=&s"(keep)
      : "v"(voff), "s"(ubase), "s"(lds_dst)
      : "memory");
}
DI void gstore16_sc1(void* ubase, unsigned voff, bf16x8 v) {
  asm volatile("global_store_dwordx4 %0, %1, %2 sc1\n\ts_nop 1" :: "v"(voff), "v"(v), "s"(ubase) : "memory");
}
DI void gstore8_sc1(void* ubase, unsigned voff, bf16x4 v) {
  asm volatile("global_store_dwordx2 %0, %1, %2 sc1\n\ts_nop 1" :: "v"(voff), "v"(v), "s"(ubase) : "memory");
}
DI void gstore8f_sc1(void* ubase, unsigned voff, f32x2 v) {
  asm volatile("global_store_dwordx2 %0, %1, %2 sc1\n\ts_nop 1" :: "v"(voff), "v"(v), "s"(ubase) : "memory");
}
#define TIE(x) asm volatile("" : "+v"(x))
#define WAIT_VM(n) do { __builtin_amdgcn_sched_barrier(0); asm volatile("s_waitcnt vmcnt(" #n ") lgkmcnt(0)" ::: "memory"); } while (0)
DI void wait_vm(int n) {
  switch (n) {
    case 0: WAIT_VM(0); break;   case 1: WAIT_VM(1); break;   case 2: WAIT_VM(2); break;
    case 3: WAIT_VM(3); break;   case 4: WAIT_VM(4); break;   case 5: WAIT_VM(5); break;
    case 6: WAIT_VM(6); break;   case 7: WAIT_VM(7); break;   case 8: WAIT_VM(8); break;
    case 9: WAIT_VM(9); break;   case 10: WAIT_VM(10); break; case 11: WAIT_VM(11); break;
    case 12: WAIT_VM(12); break; case 13: WAIT_VM(13); break; case 14: WAIT_VM(14); break;
    case 15: WAIT_VM(15); break; case 16: WAIT_VM(16); break; case 17: WAIT_VM(17); break;
    case 18: WAIT_VM(18); break; case 19: WAIT_VM(19); break; case 20: WAIT_VM(20); break;
    case 21: WAIT_VM(21); break; case 22: WAIT_VM(22); break; case 23: WAIT_VM(23); break;
    case 24: WAIT_VM(24); break; case 25: WAIT_VM(25); break; case 26: WAIT_VM(26); break;
    case 27: WAIT_VM(27); break; case 28: WAIT_VM(28); break; case 29: WAIT_VM(29); break;
    case 30: WAIT_VM(30); break; case 31: WAIT_VM(31); break; case 32: WAIT_VM(32); break;
    case 33: WAIT_VM(33); break; case 34: WAIT_VM(34); break; case 35: WAIT_VM(35); break;
    case 36: WAIT_VM(36); break; case 37: WAIT_VM(37); break; case 38: WAIT_VM(38); break;
    case 39: WAIT_VM(39); break; case 40: WAIT_VM(40); break; case 41: WAIT_VM(41); break;
    case 42: WAIT_VM(42); break; case 43: WAIT_VM(43); break; case 44: WAIT_VM(44); break;
    default: WAIT_VM(0); break;
  }
}
#define SB __builtin_amdgcn_sched_barrier(0)
DI void bar() { __builtin_amdgcn_s_barrier(); __builtin_amdgcn_sched_barrier(0); }

DI bf16x8 ldB(const char* smem_lane_base, int off0, int off1) {
  s16x4 lo = __builtin_amdgcn_ds_read_tr16_b64_v4i16((lds_s16x4_ptr)(smem_lane_base + off0));
  s16x4 hi = __builtin_amdgcn_ds_read_tr16_b64_v4i16((lds_s16x4_ptr)(smem_lane_base + off1));
  typedef __attribute__((ext_vector_type(8))) short s16x8;
  s16x8 v = __builtin_shufflevector(lo, hi, 0, 1, 2, 3, 4, 5, 6, 7);
  return __builtin_bit_cast(bf16x8, v);
}

DI float phi(float t) {
  float r = t > 0.f ? bfr(t + 1.0f) : bfr(__expf(t));
  return bfr(r + 1e-6f);
}

constexpr int K1_RING = 65536 + 7 * 12288;
constexpr int K1_LDS = K1_RING + 4096;
constexpr int k1_gcnt(int n) { return n > 15 ? 0 : (3 + ((n & 1) == 0 ? 2 : 0)); }
constexpr int k1_younger(int n) {
  return k1_gcnt(n + 1) + k1_gcnt(n + 2) + k1_gcnt(n + 3) + k1_gcnt(n + 4);
}
DI void acc8(f32x2 (&a)[4], bf16x8 v) {
  const u32x4 u = __builtin_bit_cast(u32x4, v);
#pragma unroll
  for (int j = 0; j < 4; ++j)
    a[j] += (f32x2){__builtin_bit_cast(float, u[j] << 16), __builtin_bit_cast(float, u[j] & 0xffff0000u)};
}
__global__ __launch_bounds__(768, 3) void k1_qkv(const __bf16* x, const __bf16* w, __bf16* pq, float* kvpart, unsigned* cnt,
                                                 const __bf16* wo, const __bf16* wg, const __bf16* wu, const __bf16* wd) {
  __shared__ __attribute__((aligned(16))) char smem[K1_LDS];
  STAMP_DECL; STAMP(0);
  const int tid = threadIdx.x, lane = tid & 63, wv = __builtin_amdgcn_readfirstlane(tid >> 6);
  const int g = lane >> 4, c = lane & 15;
  const int b = blockIdx.x, xcd = b & 7, idx = b >> 3;
  const int mt = (xcd >> 1) * 8 + (idx >> 2), hq = (xcd & 1) * 4 + (idx & 3);
  const unsigned lds0 = (unsigned)(size_t)smem;
  float* tcs = (float*)(smem + K1_RING);
  float* tst = tcs + 256;

  if (wv >= 8) {
    const int hl = wv - 8;
    const __bf16* wsrc = w + (size_t)(hl * 8) * 1536 + hq * 64;
    const __bf16* xsrc = x + (size_t)(mt * 64 + hl * 8) * D_;
    const int r8 = lane >> 3, pos = lane & 7;
    const unsigned xoff = r8 * (D_ * 2) + (pos ^ (((hl * 8 + r8) >> 1) & 7)) * 16;
    const unsigned woff = r8 * 3072 + ((pos >> 1) ^ (((r8 >> 1) & 1) | ((hl & 1) << 1))) * 32 + (pos & 1) * 16;
    auto issue = [&](int n) {
      const unsigned slot = lds0 + 65536 + (n % 7) * 12288;
#pragma unroll
      for (int p = 0; p < 3; ++p) glds16s(wsrc + (size_t)n * 32 * 1536 + p * 512, woff, slot + (p * 4 + hl) * 1024);
      if ((n & 1) == 0) {
        glds16s(xsrc + (n >> 1) * 64, xoff, lds0 + (n >> 1) * 8192 + hl * 1024);
        glds16s(xsrc + 32 * D_ + (n >> 1) * 64, xoff, lds0 + (n >> 1) * 8192 + (hl + 4) * 1024);
      }
    };
#pragma unroll
    for (int n = 0; n < 6; ++n) issue(n);
    f32x2 sa[4], sq[4];
#pragma unroll
    for (int j = 0; j < 4; ++j) { sa[j] = (f32x2){0.f, 0.f}; sq[j] = (f32x2){0.f, 0.f}; }
    const int rgp = lane >> 3, ch = lane & 7;
    const int coff = 65536 + (hl * 4 + (rgp >> 1)) * 1024 + (rgp & 1) * 512 + (ch & 1) * 16;
    bf16x8 pfx[6];
    wait_vm(k1_gcnt(5) + k1_younger(0));
    bar();
#pragma unroll
    for (int n = 0; n < 16; ++n) {
      if (n < 15) {
        wait_vm(k1_younger(n + 1) + (n > 10 ? 6 : 0));
        bar();
      }
      if (n + 6 < 16) issue(n + 6);
      if (n == 10) {
#pragma unroll
        for (int j = 0; j < 6; ++j) {
          int pc = b * 4 + hl + 1024 * j;
          pc = pc < 4640 ? pc : pc - 2048;
          const char* src = pc < 512 ? (const char*)wo + (size_t)pc * 1024
                          : pc < 1888 ? (const char*)wg + (size_t)(pc - 512) * 1024
                          : pc < 3264 ? (const char*)wu + (size_t)(pc - 1888) * 1024 : (const char*)wd + (size_t)(pc - 3264) * 1024;
          gload16(pfx[j], src, lane * 16);
        }
      }
    }
    bar();
    asm volatile("s_waitcnt lgkmcnt(0)" ::: "memory");
    bar();
    if (b == 0 && hl == 0 && lane < 32) cnt[lane * 32] = 0u;
    WAIT_VM(0);
#pragma unroll
    for (int j = 0; j < 6; ++j) TIE(pfx[j]);
    return;
  }

  const int hw = wv & 3, rh = wv >> 2;
  f32x4 acc[2][3];
#pragma unroll
  for (int i = 0; i < 2; ++i)
#pragma unroll
    for (int j = 0; j < 3; ++j) acc[i][j] = (f32x4){0.f, 0.f, 0.f, 0.f};
  f32x4 stt = {0.f, 0.f, 0.f, 0.f};
  f32x4 csum[3];
#pragma unroll
  for (int j = 0; j < 3; ++j) csum[j] = (f32x4){0.f, 0.f, 0.f, 0.f};
  bf16x8 ones;
#pragma unroll
  for (int j = 0; j < 8; ++j) ones[j] = (__bf16)1.0f;
  const int aoff = (rh * 32 + c) * 128 + ((g ^ (c >> 1)) * 16);
  const int boff = 65536 + g * 1024 + (c >> 2) * 128 + ((hw ^ (((c >> 3) & 1) | ((g & 1) << 1))) * 32) + (c & 3) * 8;
  bf16x8 fa[2][2], fb[2][3];
  auto rdA = [&](int n, int bu) {
    const char* sl = smem + (n >> 1) * 8192 + (aoff ^ ((n & 1) << 6));
    fa[bu][0] = *(const bf16x8*)(sl);
    fa[bu][1] = *(const bf16x8*)(sl + 2048);
  };
  auto rdB = [&](int n, int bu, int p) { fb[bu][p] = ldB(smem + boff + (n % 7) * 12288 + p * 4096, 0, 512); };
  STAMP(1);
  SB; bar();
  STAMP(8);
  rdA(0, 0); rdB(0, 0, 0); rdB(0, 0, 1); rdB(0, 0, 2);
#pragma unroll
  for (int n = 0; n < 16; ++n) {
    const int cu = n & 1, nx = cu ^ 1;
    if (n < 15) {
      SB; asm volatile("s_waitcnt lgkmcnt(0)" ::: "memory");
      bar();
    }
    acc[0][0] = MFMA(fb[cu][0], fa[cu][0], acc[0][0]); SB;
    if (n < 15) rdA(n + 1, nx);
    SB;
    acc[1][0] = MFMA(fb[cu][0], fa[cu][1], acc[1][0]); SB;
    if (n < 15) rdB(n + 1, nx, 0);
    SB;
    acc[0][1] = MFMA(fa[cu][0], fb[cu][1], acc[0][1]); SB;
    if (n < 15) rdB(n + 1, nx, 1);
    SB;
    acc[1][1] = MFMA(fa[cu][1], fb[cu][1], acc[1][1]); SB;
    if (n < 15) rdB(n + 1, nx, 2);
    SB;
    acc[0][2] = MFMA(fa[cu][0], fb[cu][2], acc[0][2]);
    acc[1][2] = MFMA(fa[cu][1], fb[cu][2], acc[1][2]);
    if (hw == 0) stt = MFMA(ones, fa[cu][0], stt);
    else if (hw == 1) stt = MFMA(fa[cu][0], fa[cu][0], stt);
    else if (hw == 2) stt = MFMA(ones, fa[cu][1], stt);
    else stt = MFMA(fa[cu][1], fa[cu][1], stt);
    if (cu == rh) {
      csum[0] = MFMA(ones, fb[cu][0], csum[0]);
      csum[1] = MFMA(ones, fb[cu][1], csum[1]);
      csum[2] = MFMA(ones, fb[cu][2], csum[2]);
    }
  }
  STAMP(6);
  float* tsum = tcs + 512;
  float* tsq = tsum + 64;
  float* tc2 = tsum + 128;
  {
    const int row = rh * 32 + (hw >> 1) * 16 + c;
    if ((hw & 1) == 0) { if (g == 0) tsum[row] = stt[0]; }
    else if (g == (c >> 2)) tsq[row] = (c & 2) ? ((c & 1) ? stt[3] : stt[2]) : ((c & 1) ? stt[1] : stt[0]);
    if (g == 0) {
#pragma unroll
      for (int p = 0; p < 3; ++p) tc2[rh * 192 + p * 64 + hw * 16 + c] = csum[p][0];
    }
  }
  SB; asm volatile("s_waitcnt lgkmcnt(0)" ::: "memory"); bar();
  if (tid < 64) {
    const float mean = tsum[tid] * (1.0f / 512.0f);
    const float var = fmaxf(tsq[tid] * (1.0f / 512.0f) - mean * mean, 0.f);
    const float sb = bfr(sqrtf(bfr(bfr(var) + 1e-5f)));
    *(f32x2*)(tst + tid * 2) = (f32x2){bfr(mean), 1.0f / sb};
  }
  SB; asm volatile("s_waitcnt lgkmcnt(0)" ::: "memory"); bar();
  const f32x4 csq = *(const f32x4*)(tc2 + hw * 16 + 4 * g) + *(const f32x4*)(tc2 + 192 + hw * 16 + 4 * g);
  const float csk = tc2[64 + hw * 16 + c] + tc2[192 + 64 + hw * 16 + c];
  const float csv = tc2[128 + hw * 16 + c] + tc2[192 + 128 + hw * 16 + c];
#pragma unroll
  for (int i = 0; i < 2; ++i) {
    const f32x2 st = *(const f32x2*)(tst + (rh * 32 + i * 16 + c) * 2);
    bf16x4 o;
#pragma unroll
    for (int r = 0; r < 4; ++r) o[r] = (__bf16)phi(bfr(st[1] * (acc[i][0][r] - st[0] * csq[r])));
    const int rt = mt * 4 + rh * 2 + i;
    *(bf16x4*)(pq + ((size_t)(rt * 16 + hq * 2 + (hw >> 1)) * 64 + lane) * 8 + (hw & 1) * 4) = o;
  }
  STAMP(7);
  {
    bf16x8 pk, vv;
#pragma unroll
    for (int i = 0; i < 2; ++i)
#pragma unroll
      for (int r = 0; r < 4; r += 2) {
        const f32x4 st = *(const f32x4*)(tst + (rh * 32 + i * 16 + 4 * g + r) * 2);
        pk[i * 4 + r] = (__bf16)phi(bfr(st[1] * (acc[i][1][r] - st[0] * csk)));
        pk[i * 4 + r + 1] = (__bf16)phi(bfr(st[3] * (acc[i][1][r + 1] - st[2] * csk)));
        vv[i * 4 + r] = (__bf16)(st[1] * (acc[i][2][r] - st[0] * csv));
        vv[i * 4 + r + 1] = (__bf16)(st[3] * (acc[i][2][r + 1] - st[2] * csv));
      }
    const f32x4 z4 = {0.f, 0.f, 0.f, 0.f};
    f32x4 kv = MFMA(pk, vv, z4);
    f32x4 zz = MFMA(pk, ones, z4);
    float* dst = kvpart + (size_t)((mt * 2 + rh) * 32 + hq * 4 + hw) * KVREC;
    *(f32x4*)(dst + lane * 4) = kv;
    if (c == 0) *(f32x4*)(dst + 256 + g * 4) = zz;
  }
  STAMP(2); STAMP_OUT(0, pq);
}

__global__ __launch_bounds__(256) void k1b_reduce(const float* kvpart, __bf16* kvf, const __bf16* wo, const __bf16* wg,
                                                  const __bf16* wu, const __bf16* wd, float* csp) {
  __shared__ __attribute__((aligned(16))) float red[32 * 64];
  STAMP_DECL; STAMP(0);
  const int tid = threadIdx.x, b = blockIdx.x, lane = tid & 63, wv = __builtin_amdgcn_readfirstlane(tid >> 6);
  if (b < 34) {
    const int q = b * 64 + lane;
    f32x4 v[16];
#pragma unroll
    for (int m = 0; m < 16; ++m) v[m] = *(const f32x4*)(kvpart + (size_t)(wv * 16 + m) * 32 * KVREC + q * 4);
    f32x4 s = v[0];
#pragma unroll
    for (int m = 1; m < 16; ++m) s += v[m];
    f32x4* r4 = (f32x4*)red;
    r4[tid] = s;
    __syncthreads();
    if (tid < 64) {
      f32x4 t = (r4[tid] + r4[64 + tid]) + (r4[128 + tid] + r4[192 + tid]);
      bf16x4 o;
#pragma unroll
      for (int j = 0; j < 4; ++j) o[j] = (__bf16)t[j];
      *(bf16x4*)(kvf + q * 4) = o;
    }
    STAMP(1); STAMP_OUT(1, (const char*)kvpart - (10u << 20));
    return;
  }
  if (b >= 77) {
    const int pb = b - 77;
    const u32x4* src = pb < 32 ? (const u32x4*)wo + (size_t)pb * 1024 : (const u32x4*)wd + (size_t)(pb - 32) * 1024;
    u32x4 v0 = src[tid], v1 = src[256 + tid], v2 = src[512 + tid], v3 = src[768 + tid];
    asm volatile("" :: "v"(v0), "v"(v1), "v"(v2), "v"(v3));
    return;
  }
  const int cb = b - 34;
  const int col0 = cb * 64 + (tid & 7) * 8;
  const __bf16* src = col0 < F_ ? (wg + col0) : (wu + (col0 - F_));
  const int kq = tid >> 3;
  bf16x8 v[16];
#pragma unroll
  for (int r = 0; r < 16; ++r) v[r] = *(const bf16x8*)(src + (size_t)(kq * 16 + r) * F_);
  float s[8];
#pragma unroll
  for (int j = 0; j < 8; ++j) s[j] = 0.f;
#pragma unroll
  for (int r = 0; r < 16; ++r)
#pragma unroll
    for (int j = 0; j < 8; ++j) s[j] += (float)v[r][j];
#pragma unroll
  for (int j = 0; j < 8; ++j) red[kq * 64 + (tid & 7) * 8 + j] = s[j];
  __syncthreads();
  if (tid < 64) {
    float t = 0.f;
#pragma unroll
    for (int q = 0; q < 32; ++q) t += red[q * 64 + tid];
    csp[cb * 64 + tid] = t;
  }
  STAMP(2); STAMP_OUT(1, (const char*)kvpart - (10u << 20));
}

constexpr int B3_RING = 6 * 12288;
constexpr int R_OFF = B3_RING;
constexpr int p3_wait(int n, int lo) {
  const int gs = lo ? 3 : 2;
  int c = 0;
  if (n + 1 <= 5) {
    c = (n + 1 == 1) ? 6 : 5 - (n + 1);
    c += gs * n;
  } else {
    for (int j = n - 4; j <= n - 1; ++j) c += (j <= 9) ? gs : 0;
  }
  return c;
}
DI void group_wait(unsigned* cnt) {
  unsigned spins = 0;
  while ((unsigned)__builtin_amdgcn_readfirstlane(__hip_atomic_load(cnt, __ATOMIC_RELAXED, __HIP_MEMORY_SCOPE_AGENT)) < 16u) {
    __builtin_amdgcn_s_sleep(1);
    if (++spins > (1u << 18)) break;
  }
}
__global__ __launch_bounds__(512, 2) void k234(const __bf16* x, const __bf16* wo, const __bf16* pq, const __bf16* kvf,
                                               const __bf16* wg, const __bf16* wu, const float* csp, const __bf16* wd,
                                               __bf16* hfr, float* hstat, __bf16* hh, unsigned* cnt, __bf16* out) {
  __shared__ __attribute__((aligned(16))) char smem[163840];
  STAMP_DECL; STAMP(0);
  const int tid = threadIdx.x, lane = tid & 63, wv = __builtin_amdgcn_readfirstlane(tid >> 6);
  const int g = lane >> 4, c = lane & 15;
  const int b = blockIdx.x, xcd = b & 7, idx = b >> 3;
  const int grp = xcd * 2 + (idx >> 4), mem = idx & 15;
  const unsigned lds0 = (unsigned)(size_t)smem;
  const int mt = grp * 2 + (mem >> 3), nt = mem & 7;
  const int ft0 = 2 * ((mem * 43) >> 4);
  const unsigned woff = (lane >> 3) * (D_ * 2) + ((((lane & 7) >> 1) ^ ((lane >> 4) & 3)) * 32) + (lane & 1) * 16;
  const int brow = (g & 1) * 4 + (c >> 2);
  const int bsw = (brow >> 1) & 3;
  const int btr = (g >> 1) * 1024 + brow * 128 + (c & 3) * 8;

  const unsigned boffl = (lane >> 2) * (F_ * 2) + ((((lane >> 1) & 1) ^ ((lane >> 4) & 1)) * 32) + (lane & 1) * 16;
  const __bf16* pB0 = (wv < 6 ? wg : wu) + (size_t)((wv & 1) * 16) * F_ + (ft0 + ((wv % 6) >> 1) * 2) * 16;
  const __bf16* pB1 = wu + (size_t)((wv & 1) * 16) * F_ + (ft0 + ((2 + (wv & 3)) >> 1) * 2) * 16;
  auto issueB3 = [&](int n, int e) {
    const unsigned slot = lds0 + (n % 6) * 12288;
    if (e == 0) glds16s(pB0 + (size_t)n * 32 * F_, boffl, slot + wv * 1024);
    if (e == 1) { if (wv < 4) glds16s(pB1 + (size_t)n * 32 * F_, boffl, slot + 8192 + wv * 1024); }
  };

  {
    const int rg = wv & 3, kh = wv >> 2;
    const int rt = mt * 4 + rg;
    bf16x8 pf[8];
#pragma unroll
    for (int h = 0; h < 8; ++h) gload16(pf[h], (const char*)pq + (size_t)(rt * 16 + kh * 8 + h) * 1024, lane * 16);
#pragma unroll
    for (int i = 0; i < 3; ++i) {
      const int ins = i * 8 + wv;
      if (ins < 17) glds16s((const char*)kvf + ins * 1024, lane * 16, lds0 + R_OFF + 65536 + ins * 1024);
    }
#pragma unroll
    for (int i = 0; i < 8; ++i) {
      const int kb = i * 8 + wv;
      glds16s(wo + (size_t)kb * 8 * D_ + nt * 64, woff, lds0 + R_OFF + kb * 1024);
    }
    bf16x4 xr[2];
#pragma unroll
    for (int p = 0; p < 2; ++p) gload8(xr[p], x + (size_t)(rt * 16) * D_ + nt * 64 + (kh * 2 + p) * 16, c * (D_ * 2) + g * 8);
    f32x4 acc[4];
#pragma unroll
    for (int j = 0; j < 4; ++j) acc[j] = (f32x4){0.f, 0.f, 0.f, 0.f};
    STAMP(1);
    WAIT_VM(10);
    bar();
    STAMP(2);
    const char* bl = smem + R_OFF + kh * 32768 + btr;
    const char* kvl = smem + R_OFF + 65536 + kh * 16 * (KVREC * 2);
    const f32x4 z4 = {0.f, 0.f, 0.f, 0.f};
    f32x4 n0[8], n1[8];
    float e0[8], e1[8];
#pragma unroll
    for (int h = 0; h < 8; ++h) {
      TIE(pf[h]);
      bf16x4 k0 = *(const bf16x4*)(kvl + (2 * h) * (KVREC * 2) + lane * 8);
      bf16x4 k1 = *(const bf16x4*)(kvl + (2 * h + 1) * (KVREC * 2) + lane * 8);
      bf16x4 z0 = *(const bf16x4*)(kvl + (2 * h) * (KVREC * 2) + 512 + g * 8);
      bf16x4 z1 = *(const bf16x4*)(kvl + (2 * h + 1) * (KVREC * 2) + 512 + g * 8);
      const bf16x4 zz = {(__bf16)0.f, (__bf16)0.f, (__bf16)0.f, (__bf16)0.f};
      bf16x8 a0 = __builtin_shufflevector(k0, zz, 0, 1, 2, 3, 4, 5, 6, 7);
      bf16x8 a1 = __builtin_shufflevector(zz, k1, 0, 1, 2, 3, 4, 5, 6, 7);
      bf16x8 d0 = __builtin_shufflevector(z0, zz, 0, 1, 2, 3, 4, 5, 6, 7);
      bf16x8 d1 = __builtin_shufflevector(zz, z1, 0, 1, 2, 3, 4, 5, 6, 7);
      n0[h] = MFMA(a0, pf[h], z4);
      n1[h] = MFMA(a1, pf[h], z4);
      e0[h] = MFMA(d0, pf[h], z4)[0];
      e1[h] = MFMA(d1, pf[h], z4)[0];
    }
    WAIT_VM(2);
    bar();
#pragma unroll
    for (int n = 0; n < 6; ++n) { issueB3(n, 0); issueB3(n, 1); }
#pragma unroll
    for (int h = 0; h < 8; ++h) {
      const float r0 = __builtin_amdgcn_rcpf(bfr(bfr(e0[h]) + 1e-6f));
      const float r1 = __builtin_amdgcn_rcpf(bfr(bfr(e1[h]) + 1e-6f));
      bf16x8 nb;
#pragma unroll
      for (int r = 0; r < 4; ++r) { nb[r] = (__bf16)n0[h][r]; nb[4 + r] = (__bf16)n1[h][r]; }
      const u32x4 nu = __builtin_bit_cast(u32x4, nb);
      bf16x8 of;
#pragma unroll
      for (int q = 0; q < 4; ++q) {
        const float rr = q < 2 ? r0 : r1;
        of[2 * q] = (__bf16)(__builtin_bit_cast(float, nu[q] << 16) * rr);
        of[2 * q + 1] = (__bf16)(__builtin_bit_cast(float, nu[q] & 0xffff0000u) * rr);
      }
#pragma unroll
      for (int ct = 0; ct < 4; ++ct) {
        bf16x8 wf = ldB(bl + ((ct ^ bsw) * 32), h * 4096, h * 4096 + 2048);
        acc[ct] = MFMA(wf, of, acc[ct]);
      }
    }
    STAMP(3);
    if (wv < 4) WAIT_VM(12); else WAIT_VM(6);
    bar();
    TIE(xr[0]); TIE(xr[1]);
    f32x4* red = (f32x4*)(smem + R_OFF);
    red[((rg * 2 + kh) * 2 + 0) * 64 + lane] = kh ? acc[0] : acc[2];
    red[((rg * 2 + kh) * 2 + 1) * 64 + lane] = kh ? acc[1] : acc[3];
    asm volatile("s_waitcnt lgkmcnt(0)" ::: "memory");
    bar();
    f32x4 f0 = (kh ? acc[2] : acc[0]), f1 = (kh ? acc[3] : acc[1]);
    {
      f32x4 o0 = red[((rg * 2 + (kh ^ 1)) * 2 + 0) * 64 + lane], o1 = red[((rg * 2 + (kh ^ 1)) * 2 + 1) * 64 + lane];
      f0 = kh ? (o0 + f0) : (f0 + o0);
      f1 = kh ? (o1 + f1) : (f1 + o1);
    }
    float hv[8];
    float s = 0.f;
#pragma unroll
    for (int r = 0; r < 4; ++r) {
      hv[r] = bfr((float)xr[0][r] + bfr(f0[r]));
      hv[4 + r] = bfr((float)xr[1][r] + bfr(f1[r]));
      s += hv[r] + hv[4 + r];
    }
    {
      bf16x8 o;
#pragma unroll
      for (int r = 0; r < 8; ++r) o[r] = (__bf16)hv[r];
      gstore16_sc1((char*)hfr + (size_t)(rt * 16 + nt * 2 + kh) * 1024, lane * 16, o);
    }
    s += __shfl_xor(s, 16, 64);
    s += __shfl_xor(s, 32, 64);
    const float mean = s * (1.0f / 32.0f);
    float m2 = 0.f;
#pragma unroll
    for (int r = 0; r < 8; ++r) { float d = hv[r] - mean; m2 += d * d; }
    m2 += __shfl_xor(m2, 16, 64);
    m2 += __shfl_xor(m2, 32, 64);
    if (g == 0) gstore8f_sc1((char*)hstat + (size_t)(rt * 16 + nt * 2 + kh) * 128, c * 8, (f32x2){s, m2});
    WAIT_VM(0);
    bar();
    if (tid == 0) __hip_atomic_fetch_add(cnt + grp * 32, 1u, __ATOMIC_RELAXED, __HIP_MEMORY_SCOPE_AGENT);
    STAMP(4);
  }
  {
    if (wv == 0) group_wait(cnt + grp * 32);
    WAIT_VM(0);
    bar();
    STAMP(5);
    const int mw = wv & 3, nw = wv >> 2;
    const int rt0 = grp * 8 + mw * 2;
    const char* pA = (const char*)hfr + (size_t)(grp * 8 + wv) * 16384;
    auto issueA = [&](int n) { glds16s_sc1(pA + n * 1024, lane * 16, lds0 + R_OFF + (n % 6) * 8192 + wv * 1024); };
    issueA(0); issueA(1);
    glds16s_sc1((const char*)hstat + (size_t)grp * 16384 + wv * 2048, lane * 16, lds0 + R_OFF + 49152 + wv * 2048);
    glds16s_sc1((const char*)hstat + (size_t)grp * 16384 + wv * 2048 + 1024, lane * 16, lds0 + R_OFF + 49152 + wv * 2048 + 1024);
    issueA(2); issueA(3); issueA(4); issueA(5);

    f32x4 acc[2][6];
#pragma unroll
    for (int i = 0; i < 2; ++i)
#pragma unroll
      for (int j = 0; j < 6; ++j) acc[i][j] = (f32x4){0.f, 0.f, 0.f, 0.f};
    bf16x8 fa[2][2], fb[2][6];
    const int bl0 = (4 * g + (c >> 2)) * 64 + (c & 3) * 8;
    auto rdA = [&](int n, int bu) {
      const char* sl = smem + R_OFF + (n % 6) * 8192 + lane * 16;
      fa[bu][0] = *(const bf16x8*)(sl + (mw * 2) * 1024);
      fa[bu][1] = *(const bf16x8*)(sl + (mw * 2 + 1) * 1024);
    };
    auto rdB = [&](int n, int bu, int ct) {
      const int ftl = nw * 3 + (ct >> 1);
      const char* bp = smem + (n % 6) * 12288 + bl0 + ((ct & 1) * 6 + (ftl >> 1) * 2) * 1024 + (((ftl & 1) ^ (g & 1)) * 32);
      fb[bu][ct] = ldB(bp, 0, 1024);
    };
    WAIT_VM(7);
    bar();
    STAMP(6);
    rdA(0, 0);
#pragma unroll
    for (int ct = 0; ct < 6; ++ct) rdB(0, 0, ct);
    float mb[2], rs[2];
    f32x4 cs[6];
#pragma unroll
    for (int n = 0; n < 16; ++n) {
      if (n < 15) {
        if (wv < 4) wait_vm(p3_wait(n, 1)); else wait_vm(p3_wait(n, 0));
        bar();
      }
      if (n == 2) {
#pragma unroll
        for (int i = 0; i < 2; ++i) {
          const char* sp = smem + R_OFF + 49152 + ((mw * 2 + i) * 16 + g * 4) * 128 + c * 8;
          const f32x2 t0 = *(const f32x2*)(sp), t1 = *(const f32x2*)(sp + 128), t2 = *(const f32x2*)(sp + 256), t3 = *(const f32x2*)(sp + 384);
          const f32x4 s0 = {t0[0], t0[1], t1[0], t1[1]}, s1 = {t2[0], t2[1], t3[0], t3[1]};
          float s = (s0[0] + s0[2]) + (s1[0] + s1[2]);
          s += __shfl_xor(s, 16, 64);
          s += __shfl_xor(s, 32, 64);
          const float mean = s * (1.0f / 512.0f);
          const float d0 = s0[0] * (1.0f / 32.0f) - mean, d1 = s0[2] * (1.0f / 32.0f) - mean;
          const float d2 = s1[0] * (1.0f / 32.0f) - mean, d3 = s1[2] * (1.0f / 32.0f) - mean;
          float m2 = (s0[1] + s0[3]) + (s1[1] + s1[3]) + 32.0f * ((d0 * d0 + d1 * d1) + (d2 * d2 + d3 * d3));
          m2 += __shfl_xor(m2, 16, 64);
          m2 += __shfl_xor(m2, 32, 64);
          mb[i] = bfr(mean);
          rs[i] = 1.0f / bfr(sqrtf(bfr(bfr(m2 * (1.0f / 512.0f)) + 1e-5f)));
        }
      }
      if (n == 10) {
#pragma unroll
        for (int ct = 0; ct < 6; ++ct)
          cs[ct] = *(const f32x4*)(csp + (ct & 1) * F_ + (ft0 + nw * 3 + (ct >> 1)) * 16 + g * 4);
      }
#pragma unroll
      for (int ct = 0; ct < 6; ++ct) {
        acc[0][ct] = MFMA(fb[n & 1][ct], fa[n & 1][0], acc[0][ct]);
        SB;
        if (n < 15) { if (ct == 0) rdA(n + 1, (n + 1) & 1); rdB(n + 1, (n + 1) & 1, ct); }
        SB;
        acc[1][ct] = MFMA(fb[n & 1][ct], fa[n & 1][1], acc[1][ct]);
        SB;
        if (n + 6 < 16) { if (ct == 1) issueA(n + 6); if (ct == 2) issueB3(n + 6, 0); if (ct == 3) issueB3(n + 6, 1); }
        SB;
      }
    }
    STAMP(7);
    bar();
#pragma unroll
    for (int i = 0; i < 2; ++i) {
#pragma unroll
      for (int j = 0; j < 3; ++j) {
        const int ftl = nw * 3 + j;
        bf16x4 o;
#pragma unroll
        for (int r = 0; r < 4; ++r) {
          float gg = bfr(rs[i] * (acc[i][2 * j][r] - mb[i] * cs[2 * j][r]));
          float uu = bfr(rs[i] * (acc[i][2 * j + 1][r] - mb[i] * cs[2 * j + 1][r]));
          o[r] = (__bf16)(gg * uu);
        }
        *(bf16x4*)(smem + ((mw * 2 + i) * 3 + (ftl >> 1)) * 1024 + lane * 16 + (ftl & 1) * 8) = o;
      }
    }
    asm volatile("s_waitcnt lgkmcnt(0)" ::: "memory");
    bar();
#pragma unroll
    for (int pr = 0; pr < 3; ++pr) {
      bf16x8 v = *(const bf16x8*)(smem + (wv * 3 + pr) * 1024 + lane * 16);
      gstore16_sc1((char*)hh + (size_t)((grp * 8 + wv) * 43 + (ft0 >> 1) + pr) * 1024, lane * 16, v);
    }
    WAIT_VM(0);
    bar();
    if (tid == 0) __hip_atomic_fetch_add(cnt + (16 + grp) * 32, 1u, __ATOMIC_RELAXED, __HIP_MEMORY_SCOPE_AGENT);
    STAMP(8);
  }
  {
    const int mi = wv & 1, kq = wv >> 1;
    const int rt0 = mt * 4 + mi * 2;
    const int u0 = kq * 11;
    const int fi = kq >> 1, fc = (kq & 1) * 2;
    const __bf16* wsrc = wd + (size_t)(u0 * 32 + mi * 16) * D_ + nt * 64;
    const unsigned ldsq = lds0 + kq * 40960 + mi * 2048;
    auto issueB = [&](int s) {
      const int ss = (s == 10 && kq == 3) ? 9 : s;
      glds16s(wsrc + (size_t)(ss * 32) * D_, woff, ldsq + (s % 10) * 4096);
      glds16s(wsrc + (size_t)(ss * 32 + 8) * D_, woff, ldsq + (s % 10) * 4096 + 1024);
    };
    if (wv == 0) { group_wait(cnt + (16 + grp) * 32); WAIT_VM(0); }
#pragma unroll
    for (int s = 0; s < 10; ++s) issueB(s);
    bar();
    STAMP(9);
    bf16x8 af[2][11];
    const char* asrc = (const char*)hh + (size_t)(rt0 * 43 + u0) * 1024;
#pragma unroll
    for (int s = 0; s < 11; ++s) {
      const int ss = (s == 10 && kq == 3) ? 9 : s;
      gload16_sc1(af[0][s], asrc + ss * 1024, lane * 16);
      gload16_sc1(af[1][s], asrc + (43 + ss) * 1024, lane * 16);
    }
    f32x4 acc[2][4];
#pragma unroll
    for (int i = 0; i < 2; ++i)
#pragma unroll
      for (int j = 0; j < 4; ++j) acc[i][j] = (f32x4){0.f, 0.f, 0.f, 0.f};
    const char* bl = smem + kq * 40960 + btr;
    auto comp = [&](int s) {
      TIE(af[0][s]); TIE(af[1][s]);
#pragma unroll
      for (int ct = 0; ct < 4; ++ct) {
        bf16x8 bf = ldB(bl + ((ct ^ bsw) * 32), (s % 10) * 4096, (s % 10) * 4096 + 2048);
        acc[0][ct] = MFMA(bf, af[0][s], acc[0][ct]);
        acc[1][ct] = MFMA(bf, af[1][s], acc[1][ct]);
      }
    };
    WAIT_VM(18);
    bar();
    STAMP(10);
    comp(0); comp(1);
    WAIT_VM(12);
    bar();
    issueB(10);
    bf16x8 hres;
    gload16_sc1(hres, (const char*)hfr + (size_t)((rt0 + fi) * 16 + nt * 2 + (kq & 1)) * 1024, lane * 16);
    comp(2); comp(3); comp(4);
    WAIT_VM(9);
    comp(5); comp(6); comp(7);
    WAIT_VM(1);
    bar();
    comp(8); comp(9);
    if (kq < 3) comp(10);
    STAMP(11);
    WAIT_VM(0);
    TIE(hres);
    bar();
    f32x4* red = (f32x4*)smem;
#pragma unroll
    for (int i = 0; i < 2; ++i)
#pragma unroll
      for (int ct = 0; ct < 4; ++ct)
        if (i != fi || (ct >> 1) != (kq & 1)) red[(((mi * 4 + kq) * 2 + i) * 4 + ct) * 64 + lane] = acc[i][ct];
    __syncthreads();
#pragma unroll
    for (int p = 0; p < 2; ++p) {
      const int ct = fc + p;
      const f32x4 own = fi ? (fc ? acc[1][2 + p] : acc[1][p]) : (fc ? acc[0][2 + p] : acc[0][p]);
      f32x4 v[4];
#pragma unroll
      for (int q = 0; q < 4; ++q) v[q] = q == kq ? own : red[(((mi * 4 + q) * 2 + fi) * 4 + ct) * 64 + lane];
      f32x4 s = (v[0] + v[1]) + (v[2] + v[3]);
      bf16x4 o;
#pragma unroll
      for (int r = 0; r < 4; ++r) o[r] = (__bf16)((float)hres[p * 4 + r] + bfr(s[r]));
      *(bf16x4*)(out + (size_t)((rt0 + fi) * 16 + c) * D_ + nt * 64 + ct * 16 + g * 4) = o;
    }
  }
  STAMP(12); STAMP_OUT(2, pq);
}

extern "C" void kernel_launch(void* const* d_in, const int* in_sizes, int n_in, void* d_out, int out_size,
                              void* d_ws, size_t ws_size, hipStream_t stream) {
  const __bf16* x = (const __bf16*)d_in[0];
  const __bf16* wqkv = (const __bf16*)d_in[1];
  const __bf16* wo = (const __bf16*)d_in[2];
  const __bf16* wg = (const __bf16*)d_in[4];
  const __bf16* wu = (const __bf16*)d_in[5];
  const __bf16* wd = (const __bf16*)d_in[6];
  char* ws = (char*)d_ws;
  __bf16* pq = (__bf16*)(ws);
  __bf16* hfr = (__bf16*)(ws + (2u << 20));
  __bf16* hh = (__bf16*)(ws + (4u << 20));
  float* kvpart = (float*)(ws + (10u << 20));
  __bf16* kvf = (__bf16*)(ws + (13u << 20));
  float* csp = (float*)(ws + (13u << 20) + 65536);
  float* hstat = (float*)(ws + (14u << 20));
  unsigned* cnt = (unsigned*)(ws + (13u << 20) + 131072);
  k1_qkv<<<256, 768, 0, stream>>>(x, wqkv, pq, kvpart, cnt, wo, wg, wu, wd);
  k1b_reduce<<<77, 256, 0, stream>>>(kvpart, kvf, wo, wg, wu, wd, csp);
  k234<<<256, 512, 0, stream>>>(x, wo, pq, kvf, wg, wu, csp, wd, hfr, hstat, hh, cnt, (__bf16*)d_out);
}
```
